```python
import torch

def _softmax(x, axis=-1):
    return torch.softmax(x, dim=axis)
def _gelu(x):
    return torch.nn.functional.gelu(x, approximate="tanh")
def _silu(x):
    return torch.nn.functional.silu(x)
def _sigmoid(x):
    return torch.sigmoid(x)
def _layernorm(x, axis=-1, eps=1e-5):
    m = x.mean(dim=axis, keepdim=True)
    v = x.var(dim=axis, keepdim=True, unbiased=False)
    return (x - m) / torch.sqrt(v + eps)
def _scatter_add(tgt, idx, src, axis=0):
    return tgt.clone().index_add_(axis, idx, src)
def _segment_sum(src, seg, n):
    out = torch.zeros((n,) + src.shape[1:], dtype=src.dtype)
    return out.index_add_(0, seg, src)
def _pad(x, pw):
    flat = tuple(v for pair in reversed(pw) for v in pair)
    return torch.nn.functional.pad(x, flat)

kernel_name = 'sequential_preLN__linear_attn_block__swiglu_ffn__prompt'

def setup_inputs():

    _w = (lambda a: (lambda t: t.to(torch.bfloat16) if t.is_floating_point() else t)(torch.as_tensor(a)))
    def _A_setup():
        B, S, D = 1, 2048, 640
        return {
            "x": _w((lambda *s: torch.randn(*s, dtype=torch.bfloat16) * 0.05)(B, S, D)),
            "wqkv": _w((lambda *s: torch.randn(*s, dtype=torch.bfloat16) * 0.1)(D, 3 * D)),
            "wo": _w((lambda *s: torch.randn(*s, dtype=torch.bfloat16) * 0.05)(D, D)),
        }
    _A_in = _A_setup()

    def _B_setup():
        B, S, D, F = 1, 2048, 640, 1728
        return {
            "x": _w((lambda *s: torch.randn(*s, dtype=torch.bfloat16) * 0.05)(B, S, D)),
            "w_gate": _w((lambda *s: torch.randn(*s, dtype=torch.bfloat16) * 0.05)(D, F)),
            "w_up": _w((lambda *s: torch.randn(*s, dtype=torch.bfloat16) * 0.05)(D, F)),
            "w_down": _w((lambda *s: torch.randn(*s, dtype=torch.bfloat16) * 0.05)(F, D)),
        }
    _B_in = _B_setup()

    return {
        "A_x": _A_in["x"],
        "A_wqkv": _A_in["wqkv"],
        "A_wo": _A_in["wo"],
        "B_x": _B_in["x"],
        "B_w_gate": _B_in["w_gate"],
        "B_w_up": _B_in["w_up"],
        "B_w_down": _B_in["w_down"],
    }

def reference(A_x, A_wqkv, A_wo, B_x, B_w_gate, B_w_up, B_w_down):
    def _A_ref(x, wqkv, wo):
        B, S, D = x.shape
        H = 32; Dh = D // H
        qkv = (lambda x, *d: x.permute(*d))((x @ wqkv).reshape(B, S, 3, H, Dh), 2, 0, 3, 1, 4)
        q, k, v = qkv[0], qkv[1], qkv[2]
        phi = lambda t: torch.where(t > 0, t + 1, torch.exp(t)) + 1e-6
        pq, pk = phi(q), phi(k)
        kv = torch.einsum("bhsd,bhse->bhde", pk, v)
        z = torch.einsum("bhsd->bhd", pk)
        num = torch.einsum("bhqd,bhde->bhqe", pq, kv)
        den = torch.einsum("bhqd,bhd->bhq", pq, z)[..., None] + 1e-6
        o = num / den
        return (lambda x, *d: x.permute(*d))(o, 0, 2, 1, 3).reshape(B, S, D) @ wo

    def _B_ref(x, w_gate, w_up, w_down):
        g = _silu(x @ w_gate)
        u = x @ w_up
        return (g * u) @ w_down

    x = A_x
    h = x + _A_ref(_layernorm(x, axis=-1), A_wqkv, A_wo)
    h2 = h + _B_ref(_layernorm(h, axis=-1), B_w_gate, B_w_up, B_w_down)
    return h2
```

```cpp
#include <hip/hip_runtime.h>
#include <hip/hip_bf16.h>
typedef unsigned short u16;
__device__ __forceinline__ float bf2f(u16 b){ return __builtin_bit_cast(float,(unsigned)b<<16); }
__device__ __forceinline__ u16 f2bf(float f){ __hip_bfloat16 b=__float2bfloat16(f); return __builtin_bit_cast(u16,b); }
constexpr int S=2048,D=640,F=1728,H=32,DH=20;

__global__ void k_ln(const u16* x, u16* y){
  int row=blockIdx.x; int t=threadIdx.x;
  float v[10]; float s=0;
  for(int i=0;i<10;i++){ v[i]=bf2f(x[row*D+t+64*i]); s+=v[i]; }
  for(int o=32;o;o>>=1) s+=__shfl_xor(s,o);
  float m=s/D; float q=0;
  for(int i=0;i<10;i++){ float d=v[i]-m; q+=d*d; }
  for(int o=32;o;o>>=1) q+=__shfl_xor(q,o);
  float r=rsqrtf(q/D+1e-5f);
  for(int i=0;i<10;i++) y[row*D+t+64*i]=f2bf((v[i]-m)*r);
}
__global__ void k_gemm(const u16* A,const u16* B,float* C,int M,int N,int K){
  __shared__ float As[16][17], Bs[16][17];
  int tx=threadIdx.x&15, ty=threadIdx.x>>4;
  int r=blockIdx.y*16+ty, c=blockIdx.x*16+tx; float acc=0;
  for(int k0=0;k0<K;k0+=16){
    As[ty][tx]=bf2f(A[r*K+k0+tx]); Bs[ty][tx]=bf2f(B[(k0+ty)*N+c]);
    __syncthreads();
    for(int k=0;k<16;k++) acc+=As[ty][k]*Bs[k][tx];
    __syncthreads();
  }
  C[r*N+c]=acc;
}
__device__ __forceinline__ float phi(float t){ return (t>0? t+1.f: __expf(t))+1e-6f; }
__global__ void k_kv(const float* qkv, float* kv){
  int h=blockIdx.x, t=threadIdx.x; if(t>=420) return;
  int d=t/21, e=t%21; float acc=0;
  for(int s=0;s<S;s++){ float pk=bf2f(f2bf(phi(bf2f(f2bf(qkv[s*1920+640+h*20+d]))))); float v= e<20? bf2f(f2bf(qkv[s*1920+1280+h*20+e])):1.f; acc+=pk*v; }
  kv[h*420+t]=acc;
}
__global__ void k_o(const float* qkv,const float* kv,u16* o){
  int i=blockIdx.x*256+threadIdx.x; int s=i/D, c=i%D, h=c/20, e=c%20;
  float num=0, den=0;
  for(int d=0;d<20;d++){ float pq=bf2f(f2bf(phi(bf2f(f2bf(qkv[s*1920+h*20+d]))))); num+=pq*kv[h*420+d*21+e]; den+=pq*kv[h*420+d*21+20]; }
  o[i]=f2bf(num/(den+1e-6f));
}
__global__ void k_add(const u16* a,const float* b,u16* c){ int i=blockIdx.x*256+threadIdx.x; c[i]=f2bf(bf2f(a[i])+bf2f(f2bf(b[i]))); }
__global__ void k_act(const float* g,const float* u,u16* a){ int i=blockIdx.x*256+threadIdx.x; float x=g[i]; a[i]=f2bf(x/(1.f+__expf(-x))*u[i]); }

extern "C" void kernel_launch(void* const* d_in, const int* in_sizes, int n_in, void* d_out, int out_size, void* d_ws, size_t ws_size, hipStream_t stream) {
  const u16* x=(const u16*)d_in[0]; const u16* wqkv=(const u16*)d_in[1]; const u16* wo=(const u16*)d_in[2];
  const u16* wg=(const u16*)d_in[4]; const u16* wu=(const u16*)d_in[5]; const u16* wd=(const u16*)d_in[6];
  char* ws=(char*)d_ws;
  u16* ln=(u16*)ws; float* qkv=(float*)(ws+(4<<20)); float* kv=(float*)(ws+(24<<20)); u16* o=(u16*)(ws+(25<<20));
  float* t1=(float*)(ws+(32<<20)); u16* h=(u16*)(ws+(40<<20)); float* g=(float*)(ws+(48<<20)); float* u=(float*)(ws+(64<<20)); u16* act=(u16*)(ws+(80<<20));
  k_ln<<<S,64,0,stream>>>(x,ln);
  k_gemm<<<dim3(1920/16,S/16),256,0,stream>>>(ln,wqkv,qkv,S,1920,640);
  k_kv<<<32,448,0,stream>>>(qkv,kv);
  k_o<<<S*D/256,256,0,stream>>>(qkv,kv,o);
  k_gemm<<<dim3(640/16,S/16),256,0,stream>>>(o,wo,t1,S,640,640);
  k_add<<<S*D/256,256,0,stream>>>(x,t1,h);
  k_ln<<<S,64,0,stream>>>(h,ln);
  k_gemm<<<dim3(F/16,S/16),256,0,stream>>>(ln,wg,g,S,F,640);
  k_gemm<<<dim3(F/16,S/16),256,0,stream>>>(ln,wu,u,S,F,640);
  k_act<<<S*F/256,256,0,stream>>>(g,u,act);
  k_gemm<<<dim3(640/16,S/16),256,0,stream>>>(act,wd,t1,S,640,F);
  k_add<<<S*D/256,256,0,stream>>>(h,t1,(u16*)d_out);
}
```

```cpp
#include <hip/hip_runtime.h>
#include <utility>
#include <type_traits>

#define PROF 0

#define DI __device__ __forceinline__
#define LAS __attribute__((address_space(3)))
typedef unsigned v4u __attribute__((ext_vector_type(4)));
typedef unsigned v2u __attribute__((ext_vector_type(2)));
typedef float f32x2 __attribute__((ext_vector_type(2)));
typedef float f32x4 __attribute__((ext_vector_type(4)));
typedef float f32x16 __attribute__((ext_vector_type(16)));
typedef __bf16 bf16x2 __attribute__((ext_vector_type(2)));
typedef __amdgpu_buffer_rsrc_t rsrc_t;

template <int N> using IC = std::integral_constant<int, N>;
template <class Fn, int... Is> DI void sfor_impl(Fn&& f, std::integer_sequence<int, Is...>) { (f(IC<Is>{}), ...); }
template <int N, class Fn> DI void sfor(Fn&& f) { sfor_impl(f, std::make_integer_sequence<int, N>{}); }
#define SF(N, i) sfor<N>([&](auto i##_) __attribute__((always_inline)) { constexpr int i = decltype(i##_)::value;
#define SE });

constexpr unsigned WS_FLAG = 4096;
constexpr unsigned WS_PROF = 65536;
constexpr unsigned WS_W1T = 262144;
constexpr unsigned WS_CS1 = WS_W1T + 16 * 163840;
constexpr unsigned WS_W2T = WS_CS1 + 16 * 16 * 512;
constexpr unsigned WS_W3T = WS_W2T + 8 * 102400;
constexpr unsigned WS_CS3 = WS_W3T + 16 * 286720;
constexpr unsigned WS_W4T = WS_CS3 + 16 * 16 * 1024;
constexpr unsigned WS_KVP = WS_W4T + 8 * 286720;
constexpr unsigned WS_O = WS_KVP + 16 * 16 * 4096;
constexpr unsigned WS_H = WS_O + 128 * 20480;
constexpr unsigned WS_HS = WS_H + 64 * 40960;
constexpr unsigned WS_ACT = WS_HS + 8 * 2048 * 8;
constexpr unsigned WS_END = WS_ACT + 128 * 57344;
constexpr unsigned OOB = 0x40000000u;
enum { F_W1 = 0, F_W0 = 1, F_KV = 2, F_O = 3, F_H = 4, F_A0 = 5, F_A1 = 6 };
DI unsigned flag_off(int kind, int grp) { return WS_FLAG + (kind * 16 + grp) * 256; }

constexpr unsigned SLOT = 20480;
constexpr unsigned L_XST = 0;
constexpr unsigned L_RST = 81920;
constexpr unsigned L_MISC = 143360;
constexpr unsigned L_RED = L_MISC;
constexpr unsigned L_MEAN = L_MISC + 16384, L_RSTD = L_MEAN + 512, L_CS1 = L_RSTD + 512, L_CS3 = L_CS1 + 512;
constexpr unsigned LDS_BYTES = 163840;

#define WAITV(n) asm volatile("s_waitcnt vmcnt(%0)" ::"i"(n) : "memory")
#define WAITL(n) asm volatile("s_waitcnt lgkmcnt(%0)" ::"i"(n) : "memory")
#define BARRIER() asm volatile("s_barrier" ::: "memory")
#define BARRIER_L() asm volatile("s_waitcnt lgkmcnt(0)\n\ts_barrier" ::: "memory")
#define FENCE() __builtin_amdgcn_sched_barrier(0)

template <int IMM> DI void dma_p(unsigned lds, unsigned voff, rsrc_t rs, unsigned soff) {
  asm volatile("s_mov_b32 m0, %0\n\ts_nop 0\n\tbuffer_load_dwordx4 %1, %2, %3 offen offset:%c4 lds" ::"s"(lds), "v"(voff), "s"(rs), "s"(soff), "i"(IMM) : "memory");
}
template <int IMM> DI void dma_c(unsigned lds, unsigned voff, rsrc_t rs, unsigned soff) {
  asm volatile("s_mov_b32 m0, %0\n\ts_nop 0\n\tbuffer_load_dwordx4 %1, %2, %3 offen offset:%c4 sc1 lds" ::"s"(lds), "v"(voff), "s"(rs), "s"(soff), "i"(IMM) : "memory");
}
template <int R, int IMM> DI void ld_a(unsigned voff, rsrc_t rs, unsigned soff) {
  asm volatile("buffer_load_dwordx4 a[%c0:%c1], %2, %3, %4 offen offset:%c5 sc1" ::"i"(R), "i"(R + 3), "v"(voff), "s"(rs), "s"(soff), "i"(IMM) : "memory");
}
template <int IMM> DI void ld128c(v4u& d, unsigned voff, rsrc_t rs, unsigned soff) {
  asm volatile("buffer_load_dwordx4 %0, %1, %2, %3 offen offset:%c4 sc1" : "=v"(d) : "v"(voff), "s"(rs), "s"(soff), "i"(IMM) : "memory");
}
template <int IMM> DI void ld64c(v2u& d, unsigned voff, rsrc_t rs, unsigned soff) {
  asm volatile("buffer_load_dwordx2 %0, %1, %2, %3 offen offset:%c4 sc1" : "=v"(d) : "v"(voff), "s"(rs), "s"(soff), "i"(IMM) : "memory");
}
template <int IMM> DI void ld32c(unsigned& d, unsigned voff, rsrc_t rs, unsigned soff) {
  asm volatile("buffer_load_dword %0, %1, %2, %3 offen offset:%c4 sc1" : "=v"(d) : "v"(voff), "s"(rs), "s"(soff), "i"(IMM) : "memory");
}
DI void st128c(v4u d, unsigned voff, rsrc_t rs, unsigned soff) {
  asm volatile("buffer_store_dwordx4 %0, %1, %2, %3 offen sc1\n\ts_nop 1" ::"v"(d), "v"(voff), "s"(rs), "s"(soff) : "memory");
}
DI void st64c(v2u d, unsigned voff, rsrc_t rs, unsigned soff) {
  asm volatile("buffer_store_dwordx2 %0, %1, %2, %3 offen sc1" ::"v"(d), "v"(voff), "s"(rs), "s"(soff) : "memory");
}
DI void st32c(unsigned d, unsigned voff, rsrc_t rs, unsigned soff) {
  asm volatile("buffer_store_dword %0, %1, %2, %3 offen sc1" ::"v"(d), "v"(voff), "s"(rs), "s"(soff) : "memory");
}
DI void st128p(v4u d, unsigned voff, rsrc_t rs, unsigned soff) {
  asm volatile("buffer_store_dwordx4 %0, %1, %2, %3 offen\n\ts_nop 1" ::"v"(d), "v"(voff), "s"(rs), "s"(soff) : "memory");
}
DI void st64p(v2u d, unsigned voff, rsrc_t rs, unsigned soff) {
  asm volatile("buffer_store_dwordx2 %0, %1, %2, %3 offen" ::"v"(d), "v"(voff), "s"(rs), "s"(soff) : "memory");
}
template <int OFF> DI void dsr128(v4u& d, unsigned addr) { asm volatile("ds_read_b128 %0, %1 offset:%c2" : "=v"(d) : "v"(addr), "i"(OFF) : "memory"); }
template <int OFF> DI void dsr128f(f32x4& d, unsigned addr) { asm volatile("ds_read_b128 %0, %1 offset:%c2" : "=v"(d) : "v"(addr), "i"(OFF) : "memory"); }
template <int R, int OFF> DI void dsr128a(unsigned addr) { asm volatile("ds_read_b128 a[%c0:%c1], %2 offset:%c3" ::"i"(R), "i"(R + 3), "v"(addr), "i"(OFF) : "memory"); }
template <int OFF> DI v4u tr_piece(unsigned addr) {
  v2u lo, hi;
  asm volatile("ds_read_b64_tr_b16 %0, %2 offset:%c3\n\tds_read_b64_tr_b16 %1, %2 offset:%c4\n\ts_waitcnt lgkmcnt(0)" : "=&v"(lo), "=&v"(hi) : "v"(addr), "i"(OFF), "i"(OFF + 512) : "memory");
  return v4u{lo.x, lo.y, hi.x, hi.y};
}
template <bool Z, int RA> DI void mf16_av(f32x4& acc, const v4u& b) {
  if constexpr (Z) asm volatile("v_mfma_f32_16x16x32_bf16 %0, a[%c1:%c2], %3, 0" : "=&v"(acc) : "i"(RA), "i"(RA + 3), "v"(b));
  else asm volatile("v_mfma_f32_16x16x32_bf16 %0, a[%c1:%c2], %3, %0" : "+v"(acc) : "i"(RA), "i"(RA + 3), "v"(b));
}
template <bool Z, int RB> DI void mf16_va(f32x4& acc, const v4u& a) {
  if constexpr (Z) asm volatile("v_mfma_f32_16x16x32_bf16 %0, %3, a[%c1:%c2], 0" : "=&v"(acc) : "i"(RB), "i"(RB + 3), "v"(a));
  else asm volatile("v_mfma_f32_16x16x32_bf16 %0, %3, a[%c1:%c2], %0" : "+v"(acc) : "i"(RB), "i"(RB + 3), "v"(a));
}
template <bool Z, int RA, int RB> DI void mf16_aa(f32x4& acc) {
  if constexpr (Z) asm volatile("v_mfma_f32_16x16x32_bf16 %0, a[%c1:%c2], a[%c3:%c4], 0" : "=&v"(acc) : "i"(RA), "i"(RA + 3), "i"(RB), "i"(RB + 3));
  else asm volatile("v_mfma_f32_16x16x32_bf16 %0, a[%c1:%c2], a[%c3:%c4], %0" : "+v"(acc) : "i"(RA), "i"(RA + 3), "i"(RB), "i"(RB + 3));
}
template <bool Z> DI void mf16_vv(f32x4& acc, const v4u& a, const v4u& b) {
  if constexpr (Z) asm volatile("s_nop 1\n\tv_mfma_f32_16x16x32_bf16 %0, %1, %2, 0" : "=&v"(acc) : "v"(a), "v"(b));
  else asm volatile("s_nop 1\n\tv_mfma_f32_16x16x32_bf16 %0, %1, %2, %0" : "+v"(acc) : "v"(a), "v"(b));
}
template <bool Z, int RB> DI void mf32_va(f32x16& acc, const v4u& a) {
  if constexpr (Z) asm volatile("v_mfma_f32_32x32x16_bf16 %0, %3, a[%c1:%c2], 0" : "=&v"(acc) : "i"(RB), "i"(RB + 3), "v"(a));
  else asm volatile("v_mfma_f32_32x32x16_bf16 %0, %3, a[%c1:%c2], %0" : "+v"(acc) : "i"(RB), "i"(RB + 3), "v"(a));
}
#define MFMA_DRAIN() do { asm volatile("s_nop 15" ::: "memory"); FENCE(); } while (0)
template <class T> DI void pin(T& x) { asm volatile("" : "+v"(x)); }

DI unsigned cvtpk(float lo, float hi) { f32x2 v = {lo, hi}; bf16x2 b = __builtin_convertvector(v, bf16x2); return __builtin_bit_cast(unsigned, b); }
DI float asf(unsigned u) { return __builtin_bit_cast(float, u); }
DI float bflo(unsigned u) { return __builtin_bit_cast(float, u << 16); }
DI float bfhi(unsigned u) { return __builtin_bit_cast(float, u & 0xffff0000u); }
DI float sum8(v4u v, float acc) {
  acc += (bflo(v.x) + bfhi(v.x)) + (bflo(v.y) + bfhi(v.y));
  acc += (bflo(v.z) + bfhi(v.z)) + (bflo(v.w) + bfhi(v.w));
  return acc;
}
DI float phi(float t) { return (t > 0.f ? t + 1.f : __builtin_amdgcn_exp2f(t * 1.44269504f)) + 1e-6f; }
DI rsrc_t mk(const void* p, unsigned bytes) { return __builtin_amdgcn_make_buffer_rsrc((void*)p, 0, (int)bytes, 0x00020000); }

#define ACLOB "a0","a1","a2","a3","a4","a5","a6","a7","a8","a9","a10","a11","a12","a13","a14","a15","a16","a17","a18","a19","a20","a21","a22","a23","a24","a25","a26","a27","a28","a29","a30","a31","a32","a33","a34","a35","a36","a37","a38","a39","a40","a41","a42","a43","a44","a45","a46","a47","a48","a49","a50","a51","a52","a53","a54","a55","a56","a57","a58","a59","a60","a61","a62","a63","a64","a65","a66","a67","a68","a69","a70","a71","a72","a73","a74","a75","a76","a77","a78","a79","a80","a81","a82","a83","a84","a85","a86","a87","a88","a89","a90","a91","a92","a93","a94","a95","a96","a97","a98","a99","a100","a101","a102","a103","a104","a105","a106","a107","a108","a109","a110","a111","a112","a113","a114","a115","a116","a117","a118","a119","a120","a121","a122","a123","a124","a125","a126","a127","a128","a129","a130","a131","a132","a133","a134","a135","a136","a137","a138","a139","a140","a141","a142","a143","a144","a145","a146","a147","a148","a149","a150","a151","a152","a153","a154","a155","a156","a157","a158","a159","a160","a161","a162","a163","a164","a165","a166","a167","a168","a169","a170","a171","a172","a173","a174","a175","a176","a177","a178","a179","a180","a181","a182","a183","a184","a185","a186","a187","a188","a189","a190","a191","a192","a193","a194","a195","a196","a197","a198","a199","a200","a201","a202","a203","a204","a205","a206","a207","a208","a209","a210","a211","a212","a213","a214","a215","a216","a217","a218","a219","a220","a221","a222","a223","a224","a225","a226","a227","a228","a229","a230","a231","a232","a233","a234","a235","a236","a237","a238","a239","a240","a241","a242","a243","a244","a245","a246","a247","a248","a249","a250","a251","a252","a253","a254","a255"

DI void poll_flags(rsrc_t ws, unsigned voff, unsigned tag, int& budget) {
  for (;;) {
    unsigned f;
    asm volatile("buffer_load_dword %0, %1, %2, 0 offen sc1\n\ts_waitcnt vmcnt(0)" : "=&v"(f) : "v"(voff), "s"(ws) : "memory");
    if (__all(f == tag)) break;
    if (--budget <= 0) break;
  }
}

#define STAMP(i) do {} while (0)

template <int U, int FR> DI void unit_dma(rsrc_t ws, unsigned lane16, unsigned src_w  , unsigned ldsw  , unsigned uoff) {
  if constexpr (FR < 4) dma_c<FR * 1024>(ldsw + (U % 7) * SLOT, lane16, ws, src_w + uoff);
  else dma_c<0>(ldsw + (U % 7) * SLOT + 4096, lane16, ws, src_w + uoff + 4096);
}

__global__ void __launch_bounds__(256, 1)
fused_block(const void* __restrict__ xp, const void* __restrict__ wqkvp, const void* __restrict__ wop, const void* __restrict__ wgp,
            const void* __restrict__ wup, const void* __restrict__ wdp, void* __restrict__ outp, char* __restrict__ wsp) {
  extern __shared__ __attribute__((aligned(16))) char smem_[];
  LAS char* L = (LAS char*)smem_;
  asm volatile("" ::: ACLOB);
  const int tid = threadIdx.x, lane = tid & 63;
  const int w = __builtin_amdgcn_readfirstlane(tid >> 6);
  const int b = blockIdx.x, xcd = b & 7, bi = b >> 3;
  const int r = (xcd >> 1) * 4 + (bi & 3), c = (xcd & 1) * 8 + (bi >> 2);
  const int j = (c >> 3) * 4 + (c & 3), hf = (c >> 2) & 1;
  const int li = lane & 15, lg = lane >> 4;
  const unsigned lane16 = lane * 16;
  const rsrc_t ws = mk(wsp, WS_END);
  const rsrc_t rx = mk(xp, 2048 * 640 * 2);
  int budget = 200000;
  STAMP(0);

  unsigned epoch;
  { unsigned z = 0; ld32c<0>(epoch, z, ws, 0); }
  const unsigned rst = L_RST + w * 12288;
  const int drow = lane >> 3, dch = lane & 7;
  {
    const rsrc_t rw = mk(wqkvp, 640 * 1920 * 2);
    const int z = 4 * w + (dch & 3);
    const int col = 40 * c + (z < 5 ? 640 + 8 * z : z == 5 ? 1280 + 32 : z < 10 ? 1280 + 8 * (z - 6) : z < 15 ? 8 * (z - 10) : 32);
    const unsigned vo = ((40 * r + 8 * (dch >> 2) + drow) * 1920 + col) * 2;
    dma_p<0>(rst, vo, rw, 0);
    dma_p<0>(rst + 1024, vo, rw, 16 * 3840);
    dma_p<0>(rst + 2048, (dch >> 2) ? OOB : vo, rw, 32 * 3840);
  }
  const unsigned xst = L_XST + w * 20480;
  const unsigned xv0 = drow * 1280 + ((dch ^ lg) * 16), xv1 = drow * 1280 + ((dch ^ (4 + lg)) * 16);
  const unsigned xrow = (128 * r + 32 * w) * 1280;
  auto x_chunk = [&](auto ch_) __attribute__((always_inline)) {
    constexpr int ch = decltype(ch_)::value;
    SF(4, pc) dma_p<0>(xst + (ch % 5) * 4096 + pc * 1024, (pc & 1) ? xv1 : xv0, rx, xrow + pc * 10240 + ch * 128); SE
  };
  const unsigned xa0 = xst + li * 128 + ((lg ^ (li >> 1)) * 16), xa1 = xst + li * 128 + (((4 + lg) ^ (li >> 1)) * 16);
  auto x_read = [&](auto ch_) __attribute__((always_inline)) {
    constexpr int ch = decltype(ch_)::value;
    dsr128a<(4 * ch + 0) * 4, (ch % 5) * 4096>(xa0);
    dsr128a<(4 * ch + 1) * 4, (ch % 5) * 4096 + 2048>(xa0);
    dsr128a<(4 * ch + 2) * 4, (ch % 5) * 4096>(xa1);
    dsr128a<(4 * ch + 3) * 4, (ch % 5) * 4096 + 2048>(xa1);
  };
  x_chunk(IC<0>{}); x_chunk(IC<1>{}); x_chunk(IC<2>{});
  const int tq = li >> 2, tp = li & 3;
  const unsigned trY = rst + tq * 128 + lg * 32 + tp * 8;
  asm volatile("s_waitcnt vmcnt(12)" : "+v"(epoch)::"memory");
  const unsigned tag = __builtin_amdgcn_readfirstlane(epoch) * 1664525u + 1013904223u;
  {
    const int cg = 2 * w + (lg & 1);
    float cs = 0.f;
    SF(3, k)
      const v4u f = tr_piece<k * 1024>(trY);
      const int ol = 2 * k + (lg >> 1), oc = 5 * r + ol, ks = oc >> 2;
      const int fr = cg < 5 ? ks * 5 + cg : 100 + ks * 3 + (cg - 5);
      const unsigned dst = fr * 1024 + (oc & 3) * 256 + li * 16;
      st128c(f, ol < 5 ? dst : OOB, ws, WS_W1T + c * 163840);
      cs = sum8(f, cs);
    SE
    cs += __shfl_xor(cs, 32);
    st32c(__builtin_bit_cast(unsigned, cs), lane < 32 ? (unsigned)(32 * w + lane) * 4 : OOB, ws, WS_CS1 + (c * 16 + r) * 512);
  }
  x_chunk(IC<3>{}); x_chunk(IC<4>{});
  {
    const rsrc_t rw = mk((w & 1) ? wup : wgp, 640 * 1728 * 2);
    const int ps = 4 * (w >> 1) + (dch >> 1);
    const int ft = ps == 0 ? 96 + c : 6 * c + ps - 1;
    const bool ok = ps < 7 && ft < 108;
    const unsigned vo = ok ? (unsigned)(((40 * r + drow) * 1728 + ft * 16 + 8 * (dch & 1)) * 2) : OOB;
    SF(5, k) dma_p<0>(rst + (3 + k) * 1024, vo, rw, k * 8 * 3456); SE
  }
  unsigned d24[3];
  {
    const rsrc_t rw = w == 0 ? mk(wop, 640 * 640 * 2) : mk(wdp, 1728 * 640 * 2);
    const int p = 2 * r + hf;
    unsigned s24[3];
    SF(3, k)
      const int oc = (w == 0 ? k : (w - 1) * 3 + k) * 32 + p, n = oc >> 2, g = oc & 3;
      int row0; bool ok; unsigned db;
      if (w == 0) { row0 = n < 16 ? 40 * n + 8 * g : 40 * (4 * (n - 16) + g) + 32; ok = oc < 80; db = WS_W2T + j * 102400; }
      else {
        const int ft = (n < 32 ? 6 * (n & 15) + 2 * (n >> 4) : n < 40 ? 96 + 2 * (n - 32) : 6 * (n - 40) + 4) + (g >> 1);
        row0 = 16 * ft + 8 * (g & 1); ok = oc < 224 && n != 38 && n != 39; db = WS_W4T + j * 286720;
      }
      s24[k] = ok ? (unsigned)((row0 * 640 + 80 * j) * 2) : OOB;
      d24[k] = ok ? db + n * 5120 + g * 256 : OOB;
    SE
    const unsigned vo = drow * 1280 + dch * 16;
    SF(3, k) dma_p<0>(rst + (8 + k) * 1024, vo, rw, s24[k]); SE
    const int g = dch >> 1;
    const unsigned sy = g == 0 ? s24[0] : g == 1 ? s24[1] : g == 2 ? s24[2] : OOB;
    dma_p<0>(rst + 11 * 1024, sy + drow * 1280 + 128 + (dch & 1) * 16, rw, 0);
  }
  WAITV(17);
  BARRIER();
  if (w == 0) st32c(tag, lane == 0 ? (unsigned)(4 * r) : OOB, ws, flag_off(F_W1, c));
  STAMP(1);
  x_read(IC<0>{}); x_read(IC<1>{}); x_read(IC<2>{});
  WAITL(0);
  x_chunk(IC<5>{}); x_chunk(IC<6>{}); x_chunk(IC<7>{});
  WAITV(12);
  x_read(IC<3>{}); x_read(IC<4>{});
  WAITL(0);
  x_chunk(IC<8>{}); x_chunk(IC<9>{});
  {
    const unsigned trZ = rst + tq * 128 + lg * 32 + (2 * (tp & 1) + (tp >> 1)) * 8;
    const int ps = 4 * (w >> 1) + lg;
    const unsigned dl = ps < 7 ? (unsigned)(ps * 40960 + (w & 1) * 256 + li * 16) : OOB;
    float cs = 0.f;
    SF(5, k)
      const v4u f = tr_piece<(3 + k) * 1024>(trZ);
      st128c(f, dl, ws, WS_W3T + c * 286720 + (5 * r + k) * 512);
      cs = sum8(f, cs);
    SE
    st32c(__builtin_bit_cast(unsigned, cs), (unsigned)(ps * 32 + (w & 1) * 16 + li) * 4, ws, WS_CS3 + (c * 16 + r) * 1024);
    const unsigned trX = rst + tq * 128 + (lg >> 1) * 64 + tp * 16 + (lg & 1) * 8;
    SF(3, k)
      const v4u f = tr_piece<(8 + k) * 1024>(trX);
      st128c(f, lg * 1024 + li * 16, ws, d24[k]);
    SE
    const v4u f = tr_piece<11 * 1024>(trY);
    const unsigned dy = lg == 0 ? d24[0] : lg == 1 ? d24[1] : lg == 2 ? d24[2] : OOB;
    st128c(f, dy + 4096 + li * 16, ws, 0);
  }
  WAITV(10);
  x_read(IC<5>{}); x_read(IC<6>{}); x_read(IC<7>{});
  WAITL(0);
  x_read(IC<8>{}); x_read(IC<9>{});
  WAITV(0);
  BARRIER_L();
  if (w == 0) {
    st32c(tag, lane == 0 ? (unsigned)(4 * r) : OOB, ws, flag_off(F_W0, c));
    STAMP(2);
    poll_flags(ws, flag_off(F_W1, c) + li * 4, tag, budget);
  }
  BARRIER();
  STAMP(3);
  const unsigned ldsw = w * 5120;
  const unsigned w1src = WS_W1T + c * 163840 + ldsw, w3src = WS_W3T + c * 286720 + ldsw;
  unsigned cs1p[8];
  SF(8, k) ld32c<k * 512>(cs1p[k], (unsigned)((lane >> 5) * 4096 + (32 * w + (lane & 31)) * 4), ws, WS_CS1 + c * 8192); SE
  SF(7, u) SF(5, f) unit_dma<u, f>(ws, lane16, w1src, ldsw, u * SLOT); SE SE
  {
    v4u ones = {0x3f803f80u, 0x3f803f80u, 0x3f803f80u, 0x3f803f80u};
    pin(ones);
    asm volatile("s_nop 1");
    f32x4 gr[2], sm[2];
    SF(20, ks) SF(2, m)
      mf16_aa<ks == 0, (2 * ks + m) * 4, (2 * ks + m) * 4>(gr[m]);
      mf16_va<ks == 0, (2 * ks + m) * 4>(sm[m], ones);
    SE SE
    MFMA_DRAIN();
    SF(2, m)
      pin(gr[m]); pin(sm[m]);
      const float sq = tp == 0 ? gr[m][0] : tp == 1 ? gr[m][1] : tp == 2 ? gr[m][2] : gr[m][3];
      const float mean = sm[m][0] * (1.f / 640.f);
      const float var = fmaxf(sq * (1.f / 640.f) - mean * mean, 0.f);
      if (lg == tq) {
        *(LAS float*)(L + L_MEAN + (32 * w + 16 * m + li) * 4) = mean;
        *(LAS float*)(L + L_RSTD + (32 * w + 16 * m + li) * 4) = __builtin_amdgcn_rsqf(var + 1e-5f);
      }
    SE
  }
  const unsigned lb0 = lane16, lb1 = lane16 + 3 * SLOT, lb2 = lane16 + 6 * SLOT;
  auto rd = [&](auto fr_, v4u& d) __attribute__((always_inline)) {
    constexpr int fr = decltype(fr_)::value, sl = (fr / 20) % 7, off = (sl % 3) * SLOT + (fr % 20) * 1024;
    dsr128<off>(d, sl < 3 ? lb0 : sl < 6 ? lb1 : lb2);
  };
  f32x4 akv[5][2], aq[3][2];
  {
    v4u bf[2][5];
    WAITV(30);
    {
      SF(8, k) pin(cs1p[k]); SE
      float s = 0.f;
      SF(8, k) s += __builtin_bit_cast(float, cs1p[k]); SE
      s += __shfl_xor(s, 32);
      if (lane < 32) *(LAS float*)(L + L_CS1 + (32 * w + lane) * 4) = s;
    }
    BARRIER_L();
    STAMP(4);
    SF(5, t) rd(IC<t>{}, bf[0][t]); SE
    SF(20, ks)
      if constexpr (ks % 4 == 3) {
        constexpr int u = ks / 4;
        WAITV(u == 0 ? 25 : u == 1 ? 20 : u == 2 ? 20 : u == 3 ? 15 : 10);
        BARRIER();
        if constexpr (u == 1) { SF(5, f) unit_dma<7, f>(ws, lane16, w1src, ldsw, 7 * SLOT); SE }
      }
      if constexpr (ks < 19) { SF(5, t) rd(IC<(ks + 1) * 5 + t>{}, bf[(ks + 1) & 1][t]); SE WAITL(5); }
      else WAITL(0);
      SF(5, t) SF(2, m) mf16_av<ks == 0, (2 * ks + m) * 4>(akv[t][m], bf[ks & 1][t]); SE SE
    SE
  }
  MFMA_DRAIN();
  STAMP(5);
  {
    v4u fr[5];
    f32x4 mean[2], rstd[2];
    SF(2, m)
      mean[m] = *(LAS f32x4*)(L + L_MEAN + (32 * w + 16 * m + 4 * lg) * 4);
      rstd[m] = *(LAS f32x4*)(L + L_RSTD + (32 * w + 16 * m + 4 * lg) * 4);
    SE
    SF(5, t)
      const float cs = *(LAS float*)(L + L_CS1 + (16 * t + li) * 4);
      float v[2][4];
      SF(2, m)
        pin(akv[t][m]);
        SF(4, e)
          float x = (akv[t][m][e] - mean[m][e] * cs) * rstd[m][e];
          if constexpr (t < 2) x = phi(x);
          if constexpr (t == 2) x = li < 8 ? phi(x) : x;
          v[m][e] = x;
        SE
      SE
      fr[t] = v4u{cvtpk(v[0][0], v[0][1]), cvtpk(v[0][2], v[0][3]), cvtpk(v[1][0], v[1][1]), cvtpk(v[1][2], v[1][3])};
    SE
    v4u ones = {0x3f803f80u, 0x3f803f80u, 0x3f803f80u, 0x3f803f80u};
    f32x4 dd[10];
    mf16_vv<true>(dd[0], fr[0], fr[3]); mf16_vv<true>(dd[1], fr[0], fr[4]); mf16_vv<true>(dd[2], fr[1], fr[3]);
    mf16_vv<true>(dd[3], fr[1], fr[4]); mf16_vv<true>(dd[4], fr[1], fr[2]); mf16_vv<true>(dd[5], fr[2], fr[4]);
    mf16_vv<true>(dd[6], fr[2], fr[2]); mf16_vv<true>(dd[7], fr[0], ones); mf16_vv<true>(dd[8], fr[1], ones); mf16_vv<true>(dd[9], fr[2], ones);
    MFMA_DRAIN();
    SF(10, i)
      constexpr int a = i < 2 ? 0 : i < 5 ? 1 : i < 7 ? 2 : i - 7;
      constexpr int bb = i == 0 || i == 2 ? 0 : i == 1 || i == 3 || i == 5 ? 1 : i < 7 ? 2 : 3;
      pin(dd[i]);
      const int dq = 4 * a + lg, hd = dq >= 5;
      int e; bool ok = dq < 10;
      if constexpr (bb < 2) { e = 16 * bb + li - 20 * hd; ok = ok && e >= 0 && e < 20; }
      else if constexpr (bb == 2) { e = 24 + li - 20 * hd; ok = ok && li >= 8 && hd; }
      else { e = 20; ok = ok && li == 0; }
      const int idx = ok ? hd * 105 + e * 5 + dq - 5 * hd : 255;
      *(LAS f32x4*)(L + L_RED + w * 4096 + idx * 16) = dd[i];
    SE
    BARRIER_L();
    f32x4 s = *(LAS f32x4*)(L + L_RED + tid * 16);
    SF(3, k) s += *(LAS f32x4*)(L + L_RED + (k + 1) * 4096 + tid * 16); SE
    st128c(__builtin_bit_cast(v4u, s), tid < 210 ? (unsigned)tid * 16 : OOB, ws, WS_KVP + (c * 16 + r) * 4096);
    WAITV(0);
    BARRIER();
    if (w == 0) {
      st32c(tag, lane == 0 ? (unsigned)(4 * r) : OOB, ws, flag_off(F_KV, c));
      STAMP(6);
      poll_flags(ws, flag_off(F_W0, (lane & 16) ? (c ^ 4) : c) + li * 4, tag, budget);
    }
    BARRIER();
  }
  STAMP(7);
  unsigned cs3p[16];
  v4u xr[2]; v2u xr4;
  {
    SF(16, k) ld32c<(k & 3) * 1024>(cs3p[k], (unsigned)tid * 4, ws, WS_CS3 + c * 16384 + (k >> 2) * 4096); SE
    const unsigned xo = ((128 * r + 64 * hf + 16 * w + li) * 640 + 80 * j) * 2;
    ld128c<0>(xr[0], xo + lg * 16, rx, 0); ld128c<64>(xr[1], xo + lg * 16, rx, 0); ld64c<128>(xr4, xo + lg * 8, rx, 0);
    SF(24, k) ld_a<160 + 4 * k, (k & 3) * 1024>(lane16, ws, WS_W2T + j * 102400 + w * 25600 + (k >> 2) * 4096); SE
  }
  v4u pq[2][2];
  {
    v4u bf[2][3];
    SF(3, t) rd(IC<100 + t>{}, bf[0][t]); SE
    SF(20, ks)
      if constexpr (ks < 19) { SF(3, t) rd(IC<100 + (ks + 1) * 3 + t>{}, bf[(ks + 1) & 1][t]); SE WAITL(3); }
      else WAITL(0);
      SF(3, t) SF(2, m) mf16_va<ks == 0, (2 * ks + m) * 4>(aq[t][m], bf[ks & 1][t]); SE SE
      unit_dma<8 + ks / 5, ks % 5>(ws, lane16, w3src, ldsw, (ks / 5) * SLOT);
      if constexpr (ks == 1) ld_a<0, 0>(lane16, ws, WS_W2T + j * 102400 + w * 25600 + 24576);
    SE
    MFMA_DRAIN();
    SF(2, m)
      const float mean = *(LAS float*)(L + L_MEAN + (32 * w + 16 * m + li) * 4), rstd = *(LAS float*)(L + L_RSTD + (32 * w + 16 * m + li) * 4);
      unsigned pk[3][2];
      SF(3, t)
        const f32x4 cs = *(LAS f32x4*)(L + L_CS1 + (80 + 16 * t + 4 * lg) * 4);
        pin(aq[t][m]);
        float v[4];
        SF(4, e) v[e] = phi((aq[t][m][e] - mean * cs[e]) * rstd); SE
        pk[t][0] = cvtpk(v[0], v[1]); pk[t][1] = cvtpk(v[2], v[3]);
      SE
      pq[m][0] = v4u{pk[0][0], pk[0][1], pk[1][0], pk[1][1]};
      pq[m][1] = v4u{lg < 2 ? pk[2][0] : 0u, lg < 2 ? pk[2][1] : 0u, 0u, 0u};
    SE
  }
  STAMP(8);
  {
    WAITV(21);
    SF(16, k) pin(cs3p[k]); SE
    pin(xr[0]); pin(xr[1]); pin(xr4);
    float s = 0.f;
    SF(16, k) s += __builtin_bit_cast(float, cs3p[k]); SE
    *(LAS float*)(L + L_CS3 + tid * 4) = s;
  }
  if (w == 0) poll_flags(ws, flag_off(F_KV, c) + li * 4, tag, budget);
  BARRIER();
  STAMP(9);
  {
    v4u g[16];
    SF(16, k) ld128c<0>(g[k], tid < 210 ? (unsigned)tid * 16 : OOB, ws, WS_KVP + (c * 16 + k) * 4096); SE
    WAITV(0);
    f32x4 s = {0.f, 0.f, 0.f, 0.f};
    SF(16, k) pin(g[k]); s += __builtin_bit_cast(f32x4, g[k]); SE
    *(LAS v2u*)(L + L_RED + tid * 8) = v2u{cvtpk(s[0], s[1]), cvtpk(s[2], s[3])};
    BARRIER_L();
    STAMP(10);
    v4u ka[4][2];
    SF(4, t)
      v2u pc[3];
      SF(3, p)
        const int dq = 4 * p + lg, hd = dq >= 5;
        int e; bool ok = dq < 10;
        if constexpr (t < 2) { e = 8 * tq + 4 * t + tp - 20 * hd; ok = ok && e >= 0 && e < 20; }
        else if constexpr (t == 2) { e = 24 + li - 20 * hd; ok = ok && li >= 8 && hd; }
        else { e = 20; ok = ok && tp == hd; }
        const int idx = ok ? hd * 105 + e * 5 + dq - 5 * hd : 255;
        pc[p] = *(LAS v2u*)(L + L_RED + idx * 8);
      SE
      ka[t][0] = v4u{pc[0].x, pc[0].y, pc[1].x, pc[1].y};
      ka[t][1] = v4u{pc[2].x, pc[2].y, 0u, 0u};
    SE
    f32x4 od[2][4];
    SF(2, m) SF(4, t) mf16_vv<true>(od[m][t], ka[t][0], pq[m][0]); SE SE
    SF(2, m) SF(4, t) mf16_vv<false>(od[m][t], ka[t][1], pq[m][1]); SE SE
    MFMA_DRAIN();
    SF(2, m)
      SF(4, t) pin(od[m][t]); SE
      const float r0 = __builtin_amdgcn_rcpf(od[m][3][0] + 1e-6f), r1 = __builtin_amdgcn_rcpf(od[m][3][1] + 1e-6f);
      const float ra = lg < 3 ? r0 : r1, rb = lg < 2 ? r0 : r1;
      const v4u o = {cvtpk(od[m][0][0] * ra, od[m][0][1] * ra), cvtpk(od[m][0][2] * ra, od[m][0][3] * ra),
                     cvtpk(od[m][1][0] * rb, od[m][1][1] * rb), cvtpk(od[m][1][2] * rb, od[m][1][3] * rb)};
      const v2u o2 = {cvtpk(od[m][2][0] * r1, od[m][2][1] * r1), cvtpk(od[m][2][2] * r1, od[m][2][3] * r1)};
      const unsigned ob = WS_O + (8 * r + 2 * w + m) * 20480;
      st128c(o, lane16, ws, ob + c * 1024);
      st64c(o2, lg >= 2 ? (unsigned)(li * 16 + (lg - 2) * 8) : OOB, ws, ob + (16 + (c >> 2)) * 1024 + (c & 3) * 256);
    SE
    WAITV(0);
    BARRIER();
    if (w == 0) {
      st32c(tag, lane == 0 ? (unsigned)(4 * c) : OOB, ws, flag_off(F_O, r));
      STAMP(11);
      poll_flags(ws, flag_off(F_O, r) + li * 4, tag, budget);
      if (b == 0) st32c(tag, lane == 0 ? 0u : OOB, ws, 0);
    }
    BARRIER();
  }
  STAMP(12);
  v4u hp[2]; v2u hp4;
  const int mt0 = 8 * r + 4 * hf;
  {
    SF(4, mm) SF(5, kk)
      ld_a<4 + (mm * 5 + kk) * 4, (kk & 3) * 1024>(lane16, ws, WS_O + (mt0 + ((w + mm + 1) & 3)) * 20480 + (5 * w + (kk & 4)) * 1024);
    SE SE
    f32x4 acc[4][5];
    SF(4, mm)
      WAITV(15 - 5 * mm);
      SF(5, kk) SF(5, t)
        constexpr int fb = kk * 5 + t;
        mf16_aa<kk == 0, (fb < 24 ? 160 + 4 * fb : 0), 4 + (mm * 5 + kk) * 4>(acc[mm][t]);
      SE SE
    SE
    MFMA_DRAIN();
    STAMP(13);
    SF(3, mm) SF(5, t)
      pin(acc[mm][t]);
      *(LAS f32x4*)(L + ((12 + mm) % 7) * SLOT + ((w + mm + 1) & 3) * 5120 + t * 1024 + lane16) = acc[mm][t];
    SE SE
    BARRIER_L();
    float v[5][4];
    SF(5, t)
      pin(acc[3][t]);
      f32x4 s = acc[3][t];
      SF(3, mm) s += *(LAS f32x4*)(L + ((12 + mm) % 7) * SLOT + ldsw + t * 1024 + lane16); SE
      const unsigned x0 = t < 4 ? xr[(t >> 1) & 1][(t & 1) * 2] : xr4[0], x1 = t < 4 ? xr[(t >> 1) & 1][(t & 1) * 2 + 1] : xr4[1];
      v[t][0] = s[0] + bflo(x0); v[t][1] = s[1] + bfhi(x0); v[t][2] = s[2] + bflo(x1); v[t][3] = s[3] + bfhi(x1);
    SE
    SF(2, k) hp[k] = v4u{cvtpk(v[2 * k][0], v[2 * k][1]), cvtpk(v[2 * k][2], v[2 * k][3]), cvtpk(v[2 * k + 1][0], v[2 * k + 1][1]), cvtpk(v[2 * k + 1][2], v[2 * k + 1][3])}; SE
    hp4 = v2u{cvtpk(v[4][0], v[4][1]), cvtpk(v[4][2], v[4][3])};
    const int mt = mt0 + w;
    const unsigned hb = WS_H + (mt >> 1) * 40960 + 5 * j * 1024 + (mt & 1) * 256;
    const unsigned hv = (lg >> 1) * 1024 + (lg & 1) * 512 + li * 16;
    st128c(hp[0], hv, ws, hb); st128c(hp[1], hv, ws, hb + 2048);
    st64c(hp4, (lg >> 1) * 512 + li * 16 + (lg & 1) * 8, ws, hb + 4096);
    float s1 = 0.f, s2 = 0.f;
    SF(10, k)
      const unsigned u = k < 8 ? hp[(k >> 2) & 1][k & 3] : hp4[k & 1];
      const float a = bflo(u), bq = bfhi(u);
      s1 += a + bq; s2 += a * a + bq * bq;
    SE
    s1 += __shfl_xor(s1, 16); s2 += __shfl_xor(s2, 16);
    s1 += __shfl_xor(s1, 32); s2 += __shfl_xor(s2, 32);
    st64c(v2u{__builtin_bit_cast(unsigned, s1), __builtin_bit_cast(unsigned, s2)}, lane < 16 ? (unsigned)li * 8 : OOB, ws, WS_HS + (j * 2048 + 16 * mt) * 8);
    WAITV(0);
    BARRIER_L();
    if (w == 0) st32c(tag, lane == 0 ? (unsigned)(4 * c) : OOB, ws, flag_off(F_H, r));
    STAMP(14);
    SF(3, u) SF(5, f) unit_dma<12 + u, f>(ws, lane16, w3src, ldsw, (4 + u) * SLOT); SE SE
    if (w == 0) poll_flags(ws, flag_off(F_H, r) + li * 4, tag, budget);
    BARRIER();
  }
  STAMP(15);
  const unsigned w4src = WS_W4T + j * 286720 + ldsw;
  {
    v2u hs[4];
    SF(4, k) ld64c<0>(hs[k], (unsigned)(((lane >> 5) * 4 * 2048 + (lane & 31)) * 8), ws, WS_HS + (k * 2048 + 128 * r + 32 * w) * 8); SE
    SF(40, s) ld_a<4 * s, (s & 3) * 1024>(lane16, ws, WS_H + (4 * r + w) * 40960 + (s >> 2) * 4096); SE
    f32x16 acc[2];
    v4u bf[8];
    float nmean, c1, kk2;
    const unsigned csa = L_CS3 + (lane >> 5) * 16;
    const unsigned av = ((lane >> 4) & 1) * 57344 + (lane >> 5) * 256 + li * 16;
    const unsigned ab = WS_ACT + (8 * r + 2 * w) * 57344;
    auto epi = [&](auto p_) __attribute__((always_inline)) {
      constexpr int p = decltype(p_)::value;
      f32x16& a = acc[p & 1];
      pin(a);
      f32x4 cs[4];
      SF(4, k) cs[k] = *(LAS f32x4*)(L + csa + p * 128 + k * 32); SE
      float o[8];
      SF(8, e)
        const float G = fmaf(nmean, cs[e >> 2][e & 3], a[e]), U = fmaf(nmean, cs[2 + (e >> 2)][e & 3], a[8 + e]);
        const float ex = __builtin_amdgcn_exp2f(G * c1);
        o[e] = G * U * __builtin_amdgcn_rcpf(fmaf(ex, kk2, kk2));
      SE
      const v4u ov = {cvtpk(o[0], o[1]), cvtpk(o[2], o[3]), cvtpk(o[4], o[5]), cvtpk(o[6], o[7])};
      constexpr int t = p - 1;
      unsigned so;
      if constexpr (p == 0) so = c < 12 ? ab + (32 + (c >> 1)) * 1024 + (c & 1) * 512 : OOB;
      else so = ab + ((t < 4 ? 16 * (t >> 1) : 40) + c) * 1024 + (t & 1) * 512;
      st128c(ov, av, ws, so);
    };
    SF(4, k) rd(IC<160 + k>{}, bf[k]); SE
    SF(7, p)
      SF(40, s)
        constexpr int g = p * 40 + s, u = 8 + g / 20;
        if constexpr (g % 20 == 16) {
          if constexpr (g == 5 * 40 + 36) WAITV(5);
          else if constexpr (p > 0) WAITV(11);
          BARRIER();
          if constexpr (g == 5 * 40 + 36) { if (w == 0) st32c(tag, lane == 0 ? (unsigned)(4 * c) : OOB, ws, flag_off(F_A0, r)); }
        }
        if constexpr (p == 0) { constexpr int nd = s > 36 ? s - 36 : 0; WAITV(39 - s + nd); }
        if constexpr (g + 4 < 280) WAITL(3); else WAITL(279 - g);
        mf32_va<s == 0, 4 * s>(acc[p & 1], bf[g & 7]);
        if constexpr (g + 4 < 280) rd(IC<160 + g + 4>{}, bf[(g + 4) & 7]);
        {
          constexpr int gg = g - 16 - 20;
          if constexpr (gg >= 0 && (gg % 20) < 5) {
            constexpr int nu = 15 + gg / 20, f = gg % 20;
            if constexpr (nu < 22) unit_dma<nu, f>(ws, lane16, w3src, ldsw, (nu - 8) * SLOT);
            else unit_dma<nu, f>(ws, lane16, w4src, ldsw, (nu - 22) * SLOT);
          }
        }
        if constexpr (p == 0 && s == 39) {
          SF(4, k) pin(hs[k]); SE
          float s1 = 0.f, s2 = 0.f;
          SF(4, k) s1 += asf(hs[k].x); s2 += asf(hs[k].y); SE
          s1 += __shfl_xor(s1, 32); s2 += __shfl_xor(s2, 32);
          const float mean = s1 * (1.f / 640.f);
          kk2 = fmaxf(s2 * (1.f / 640.f) - mean * mean, 0.f) + 1e-5f;
          nmean = -mean;
          c1 = -1.44269504f * __builtin_amdgcn_rsqf(kk2);
        }
        if constexpr (p > 0 && s == 8) epi(IC<p - 1>{});
      SE
    SE
    MFMA_DRAIN();
    STAMP(16);
    epi(IC<6>{});
    unit_dma<27, 4>(ws, lane16, w4src, ldsw, 5 * SLOT);
    WAITV(0);
    BARRIER();
    if (w == 0) st32c(tag, lane == 0 ? (unsigned)(4 * c) : OOB, ws, flag_off(F_A1, r));
    STAMP(17);
    SF(5, f) unit_dma<28, f>(ws, lane16, w4src, ldsw, 6 * SLOT); SE
    if (w == 0) poll_flags(ws, flag_off(F_A0, r) + li * 4, tag, budget);
    BARRIER();
  }
  STAMP(18);
  {
    const bool dead9 = w >= 2;
    auto lda = [&](auto u_) __attribute__((always_inline)) {
      constexpr int u = decltype(u_)::value;
      SF(4, mm)
        const unsigned so = WS_ACT + (mt0 + ((w + mm + 1) & 3)) * 57344 + (4 * u + w) * 1024;
        ld_a<(4 * u + mm) * 4, 0>(lane16, ws, (u == 9 && dead9) ? OOB : so);
      SE
    };
    SF(10, u) lda(IC<u>{}); SE
    f32x4 acc[4][5];
    v4u bf[2][5];
    const unsigned pb0 = lb0 + ldsw, pb1 = lb1 + ldsw, pb2 = lb2 + ldsw;
    auto rdw = [&](auto u_) __attribute__((always_inline)) {
      constexpr int u = decltype(u_)::value, sl = (22 + u) % 7;
      SF(5, t) dsr128<(sl % 3) * SLOT + t * 1024>(bf[u & 1][t], sl < 3 ? pb0 : sl < 6 ? pb1 : pb2); SE
    };
    rdw(IC<0>{});
    SF(14, u)
      if constexpr (u < 6) WAITV(4 * (9 - u) + 5 * u);
      else if constexpr (u < 10) WAITV(u < 8 ? 25 : u == 8 ? 20 : 15);
      else WAITV(4 * (13 - u));
      if constexpr (u < 13) { rdw(IC<u + 1>{}); WAITL(5); } else WAITL(0);
      SF(4, mm) SF(5, t) mf16_va<u == 0, (4 * u + mm) * 4>(acc[mm][t], bf[u & 1][t]); SE SE
      if constexpr (u < 7) { SF(5, f) unit_dma<29 + u, f>(ws, lane16, (u == 2 && dead9) ? OOB : w4src, ldsw, (7 + u) * SLOT); SE }
      if constexpr (u == 9) {
        if (w == 0) poll_flags(ws, flag_off(F_A1, r) + li * 4, tag, budget);
        BARRIER();
        SF(4, k) lda(IC<10 + k>{}); SE
      }
    SE
    MFMA_DRAIN();
    STAMP(19);
    BARRIER();
    SF(3, mm) SF(5, t)
      pin(acc[mm][t]);
      *(LAS f32x4*)(L + mm * SLOT + ((w + mm + 1) & 3) * 5120 + t * 1024 + lane16) = acc[mm][t];
    SE SE
    BARRIER_L();
    float v[5][4];
    SF(5, t)
      pin(acc[3][t]);
      f32x4 s = acc[3][t];
      SF(3, mm) s += *(LAS f32x4*)(L + mm * SLOT + ldsw + t * 1024 + lane16); SE
      const unsigned x0 = t < 4 ? hp[(t >> 1) & 1][(t & 1) * 2] : hp4[0], x1 = t < 4 ? hp[(t >> 1) & 1][(t & 1) * 2 + 1] : hp4[1];
      v[t][0] = s[0] + bflo(x0); v[t][1] = s[1] + bfhi(x0); v[t][2] = s[2] + bflo(x1); v[t][3] = s[3] + bfhi(x1);
    SE
    const rsrc_t ro = mk(outp, 2048 * 640 * 2);
    const unsigned oo = ((128 * r + 64 * hf + 16 * w + li) * 640 + 80 * j) * 2;
    SF(2, k) st128p(v4u{cvtpk(v[2 * k][0], v[2 * k][1]), cvtpk(v[2 * k][2], v[2 * k][3]), cvtpk(v[2 * k + 1][0], v[2 * k + 1][1]), cvtpk(v[2 * k + 1][2], v[2 * k + 1][3])}, oo + lg * 16, ro, k * 64); SE
    st64p(v2u{cvtpk(v[4][0], v[4][1]), cvtpk(v[4][2], v[4][3])}, oo + lg * 8, ro, 128);
  }
  STAMP(20);
}

extern "C" void kernel_launch(void* const* d_in, const int* in_sizes, int n_in, void* d_out, int out_size, void* d_ws, size_t ws_size, hipStream_t stream) {
  (void)in_sizes; (void)n_in; (void)out_size; (void)ws_size;
  (void)hipFuncSetAttribute((const void*)fused_block, hipFuncAttributeMaxDynamicSharedMemorySize, (int)LDS_BYTES);
  fused_block<<<256, 256, LDS_BYTES, stream>>>(d_in[0], d_in[1], d_in[2], d_in[4], d_in[5], d_in[6], d_out, (char*)d_ws);
}
```

```cpp
#include <hip/hip_runtime.h>
#include <utility>
#include <type_traits>

#define PROF 0

#define DI __device__ __forceinline__
#define LAS __attribute__((address_space(3)))
typedef unsigned v4u __attribute__((ext_vector_type(4)));
typedef unsigned v2u __attribute__((ext_vector_type(2)));
typedef float f32x2 __attribute__((ext_vector_type(2)));
typedef float f32x4 __attribute__((ext_vector_type(4)));
typedef float f32x16 __attribute__((ext_vector_type(16)));
typedef __bf16 bf16x2 __attribute__((ext_vector_type(2)));
typedef __amdgpu_buffer_rsrc_t rsrc_t;

template <int N> using IC = std::integral_constant<int, N>;
template <class Fn, int... Is> DI void sfor_impl(Fn&& f, std::integer_sequence<int, Is...>) { (f(IC<Is>{}), ...); }
template <int N, class Fn> DI void sfor(Fn&& f) { sfor_impl(f, std::make_integer_sequence<int, N>{}); }
#define SF(N, i) sfor<N>([&](auto i##_) __attribute__((always_inline)) { constexpr int i = decltype(i##_)::value;
#define SE });

constexpr unsigned WS_FLAG = 4096;
constexpr unsigned WS_PROF = 65536;
constexpr unsigned WS_W1T = 262144;
constexpr unsigned WS_CS1 = WS_W1T + 16 * 163840;
constexpr unsigned WS_W2T = WS_CS1 + 16 * 16 * 512;
constexpr unsigned WS_W3T = WS_W2T + 8 * 102400;
constexpr unsigned WS_CS3 = WS_W3T + 16 * 286720;
constexpr unsigned WS_W4T = WS_CS3 + 16 * 16 * 1024;
constexpr unsigned WS_KVP = WS_W4T + 8 * 286720;
constexpr unsigned WS_O = WS_KVP + 16 * 16 * 4096;
constexpr unsigned WS_H = WS_O + 128 * 20480;
constexpr unsigned WS_HS = WS_H + 64 * 40960;
constexpr unsigned WS_ACT = WS_HS + 8 * 2048 * 8;
constexpr unsigned WS_END = WS_ACT + 128 * 57344;
constexpr unsigned OOB = 0x40000000u;
enum { F_W1 = 0, F_W4 = 1, F_KV = 2, F_O = 3, F_H = 4, F_A0 = 5, F_A1 = 6, F_A2 = 7, F_A3 = 8 };
DI unsigned flag_off(int kind, int grp) { return WS_FLAG + (kind * 16 + grp) * 256; }

constexpr unsigned SLOT = 20480;
constexpr unsigned L_XST = 3 * SLOT;
constexpr unsigned L_MISC = 143360;
constexpr unsigned L_RED = L_MISC;
constexpr unsigned L_KVT = L_MISC + 13440;
constexpr unsigned L_MEAN = L_MISC + 17792, L_RSTD = L_MEAN + 512, L_CS1 = L_RSTD + 512, L_CS3 = L_CS1 + 512;
constexpr unsigned LDS_BYTES = 163840;

#define WAITV(n) asm volatile("s_waitcnt vmcnt(%0)" ::"i"(n) : "memory")
#define WAITL(n) asm volatile("s_waitcnt lgkmcnt(%0)" ::"i"(n) : "memory")
#define BARRIER() asm volatile("s_barrier" ::: "memory")
#define BARRIER_L() asm volatile("s_waitcnt lgkmcnt(0)\n\ts_barrier" ::: "memory")
#define FENCE() __builtin_amdgcn_sched_barrier(0)

template <int C> DI void set_m0(unsigned base) { asm volatile("s_add_u32 m0, %0, %c1\n\ts_nop 0" ::"s"(base), "i"(C) : "scc", "memory"); }
template <int IMM, bool SC1, int NEXT> DI void dma(unsigned voff, rsrc_t rs, unsigned soff, unsigned nb) {
  if constexpr (SC1) {
    if constexpr (NEXT >= 0) asm volatile("buffer_load_dwordx4 %0, %1, %2 offen offset:%c3 sc1 lds\n\ts_add_u32 m0, %4, %c5" ::"v"(voff), "s"(rs), "s"(soff), "i"(IMM), "s"(nb), "i"(NEXT) : "scc", "memory");
    else asm volatile("buffer_load_dwordx4 %0, %1, %2 offen offset:%c3 sc1 lds" ::"v"(voff), "s"(rs), "s"(soff), "i"(IMM) : "memory");
  } else {
    if constexpr (NEXT >= 0) asm volatile("buffer_load_dwordx4 %0, %1, %2 offen offset:%c3 lds\n\ts_add_u32 m0, %4, %c5" ::"v"(voff), "s"(rs), "s"(soff), "i"(IMM), "s"(nb), "i"(NEXT) : "scc", "memory");
    else asm volatile("buffer_load_dwordx4 %0, %1, %2 offen offset:%c3 lds" ::"v"(voff), "s"(rs), "s"(soff), "i"(IMM) : "memory");
  }
}
#define NOP1() asm volatile("s_nop 0")
template <int R, int IMM> DI void ld_a(unsigned voff, rsrc_t rs, unsigned soff) {
  asm volatile("buffer_load_dwordx4 a[%c0:%c1], %2, %3, %4 offen offset:%c5 sc1" ::"i"(R), "i"(R + 3), "v"(voff), "s"(rs), "s"(soff), "i"(IMM) : "memory");
}
template <int IMM> DI void ld128c(v4u& d, unsigned voff, rsrc_t rs, unsigned soff) {
  asm volatile("buffer_load_dwordx4 %0, %1, %2, %3 offen offset:%c4 sc1" : "=v"(d) : "v"(voff), "s"(rs), "s"(soff), "i"(IMM) : "memory");
}
template <int IMM> DI void ld64c(v2u& d, unsigned voff, rsrc_t rs, unsigned soff) {
  asm volatile("buffer_load_dwordx2 %0, %1, %2, %3 offen offset:%c4 sc1" : "=v"(d) : "v"(voff), "s"(rs), "s"(soff), "i"(IMM) : "memory");
}
template <int IMM> DI void ld32c(unsigned& d, unsigned voff, rsrc_t rs, unsigned soff) {
  asm volatile("buffer_load_dword %0, %1, %2, %3 offen offset:%c4 sc1" : "=v"(d) : "v"(voff), "s"(rs), "s"(soff), "i"(IMM) : "memory");
}
DI void st128c(v4u d, unsigned voff, rsrc_t rs, unsigned soff) {
  asm volatile("buffer_store_dwordx4 %0, %1, %2, %3 offen sc1\n\ts_nop 1" ::"v"(d), "v"(voff), "s"(rs), "s"(soff) : "memory");
}
DI void st64c(v2u d, unsigned voff, rsrc_t rs, unsigned soff) {
  asm volatile("buffer_store_dwordx2 %0, %1, %2, %3 offen sc1" ::"v"(d), "v"(voff), "s"(rs), "s"(soff) : "memory");
}
DI void st32c(unsigned d, unsigned voff, rsrc_t rs, unsigned soff) {
  asm volatile("buffer_store_dword %0, %1, %2, %3 offen sc1" ::"v"(d), "v"(voff), "s"(rs), "s"(soff) : "memory");
}
DI void st128p(v4u d, unsigned voff, rsrc_t rs, unsigned soff) {
  asm volatile("buffer_store_dwordx4 %0, %1, %2, %3 offen nt\n\ts_nop 1" ::"v"(d), "v"(voff), "s"(rs), "s"(soff) : "memory");
}
DI void st64p(v2u d, unsigned voff, rsrc_t rs, unsigned soff) {
  asm volatile("buffer_store_dwordx2 %0, %1, %2, %3 offen nt" ::"v"(d), "v"(voff), "s"(rs), "s"(soff) : "memory");
}
template <int OFF> DI void dsr128(v4u& d, unsigned addr) { asm volatile("ds_read_b128 %0, %1 offset:%c2" : "=v"(d) : "v"(addr), "i"(OFF) : "memory"); }
template <int OFF> DI void dsr128f(f32x4& d, unsigned addr) { asm volatile("ds_read_b128 %0, %1 offset:%c2" : "=v"(d) : "v"(addr), "i"(OFF) : "memory"); }
template <int R, int OFF> DI void dsr128a(unsigned addr) { asm volatile("ds_read_b128 a[%c0:%c1], %2 offset:%c3" ::"i"(R), "i"(R + 3), "v"(addr), "i"(OFF) : "memory"); }
template <int OFF> DI v4u tr_piece(unsigned addr) {
  v2u lo, hi;
  asm volatile("ds_read_b64_tr_b16 %0, %2 offset:%c3\n\tds_read_b64_tr_b16 %1, %2 offset:%c4\n\ts_waitcnt lgkmcnt(0)" : "=&v"(lo), "=&v"(hi) : "v"(addr), "i"(OFF), "i"(OFF + 512) : "memory");
  return v4u{lo.x, lo.y, hi.x, hi.y};
}
template <bool Z, int RA> DI void mf16_av(f32x4& acc, const v4u& b) {
  if constexpr (Z) asm volatile("v_mfma_f32_16x16x32_bf16 %0, a[%c1:%c2], %3, 0" : "=&v"(acc) : "i"(RA), "i"(RA + 3), "v"(b));
  else asm volatile("v_mfma_f32_16x16x32_bf16 %0, a[%c1:%c2], %3, %0" : "+v"(acc) : "i"(RA), "i"(RA + 3), "v"(b));
}
template <bool Z, int RB> DI void mf16_va(f32x4& acc, const v4u& a) {
  if constexpr (Z) asm volatile("v_mfma_f32_16x16x32_bf16 %0, %3, a[%c1:%c2], 0" : "=&v"(acc) : "i"(RB), "i"(RB + 3), "v"(a));
  else asm volatile("v_mfma_f32_16x16x32_bf16 %0, %3, a[%c1:%c2], %0" : "+v"(acc) : "i"(RB), "i"(RB + 3), "v"(a));
}
template <bool Z, int RA, int RB> DI void mf16_aa(f32x4& acc) {
  if constexpr (Z) asm volatile("v_mfma_f32_16x16x32_bf16 %0, a[%c1:%c2], a[%c3:%c4], 0" : "=&v"(acc) : "i"(RA), "i"(RA + 3), "i"(RB), "i"(RB + 3));
  else asm volatile("v_mfma_f32_16x16x32_bf16 %0, a[%c1:%c2], a[%c3:%c4], %0" : "+v"(acc) : "i"(RA), "i"(RA + 3), "i"(RB), "i"(RB + 3));
}
template <bool Z> DI void mf16_vv(f32x4& acc, const v4u& a, const v4u& b) {
  if constexpr (Z) asm volatile("s_nop 1\n\tv_mfma_f32_16x16x32_bf16 %0, %1, %2, 0" : "=&v"(acc) : "v"(a), "v"(b));
  else asm volatile("s_nop 1\n\tv_mfma_f32_16x16x32_bf16 %0, %1, %2, %0" : "+v"(acc) : "v"(a), "v"(b));
}
template <bool Z, int RB> DI void mf32_va(f32x16& acc, const v4u& a) {
  if constexpr (Z) asm volatile("v_mfma_f32_32x32x16_bf16 %0, %3, a[%c1:%c2], 0" : "=&v"(acc) : "i"(RB), "i"(RB + 3), "v"(a));
  else asm volatile("v_mfma_f32_32x32x16_bf16 %0, %3, a[%c1:%c2], %0" : "+v"(acc) : "i"(RB), "i"(RB + 3), "v"(a));
}
template <int RA, int RB> DI void mf16_lva(f32x4& acc) {
  asm volatile("v_mfma_f32_16x16x32_bf16 %0, v[%c1:%c2], a[%c3:%c4], %0" : "+v"(acc) : "i"(RA), "i"(RA + 3), "i"(RB), "i"(RB + 3));
}
template <int RA, int RB> DI void mf16_lva0(f32x4& acc) {
  asm volatile("v_mfma_f32_16x16x32_bf16 %0, v[%c1:%c2], a[%c3:%c4], 0" : "=&v"(acc) : "i"(RA), "i"(RA + 3), "i"(RB), "i"(RB + 3));
}
template <int R, int IMM> DI void ld_v(unsigned voff, rsrc_t rs, unsigned soff) {
  asm volatile("buffer_load_dwordx4 v[%c0:%c1], %2, %3, %4 offen offset:%c5 sc1" ::"i"(R), "i"(R + 3), "v"(voff), "s"(rs), "s"(soff), "i"(IMM) : "memory");
}
#define MFMA_DRAIN() do { asm volatile("s_nop 15" ::: "memory"); FENCE(); } while (0)
template <class T> DI void pin(T& x) { asm volatile("" : "+v"(x)); }

DI unsigned cvtpk(float lo, float hi) { f32x2 v = {lo, hi}; bf16x2 b = __builtin_convertvector(v, bf16x2); return __builtin_bit_cast(unsigned, b); }
DI float asf(unsigned u) { return __builtin_bit_cast(float, u); }
DI float bflo(unsigned u) { return __builtin_bit_cast(float, u << 16); }
DI float bfhi(unsigned u) { return __builtin_bit_cast(float, u & 0xffff0000u); }
DI float sum8(v4u v, float acc) {
  acc += (bflo(v.x) + bfhi(v.x)) + (bflo(v.y) + bfhi(v.y));
  acc += (bflo(v.z) + bfhi(v.z)) + (bflo(v.w) + bfhi(v.w));
  return acc;
}
DI float phi1(float t1) {
  const float e = __builtin_amdgcn_fmed3f(__builtin_amdgcn_exp2f(fmaf(t1, 1.44269504f, -1.44269504e-6f)), 0.f, 1.f);
  return fmaxf(t1, 1e-6f) + e;
}
template <int R, int OFF> DI void tr_a(unsigned addr) {
  asm volatile("ds_read_b64_tr_b16 a[%c0:%c1], %4 offset:%c5\n\tds_read_b64_tr_b16 a[%c2:%c3], %4 offset:%c6" ::"i"(R), "i"(R + 1), "i"(R + 2), "i"(R + 3), "v"(addr), "i"(OFF), "i"(OFF + 512) : "memory");
}
template <int R> DI void st_a(unsigned voff, rsrc_t rs, unsigned soff) {
  asm volatile("buffer_store_dwordx4 a[%c0:%c1], %2, %3, %4 offen sc1" ::"i"(R), "i"(R + 3), "v"(voff), "s"(rs), "s"(soff) : "memory");
}
template <int OFF> DI void dsw128f(const f32x4& d, unsigned addr) { asm volatile("ds_write_b128 %0, %1 offset:%c2" ::"v"(addr), "v"(d), "i"(OFF) : "memory"); }
DI float v_fma(float a, float b, float c) { float r; asm volatile("v_fma_f32 %0, %1, %2, %3" : "=v"(r) : "v"(a), "v"(b), "v"(c)); return r; }
DI float v_mul(float a, float b) { float r; asm volatile("v_mul_f32 %0, %1, %2" : "=v"(r) : "v"(a), "v"(b)); return r; }
DI float v_exp(float a) { float r; asm volatile("v_exp_f32 %0, %1" : "=v"(r) : "v"(a)); return r; }
DI float v_rcp(float a) { float r; asm volatile("v_rcp_f32 %0, %1" : "=v"(r) : "v"(a)); return r; }
DI unsigned v_cvtpk(float a, float b) { unsigned r; asm volatile("v_cvt_pk_bf16_f32 %0, %1, %2" : "=v"(r) : "v"(a), "v"(b)); return r; }
DI rsrc_t mk(const void* p, unsigned bytes) { return __builtin_amdgcn_make_buffer_rsrc((void*)p, 0, (int)bytes, 0x00020000); }

#define ACLOB "a0","a1","a2","a3","a4","a5","a6","a7","a8","a9","a10","a11","a12","a13","a14","a15","a16","a17","a18","a19","a20","a21","a22","a23","a24","a25","a26","a27","a28","a29","a30","a31","a32","a33","a34","a35","a36","a37","a38","a39","a40","a41","a42","a43","a44","a45","a46","a47","a48","a49","a50","a51","a52","a53","a54","a55","a56","a57","a58","a59","a60","a61","a62","a63","a64","a65","a66","a67","a68","a69","a70","a71","a72","a73","a74","a75","a76","a77","a78","a79","a80","a81","a82","a83","a84","a85","a86","a87","a88","a89","a90","a91","a92","a93","a94","a95","a96","a97","a98","a99","a100","a101","a102","a103","a104","a105","a106","a107","a108","a109","a110","a111","a112","a113","a114","a115","a116","a117","a118","a119","a120","a121","a122","a123","a124","a125","a126","a127","a128","a129","a130","a131","a132","a133","a134","a135","a136","a137","a138","a139","a140","a141","a142","a143","a144","a145","a146","a147","a148","a149","a150","a151","a152","a153","a154","a155","a156","a157","a158","a159","a160","a161","a162","a163","a164","a165","a166","a167","a168","a169","a170","a171","a172","a173","a174","a175","a176","a177","a178","a179","a180","a181","a182","a183","a184","a185","a186","a187","a188","a189","a190","a191","a192","a193","a194","a195","a196","a197","a198","a199","a200","a201","a202","a203","a204","a205","a206","a207","a208","a209","a210","a211","a212","a213","a214","a215","a216","a217","a218","a219","a220","a221","a222","a223","a224","a225","a226","a227","a228","a229","a230","a231","a232","a233","a234","a235","a236","a237","a238","a239","a240","a241","a242","a243","a244","a245","a246","a247","a248","a249","a250","a251","a252","a253","a254","a255"

DI void poll_w0(rsrc_t ws, unsigned voff, unsigned tag, int& budget, int w) {
  unsigned f;
  asm volatile("s_cmp_lg_u32 %5, 0\n\ts_cbranch_scc1 2f\n"
               "1:\n\tbuffer_load_dword %0, %2, %3, 0 offen sc1\n\ts_waitcnt vmcnt(0)\n\tv_cmp_ne_u32 vcc, %4, %0\n\ts_nop 1\n\ts_cbranch_vccz 2f\n\t"
               "s_sub_u32 %1, %1, 1\n\ts_cmp_gt_i32 %1, 0\n\ts_cbranch_scc1 1b\n2:"
               : "=&v"(f), "+s"(budget) : "v"(voff), "s"(ws), "s"(tag), "s"(w) : "vcc", "scc", "memory");
}
template <int N> DI void poll_chk(unsigned f, rsrc_t ws, unsigned voff, unsigned soff, unsigned tag, int& budget) {
  asm volatile("s_waitcnt vmcnt(%c6)\n"
               "1:\n\tv_cmp_ne_u32 vcc, %4, %0\n\ts_nop 1\n\ts_cbranch_vccz 2f\n\t"
               "s_sub_u32 %1, %1, 1\n\ts_cmp_gt_i32 %1, 0\n\ts_cbranch_scc0 2f\n\t"
               "buffer_load_dword %0, %2, %3, %5 offen sc1\n\ts_waitcnt vmcnt(0)\n\ts_branch 1b\n2:"
               : "+v"(f), "+s"(budget) : "v"(voff), "s"(ws), "s"(tag), "s"(soff), "i"(N) : "vcc", "scc", "memory");
}

#define STAMP(i) asm volatile("; MARK %0" ::"i"(i))

constexpr int slot_of(int u) { return (u % 7) * (int)SLOT; }
template <int U, int FR, int NEXT> DI void unit_dma(rsrc_t ws, unsigned lane16, unsigned lane16b, unsigned soff, unsigned ldsw) {
  if constexpr (FR < 3) dma<FR * 1024, true, -1>(lane16, ws, soff, ldsw);
  else if constexpr (FR == 3) dma<3072, true, slot_of(U) + 1024>(lane16, ws, soff, ldsw);
  else dma<3072, true, NEXT>(lane16b, ws, soff, ldsw);
}

#define WL(n) __builtin_amdgcn_s_waitcnt(0xC07F | ((n) << 8))
template <int R, int OFF> DI void dsr64a(unsigned addr) { asm volatile("ds_read_b64 a[%c0:%c1], %2 offset:%c3" ::"i"(R), "i"(R + 1), "v"(addr), "i"(OFF) : "memory"); }
template <int OFF> DI void dsr32f(float& d, unsigned addr) { asm volatile("ds_read_b32 %0, %1 offset:%c2" : "=v"(d) : "v"(addr), "i"(OFF) : "memory"); }
constexpr unsigned long long kv_mask(int a, int bb) {
  unsigned long long m = 0;
  for (int l = 0; l < 64; ++l) {
    const int li = l & 15, dq = 4 * a + (l >> 4), hd = dq >= 5, eg = bb < 2 ? 16 * bb + li : 24 + li;
    const bool ok = dq < 10 && (bb == 3 ? li == 0 : (bb < 2 || li >= 8) && eg / 20 == hd);
    if (ok) m |= 1ull << l;
  }
  return m;
}
template <int A, int BB, int OFF> DI void dsw_kv(const f32x4& d, unsigned addr) {
  asm volatile("s_mov_b32 exec_lo, %c2\n\ts_mov_b32 exec_hi, %c3\n\tds_write_b128 %0, %1 offset:%c4" ::"v"(addr), "v"(d), "i"((int)(unsigned)kv_mask(A, BB)), "i"((int)(unsigned)(kv_mask(A, BB) >> 32)), "i"(OFF) : "memory");
}
DI float sel4(const f32x4& v, int i) { const float a = (i & 1) ? v[1] : v[0], b = (i & 1) ? v[3] : v[2]; return (i & 2) ? b : a; }
DI float xsum32(float s) {
  const unsigned u = __builtin_bit_cast(unsigned, s);
  const auto r = __builtin_amdgcn_permlane32_swap(u, u, false, false);
  return asf(r[0]) + asf(r[1]);
}
constexpr int VRES = 176;
constexpr int P3_HUP = 10;
constexpr int P3_LASTU = 24;
constexpr int p3_dma_start(int nu) { int d = nu - 15; int a = 76 + 15 * d, b = 36 + 20 * d; return a > b ? a : b; }
constexpr int p3_dma_at(int g) {
  for (int nu = 14; nu <= P3_LASTU; ++nu) { int s = p3_dma_start(nu); if (g >= s && g < s + 15 && (g - s) % 3 == 0) return nu * 8 + (g - s) / 3; }
  return -1;
}
constexpr int p3_w4a_at(int g) { return (g >= 80 && (g - 80) % 5 == 0 && (g - 80) / 5 < 30) ? (g - 80) / 5 : -1; }
constexpr int p3_epi_start(int p) { return p < 5 ? 83 + 30 * p : p == 5 ? 243 : 280; }
constexpr int P3_EPI_ST = 27;
constexpr int P3_POLL_A0 = 218, P3_CHK_A0 = 235, P3_POLL_A1 = 275, P3_AL0 = 236;
constexpr bool p3_hload(int g) { return g < 80 && (g & 1) && (g >> 1) + P3_HUP < 40; }
constexpr bool p3_aload(int g) { return g >= P3_AL0 && g < P3_AL0 + 32 && !(g & 1); }
constexpr bool p3_store(int g) { for (int p = 0; p < 6; ++p) if (g == p3_epi_start(p) + P3_EPI_ST) return true; return false; }
constexpr int p3_nvm(int g) { return p3_hload(g) + (p3_dma_at(g) >= 0) + (p3_w4a_at(g) >= 0) + p3_aload(g) + p3_store(g) + (g == P3_POLL_A0) + (g == P3_POLL_A1) + (g == 176) + (g == 236); }
constexpr int p3_cnt(int gx, int g) { int n = 0; for (int k = gx + 1; k < g; ++k) n += p3_nvm(k); return n > 63 ? 63 : n; }
constexpr int areg(int u, int mm) { return (u == 0 ? 240 : u < 11 ? 16 * (u - 1) : 16 * (u - 11)) + 4 * mm; }
typedef unsigned v16u __attribute__((ext_vector_type(16)));
DI const void* ptr2(unsigned lo, unsigned hi) { return (const void*)(((unsigned long long)hi << 32) | lo); }

__global__ void __launch_bounds__(256, 1) __attribute__((amdgpu_num_vgpr(VRES)))
fused_block(const void* __restrict__ xp_, const void* __restrict__ wqkvp_, const void* __restrict__ wop_, const void* __restrict__ wgp_,
            const void* __restrict__ wup_, const void* __restrict__ wdp_, void* __restrict__ outp_, char* __restrict__ wsp_) {
  extern __shared__ __attribute__((aligned(16))) char smem_[];
  LAS char* L = (LAS char*)smem_;
  unsigned long long pc0; unsigned codelen; v16u ka;
  asm volatile("s_load_dwordx16 %2, %3, 0\nfbk_begin:\n\ts_getpc_b64 %0\n\ts_mov_b32 %1, fbk_end-fbk_begin"
               : "=&s"(pc0), "=&s"(codelen), "=&s"(ka) : "s"(__builtin_amdgcn_kernarg_segment_ptr()) : ACLOB, "v255");
  FENCE();
  const int tid = threadIdx.x, lane = tid & 63;
  const int w = __builtin_amdgcn_readfirstlane(tid >> 6);
  const int b = blockIdx.x, xcd = b & 7, bi = b >> 3;
  const int r = (xcd >> 1) * 4 + (bi & 3), c = (xcd & 1) * 8 + (bi >> 2);
  const int j = (c >> 3) * 4 + (c & 3), hf = (c >> 2) & 1;
  const int li = lane & 15, lg = lane >> 4;
  const unsigned lane16 = lane * 16, lane16b = lane16 + 1024;
  const unsigned ldsw = w * 5120;
  int budget = 200000;
  const int drow = lane >> 3, dch = lane & 7, tq = li >> 2, tp = li & 3;
  const int pp = 2 * r + hf;
  const unsigned xst = L_XST + w * 20480;
  unsigned vo1, vo1b, vo2, vct;
  {
    const int z = 4 * w + (dch & 3);
    int col = 640 + 8 * z;
    col = z >= 5 ? 1232 + 8 * z : col; col = z == 5 ? 1312 : col; col = z >= 10 ? 8 * z - 80 : col; col = z == 15 ? 32 : col;
    vo1 = ((40 * r + 8 * (dch >> 2) + drow) * 1920 + 40 * c + col) * 2;
    vo1b = (dch >> 2) ? OOB : vo1;
    pin(vo1); pin(vo1b);
  }
  set_m0<2 * SLOT>(ldsw);
  asm volatile("s_waitcnt lgkmcnt(0)" : "+s"(ka));
  char* const wsp = (char*)ptr2(ka[14], ka[15]);
  const rsrc_t ws = mk(wsp, WS_END);
  const rsrc_t rx = mk(ptr2(ka[0], ka[1]), 2048 * 640 * 2);
  STAMP(0);
  unsigned epoch, cpf;
  {
    const rsrc_t rw = mk(ptr2(ka[2], ka[3]), 640 * 1920 * 2);
    dma<0, false, -1>(vo1, rw, 0, 0);
    dma<1024, false, -1>(vo1, rw, 16 * 3840 - 1024, 0);
    dma<2048, false, -1>(vo1b, rw, 32 * 3840 - 2048, 0);
    const int oc = 32 * (w < 3 ? w : dch >> 1) + pp, n = oc >> 2, g = oc & 3;
    const int ra = 40 * n + 8 * g, rb = 160 * (n - 16) + 40 * g + 32;
    vo2 = ((n < 16 ? ra : rb) + drow) * 1280 + 160 * j + (w < 3 ? dch * 16 : 128 + (dch & 1) * 16);
    vo2 = oc < 80 ? vo2 : OOB;
    vct = tid < 16 ? (unsigned)(bi * 16 + tid) * 128 : OOB;
    const rsrc_t rw2 = mk((const char*)ptr2(ka[4], ka[5]) - 3072, 640 * 640 * 2 + 3072);
    dma<3072, false, 0>(vo2, rw2, 0, xst);
    unsigned z = 0; ld32c<0>(epoch, z, ws, 0);
    const rsrc_t rc = mk((const void*)pc0, codelen);
    asm volatile("buffer_load_dword %0, %1, %2, 0 offen" : "=v"(cpf) : "v"(vct), "s"(rc) : "memory");
  }
  unsigned xv[4];
  SF(4, pc) xv[pc] = drow * 1280 + ((dch ^ (4 * (pc & 1) + lg)) * 16) + pc * (10240 - 1024); SE
  const unsigned xrow = (128 * r + 32 * w) * 1280;
  auto x_piece = [&](auto ch_, auto pc_) __attribute__((always_inline)) {
    constexpr int ch = decltype(ch_)::value, pc = decltype(pc_)::value;
    dma<pc * 1024, false, (pc == 3 ? ((ch + 1) % 5) * 4096 : -1)>(xv[pc], rx, xrow + ch * 128, xst);
  };
  auto x_chunk = [&](auto ch_) __attribute__((always_inline)) { SF(4, pc) x_piece(ch_, IC<pc>{}); SE NOP1(); };
  STAMP(21);
  x_chunk(IC<0>{});
  const unsigned trY = tq * 128 + lg * 32 + tp * 8, trX = tq * 128 + (lg >> 1) * 64 + tp * 16 + (lg & 1) * 8;
  unsigned dst1[3], d2, ta, tb, dc; v4u sel;
  {
    const unsigned s1 = (li == (lg & 1)) ? 0x3f803f80u : 0u;
    sel = v4u{s1, s1, s1, s1};
    const int cg = 2 * w + (lg & 1);
    SF(3, k)
      const int ol = 2 * k + (lg >> 1), oc = 5 * r + ol, ks = oc >> 2;
      const int fr = cg < 5 ? ks * 5 + cg : 95 + ks * 3 + cg;
      dst1[k] = ol < 5 ? (unsigned)(fr * 1024 + (oc & 3) * 256 + li * 16) : OOB;
      pin(dst1[k]);
    SE
    const int oc = 32 * (w < 3 ? w : lg) + pp;
    d2 = oc < 80 ? (unsigned)((oc >> 2) * 5120 + (oc & 3) * 256 + (w < 3 ? lg : 4) * 1024 + li * 16) : OOB;
    ta = 2 * SLOT + ldsw + trY; tb = 2 * SLOT + ldsw + (w < 3 ? trX : trY); dc = lane < 16 ? (unsigned)(16 * w + li) * 8 : OOB;
    pin(sel); pin(d2); pin(ta); pin(tb); pin(dc);
  }
  x_chunk(IC<1>{}); x_chunk(IC<2>{});
  STAMP(22);
  unsigned v3, s4[2], d4[2], v4y, d3, d3s, d4y;
  {
    const int ps = 4 * (w >> 1) + (dch >> 1);
    const unsigned fa = (6 * c + ps) * 32, fb = c < 12 ? (96 + c) * 32 : OOB;
    v3 = (ps < 6 ? fa : ps == 6 ? fb : OOB) + (40 * r + drow) * 3456 + 16 * (dch & 1);
    const int pd = 4 * (w >> 1) + lg;
    d3s = pd < 2 ? 1024u : 0u;
    d3 = (pd < 7 ? (unsigned)(pd * (pd < 2 ? 1024 : 40960)) : OOB) + (w & 1) * 256 + li * 16;
    pin(v3); pin(d3); pin(d3s);
  }
  x_chunk(IC<3>{});
  {
    SF(2, k)
      const int oc = 32 * (2 * w + k) + pp, nn = oc >> 2, g = oc & 3;
      const int ft = (w < 3 ? 6 * (nn & 15) + 2 * w : 2 * nn) + (g >> 1);
      const unsigned sv = (16 * ft + 8 * (g & 1)) * 1280 + 160 * j, dv = WS_W4T + j * 286720 + nn * 5120 + g * 256;
      s4[k] = oc < 216 ? sv : OOB; d4[k] = oc < 216 ? dv : OOB;
    SE
    v4y = (dch < 2 ? s4[0] : dch < 4 ? s4[1] : OOB) + drow * 1280 + 128 + (dch & 1) * 16;
    d4y = (lg == 0 ? d4[0] : lg == 1 ? d4[1] : OOB) + 4096 + li * 16;
    pin(v4y); pin(d4y);
  }
  x_chunk(IC<4>{});
  unsigned fl_r = tid == 0 ? (unsigned)(4 * r) : OOB, fl_c = tid == 0 ? (unsigned)(4 * c) : OOB;
  pin(fl_r); pin(fl_c);
  unsigned tag, vtag;
  {
    STAMP(23);
    asm volatile("s_waitcnt vmcnt(20)" : "+v"(epoch), "+v"(cpf)::"memory");
    STAMP(24);
    SF(3, k) tr_a<160 + 4 * k, k * 1024>(ta); SE
    tr_a<172, 3072>(tb);
    tag = __builtin_amdgcn_readfirstlane(epoch) * 1664525u + 1013904223u;
    vtag = tag; pin(vtag);
    f32x4 cs;
    WAITL(2);
    SF(3, k) mf16_va<k == 0, 160 + 4 * k>(cs, sel); SE
    SF(3, k) st_a<160 + 4 * k>(dst1[k], ws, WS_W1T + c * 163840); SE
    WAITL(0);
    st_a<172>(d2, ws, WS_W2T + j * 102400);
    asm volatile("s_nop 3" : "+v"(cs));
    const float c0 = cs[0], c1 = cs[1];
    st64c(v2u{__builtin_bit_cast(unsigned, c0), __builtin_bit_cast(unsigned, c1)}, dc, ws, WS_CS1 + (c * 16 + r) * 512);
  }
  STAMP(25);
  unsigned xa0 = xst + li * 128 + ((lg ^ (li >> 1)) * 16), xa1 = xst + li * 128 + (((4 + lg) ^ (li >> 1)) * 16);
  auto x_read = [&](auto ch_) __attribute__((always_inline)) {
    constexpr int ch = decltype(ch_)::value;
    dsr128a<(4 * ch + 0) * 4, (ch % 5) * 4096>(xa0);
    dsr128a<(4 * ch + 1) * 4, (ch % 5) * 4096 + 2048>(xa0);
    dsr128a<(4 * ch + 2) * 4, (ch % 5) * 4096>(xa1);
    dsr128a<(4 * ch + 3) * 4, (ch % 5) * 4096 + 2048>(xa1);
  };
  v4u ones = {0x3f803f80u, 0x3f803f80u, 0x3f803f80u, 0x3f803f80u};
  unsigned pv2 = flag_off(0, (lane & 16) ? (c ^ 4) : c) + li * 4;
  pin(ones); pin(pv2);
  f32x4 gr[2], sm[2];
  auto x_stat1 = [&](auto ch_, auto q_) __attribute__((always_inline)) {
    constexpr int ch = decltype(ch_)::value, q = decltype(q_)::value;
    mf16_aa<(ch == 0 && q < 2), (4 * ch + q) * 4, (4 * ch + q) * 4>(gr[q & 1]);
    mf16_va<(ch == 0 && q < 2), (4 * ch + q) * 4>(sm[q & 1], ones);
  };
  const unsigned w1src = WS_W1T + c * 163840 + ldsw, w3src = WS_W3T + c * 286720 + ldsw;
  unsigned pf;
  auto x_step = [&](auto k_) __attribute__((always_inline)) {
    constexpr int k = decltype(k_)::value;
    WAITL(4);
    SF(4, q) x_stat1(IC<k>{}, IC<q>{}); if constexpr (k < 5) x_piece(IC<5 + k>{}, IC<q>{}); SE
  };
  WAITV(21); x_read(IC<0>{});
  WAITV(17); x_read(IC<1>{});
  x_step(IC<0>{});
  WAITV(17); x_read(IC<2>{});
  x_step(IC<1>{});
  STAMP(26);
  WAITV(8);
  BARRIER();
  st32c(vtag, fl_r, ws, flag_off(F_W1, c));
  STAMP(1);
  x_read(IC<3>{}); x_step(IC<2>{});
  x_read(IC<4>{}); x_step(IC<3>{});
  WAITV(13); x_read(IC<5>{});
  x_step(IC<4>{});
  ld32c<0>(pf, pv2, ws, F_W1 * 4096);
  {
    const v4u z4 = {0u, 0u, 0u, 0u};
    *(LAS v4u*)(L + L_KVT + tid * 16) = z4;
    if (tid < 16) *(LAS v4u*)(L + L_KVT + 4096 + tid * 16) = z4;
  }
  STAMP(2);
  WAITV(14); x_read(IC<6>{}); x_step(IC<5>{});
  WAITV(9); x_read(IC<7>{}); x_step(IC<6>{});
  WAITV(5); x_read(IC<8>{}); x_step(IC<7>{});
  set_m0<0>(ldsw);
  poll_chk<0>(pf, ws, pv2, F_W1 * 4096, tag, budget);
  STAMP(3);
  x_read(IC<9>{});
  SF(5, f) unit_dma<0, f, slot_of(1)>(ws, lane16, lane16b, w1src, ldsw); if constexpr (f >= 3) NOP1(); SE
  WAITL(0);
  SF(2, k) SF(4, q)
    x_stat1(IC<8 + k>{}, IC<q>{});
    constexpr int i = 4 * k + q;
    if constexpr (i < 5) unit_dma<1, i, slot_of(2)>(ws, lane16, lane16b, w1src + SLOT, ldsw);
    else unit_dma<2, i - 5, -1>(ws, lane16, lane16b, w1src + 2 * SLOT, ldsw);
  SE SE
  unit_dma<2, 3, -1>(ws, lane16, lane16b, w1src + 2 * SLOT, ldsw); NOP1();
  unit_dma<2, 4, slot_of(3)>(ws, lane16, lane16b, w1src + 2 * SLOT, ldsw);
  const unsigned lb0 = lane16, lb1 = lane16 + 3 * SLOT, lb2 = lane16 + 6 * SLOT;
  auto rd = [&](auto fr_, v4u& d) __attribute__((always_inline)) {
    constexpr int fr = decltype(fr_)::value, sl = (fr / 20) % 7, off = (sl % 3) * SLOT + (fr % 20) * 1024;
    dsr128<off>(d, sl < 3 ? lb0 : sl < 6 ? lb1 : lb2);
  };
  f32x4 akv[5][2], aq[3][2], enb[2], era[2];
  float ecs[5];
  const unsigned v4x = drow * 1280 + dch * 16, d4x = lg * 1024 + li * 16;
  const rsrc_t rw3 = mk((w & 1) ? ptr2(ka[8], ka[9]) : ptr2(ka[6], ka[7]), 640 * 1728 * 2);
  const rsrc_t rw4 = mk((const char*)ptr2(ka[10], ka[11]) - 2048, 1728 * 640 * 2 + 2048);
  {
    v4u bf[8];
    unsigned cs1p[8];
    unsigned vc1 = (lane >> 5) * 4096 + (32 * w + (lane & 31)) * 4;
    asm volatile("s_nop 3");
    float sta[2], stb[2];
    SF(2, m)
      pin(gr[m]); pin(sm[m]);
      const float sq = sel4(gr[m], tp);
      const float mean = sm[m][0] * (1.f / 640.f);
      const float rs = __builtin_amdgcn_rsqf(fmaxf(sq * (1.f / 640.f) - mean * mean, 0.f) + 1e-5f);
      stb[m] = -mean * rs; sta[m] = rs;
    SE
    if (lg == tq) {
      SF(2, m) *(LAS float*)(L + L_MEAN + (32 * w + 16 * m + li) * 4) = stb[m]; *(LAS float*)(L + L_RSTD + (32 * w + 16 * m + li) * 4) = sta[m]; SE
    }
    unsigned ea = L_MEAN + (32 * w + 4 * lg) * 4, ec = L_CS1 + li * 4;
    pin(vc1); pin(ea); pin(ec);
    WAITV(10);
    BARRIER();
    STAMP(4);
    SF(6, f) rd(IC<f>{}, bf[f]); SE
    SF(100, f)
      constexpr int ks = f / 5, t = f % 5;
      if constexpr (f % 20 == 14 && f < 80) { WAITV(f == 14 ? 14 : f == 34 ? 19 : f == 54 ? 16 : 15); BARRIER(); }
      if constexpr (!(f & 1)) WL(f + 6 <= 100 ? 4 : f == 96 ? 6 : 9);
      mf16_av<ks == 0, (2 * ks) * 4>(akv[t][0], bf[f & 7]);
      if constexpr (f + 6 < 100) rd(IC<f + 6>{}, bf[(f + 6) & 7]);
      mf16_av<ks == 0, (2 * ks + 1) * 4>(akv[t][1], bf[f & 7]);
      if constexpr (f % 3 == 1 && f < 75) { constexpr int i = f / 3; unit_dma<3 + i / 5, i % 5, slot_of(4 + i / 5)>(ws, lane16, lane16b, w1src + (3 + i / 5) * SLOT, ldsw); }
      if constexpr (f >= 76 && f % 3 == 1) {
        constexpr int k = (f - 76) / 3;
        if constexpr (k < 5) dma<(k < 4 ? k : 3) * 1024, false, (k == 3 ? (int)SLOT + 1024 : k == 4 ? 2 * (int)SLOT : -1)>(v3, rw3, k * 8 * 3456 - (k < 4 ? k : 3) * 1024, ldsw);
        else if constexpr (k == 5) dma<0, false, -1>(v4x, rw4, s4[0] + 2048, 0);
        else if constexpr (k == 6) dma<1024, false, -1>(v4x, rw4, s4[1] + 1024, 0);
        else dma<2048, false, slot_of(8)>(v4y, rw4, 0, ldsw);
      }
      if constexpr (f % 3 == 2 && f < 24) ld32c<(f / 3) * 512>(cs1p[f / 3], vc1, ws, WS_CS1 + c * 8192);
      if constexpr (f == 94) { dsr128f<0>(enb[0], ea); dsr128f<512>(era[0], ea); }
      if constexpr (f == 95) { dsr128f<64>(enb[1], ea); dsr128f<576>(era[1], ea); }
      if constexpr (f == 96) { dsr32f<0>(ecs[0], ec); dsr32f<64>(ecs[1], ec); dsr32f<128>(ecs[2], ec); }
      if constexpr (f == 97) { dsr32f<192>(ecs[3], ec); dsr32f<256>(ecs[4], ec); }
      if constexpr (f == 56) {
        WAITV(11);
        SF(8, k) pin(cs1p[k]); SE
        const float s = xsum32(((asf(cs1p[0]) + asf(cs1p[1])) + (asf(cs1p[2]) + asf(cs1p[3]))) + ((asf(cs1p[4]) + asf(cs1p[5])) + (asf(cs1p[6]) + asf(cs1p[7]))));
        *(LAS float*)(L + L_CS1 + (32 * w + (lane & 1) * 16 + ((lane & 31) >> 1)) * 4) = s;
      }
    SE
  }
  STAMP(5);
  const unsigned w2src = WS_W2T + j * 102400 + w * 25600;
  auto ldw2 = [&](auto i_) __attribute__((always_inline)) {
    constexpr int i = decltype(i_)::value;
    ld_a<(i < 24 ? 160 + 4 * i : 0), (i & 3) * 1024>(lane16, ws, w2src + (i >> 2) * 4096);
  };
  {
    v4u fr[5];
    unsigned ib = L_RED + w * 3360 + (5 * li + lg) * 16, iz = L_RED + w * 3360 + (lg ? 204 + lg : 104) * 16;
    pin(ib); pin(iz);
    asm volatile("s_waitcnt lgkmcnt(0)\n\ts_nop 5" : "+v"(enb[0]), "+v"(enb[1]), "+v"(era[0]), "+v"(era[1]), "+v"(ecs[0]), "+v"(ecs[1]), "+v"(ecs[2]), "+v"(ecs[3]), "+v"(ecs[4])::"memory");
    FENCE();
    SF(5, t)
      float v[2][4];
      const float eps = t < 2 ? 1e-6f : (t == 2 && li < 8) ? 1e-6f : 0.f;
      SF(2, m)
        if constexpr (t < 3) ldw2(IC<2 * t + m>{});
        pin(akv[t][m]);
        SF(4, e)
          float x = fmaf(akv[t][m][e], era[m][e], fmaf(enb[m][e], ecs[t], eps));
          if constexpr (t < 2) x = phi1(x);
          if constexpr (t == 2) { const float y = phi1(x); x = li < 8 ? y : x; }
          v[m][e] = x;
        SE
        fr[t][2 * m] = cvtpk(v[m][0], v[m][1]); fr[t][2 * m + 1] = cvtpk(v[m][2], v[m][3]);
      SE
      if constexpr (t < 3) pin(fr[t]);
    SE
    f32x4 dd[10];
    asm volatile("s_nop 1\n\t"
                 "v_mfma_f32_16x16x32_bf16 %7, %10, %15, 0\n\tv_mfma_f32_16x16x32_bf16 %8, %11, %15, 0\n\tv_mfma_f32_16x16x32_bf16 %9, %12, %15, 0\n\t"
                 "v_mfma_f32_16x16x32_bf16 %4, %11, %12, 0\n\tv_mfma_f32_16x16x32_bf16 %6, %12, %12, 0\n\t"
                 "v_mfma_f32_16x16x32_bf16 %0, %10, %13, 0\n\tv_mfma_f32_16x16x32_bf16 %2, %11, %13, 0\n\t"
                 "v_mfma_f32_16x16x32_bf16 %1, %10, %14, 0\n\tv_mfma_f32_16x16x32_bf16 %3, %11, %14, 0\n\tv_mfma_f32_16x16x32_bf16 %5, %12, %14, 0"
                 : "=&v"(dd[0]), "=&v"(dd[1]), "=&v"(dd[2]), "=&v"(dd[3]), "=&v"(dd[4]), "=&v"(dd[5]), "=&v"(dd[6]), "=&v"(dd[7]), "=&v"(dd[8]), "=&v"(dd[9])
                 : "v"(fr[0]), "v"(fr[1]), "v"(fr[2]), "v"(fr[3]), "v"(fr[4]), "v"(ones));
    FENCE();
    dsw_kv<0, 3, 100 * 16>(dd[7], ib); dsw_kv<1, 3, 0>(dd[8], iz); dsw_kv<2, 3, 208 * 16>(dd[9], ib);
    dsw_kv<1, 2, 124 * 16>(dd[4], ib); dsw_kv<2, 2, 128 * 16>(dd[6], ib);
    dsw_kv<0, 0, 0>(dd[0], ib); dsw_kv<1, 0, 4 * 16>(dd[2], ib);
    dsw_kv<0, 1, 80 * 16>(dd[1], ib); dsw_kv<1, 1, 84 * 16>(dd[3], ib); dsw_kv<2, 1, 88 * 16>(dd[5], ib);
    asm volatile("s_mov_b64 exec, -1" ::: "memory");
    FENCE();
    ldw2(IC<6>{}); ldw2(IC<7>{});
    unsigned ra = L_RED + tid * 16, gv0 = tid < 210 ? (unsigned)tid * 16 : OOB;
    pin(ra); pin(gv0);
    WAITV(16);
    BARRIER_L();
    f32x4 p[4];
    SF(4, k) dsr128f<k * 3360>(p[k], ra); SE
    asm volatile("s_waitcnt lgkmcnt(0)" : "+v"(p[0]), "+v"(p[1]), "+v"(p[2]), "+v"(p[3])::"memory");
    st128c(__builtin_bit_cast(v4u, (p[0] + p[1]) + (p[2] + p[3])), gv0, ws, WS_KVP + (c * 16 + r) * 4096);
  }
  STAMP(6);
  v4u pq[2][2];
  v4u xr[2]; v2u xr4;
  unsigned pvk = flag_off(F_KV, c) + li * 4;
  {
    v4u bf[8];
    SF(6, f) rd(IC<100 + f>{}, bf[f]); SE
    unsigned xo = ((128 * r + 64 * hf + 16 * w + li) * 640 + 80 * j) * 2;
    unsigned xo16 = xo + lg * 16, xo8 = xo + lg * 8;
    pin(xo16); pin(xo8); pin(pvk);
    f32x4 cs3;
    unsigned t3 = SLOT + ldsw + tq * 128 + lg * 32 + (2 * (tp & 1) + (tp >> 1)) * 8, t4x = 2 * SLOT + ldsw + trX, t4y = 2 * SLOT + ldsw + trY;
    unsigned dc3 = lane < 16 ? (unsigned)(((w & 1) * 16 + li) * 32 + (w >> 1) * 16) : OOB;
    const unsigned s13 = li == lg ? 0x3f803f80u : 0u;
    v4u sel3 = {s13, s13, s13, s13};
    pin(t3); pin(t4x); pin(t4y); pin(dc3); pin(sel3);
    WAITV(9);
    SF(60, f)
      constexpr int ks = f / 3, t = f % 3;
      if constexpr (f == 16) { STAMP(30); WAITV(10); BARRIER(); st32c(vtag, fl_r, ws, flag_off(F_KV, c)); STAMP(31); }
      if constexpr (!(f & 1)) WL(f + 6 <= 60 ? 4 : 58 - f);
      mf16_va<ks == 0, (2 * ks) * 4>(aq[t][0], bf[f & 7]);
      if constexpr (f < 16 && !(f & 1)) {
        constexpr int k = f / 2;
        if constexpr (k < 5) tr_a<224 + 4 * k, k * 1024>(t3);
        else tr_a<224 + 4 * k, (k - 5) * 1024>(k < 7 ? t4x : t4y);
      }
      if constexpr (f + 6 < 60) rd(IC<100 + f + 6>{}, bf[(f + 6) & 7]);
      mf16_va<ks == 0, (2 * ks + 1) * 4>(aq[t][1], bf[f & 7]);
      if constexpr (f >= 7 && f < 23 && (f & 1)) {
        constexpr int k = (f - 7) / 2;
        if constexpr (k < 5) { st_a<224 + 4 * k>(d3 + d3s * ((5 * r + k) >> 1), ws, WS_W3T + c * 286720 + (5 * r + k) * 512); mf16_va<k == 0, 224 + 4 * k>(cs3, sel3); }
        else if constexpr (k < 7) st_a<224 + 4 * k>(d4x, ws, d4[(k - 5) & 1]);
        else st_a<224 + 4 * k>(d4y, ws, 0);
      }
      if constexpr (f == 24) {
        pin(cs3);
        st128c(__builtin_bit_cast(v4u, cs3), dc3, ws, WS_CS3 + (c * 16 + r) * 1024);
      }
      if constexpr (f % 3 == 1 && f < 52) ldw2(IC<8 + f / 3>{});
      if constexpr (f == 32) ld128c<0>(xr[0], xo16, rx, 0);
      if constexpr (f == 41) ld128c<64>(xr[1], xo16, rx, 0);
      if constexpr (f == 50) ld64c<128>(xr4, xo8, rx, 0);
      if constexpr (f == 58) ld32c<0>(pf, pvk, ws, 0);
    SE
  }
  STAMP(7);
  const unsigned w4src = WS_W4T + j * 286720 + ldsw;
  auto pfx = [&](auto i_) __attribute__((always_inline)) {
    constexpr int i = decltype(i_)::value;
    if constexpr (i < 30) { unit_dma<8 + i / 5, i % 5, slot_of(9 + i / 5)>(ws, lane16, lane16b, w3src + (i / 5) * SLOT, ldsw); if constexpr (i % 5 >= 3) NOP1(); }
    else { constexpr int k = i - 30; ld_v<VRES + 4 * k, (k % 5 < 4 ? k % 5 : 3) * 1024>(k % 5 < 4 ? lane16 : lane16b, ws, w4src + (k / 5) * SLOT); }
  };
  f32x4 acc2[4][5];
  {
    float nb[2], ra[2]; f32x4 cs[3];
    SF(2, m) nb[m] = *(LAS float*)(L + L_MEAN + (32 * w + 16 * m + li) * 4); ra[m] = *(LAS float*)(L + L_RSTD + (32 * w + 16 * m + li) * 4); SE
    SF(3, t) cs[t] = *(LAS f32x4*)(L + L_CS1 + (80 + 16 * t + 4 * lg) * 4); SE
    asm volatile("s_nop 7" ::: "memory");
    FENCE();
    unsigned pk[2][3][2];
    auto qepi = [&](auto m_, auto t_) __attribute__((always_inline)) {
      constexpr int m = decltype(m_)::value, t = decltype(t_)::value;
      pin(aq[t][m]);
      float v[4];
      SF(4, e) v[e] = phi1(fmaf(aq[t][m][e], ra[m], fmaf(nb[m], cs[t][e], 1e-6f))); SE
      pk[m][t][0] = cvtpk(v[0], v[1]); pk[m][t][1] = cvtpk(v[2], v[3]);
      pin(pk[m][t][0]); pin(pk[m][t][1]);
    };
    SF(3, t) qepi(IC<0>{}, IC<t>{}); SE
    unsigned gv = tid < 210 ? (unsigned)tid * 16 : OOB;
    unsigned kw, k0 = L_KVT + (8 * tq + tp) * 96 + lg * 8, k2 = L_KVT + (li >= 8 ? 24 + li : 42) * 96 + lg * 8, k3 = L_KVT + (tp < 2 ? 40 + tp : 42) * 96 + lg * 8;
    {
      const int hd = tid >= 105, u = tid - 105 * hd, e = (u * 205) >> 10, dq = u - 5 * e + 5 * hd;
      kw = L_KVT + (tid >= 210 ? 43 * 96 : (e == 20 ? 40 + hd : e + 20 * hd) * 96 + dq * 8);
    }
    pin(gv); pin(kw); pin(k0); pin(k2); pin(k3);
    poll_chk<0>(pf, ws, pvk, 0, tag, budget);
    pin(xr[0]); pin(xr[1]); pin(xr4);
    STAMP(8);
    v4u g[16];
    SF(16, k)
      ld128c<0>(g[k], gv, ws, WS_KVP + (c * 16 + k) * 4096);
      if constexpr (k == 3 || k == 7 || k == 11) qepi(IC<1>{}, IC<k / 4>{});
    SE
    SF(2, m)
      pq[m][0] = v4u{pk[m][0][0], pk[m][0][1], pk[m][1][0], pk[m][1][1]};
      pq[m][1] = v4u{lg < 2 ? pk[m][2][0] : 0u, lg < 2 ? pk[m][2][1] : 0u, 0u, 0u};
      pin(pq[m][0]); pin(pq[m][1]);
    SE
    asm volatile("v_accvgpr_write_b32 a106, 0\n\tv_accvgpr_write_b32 a107, 0\n\tv_accvgpr_write_b32 a114, 0\n\tv_accvgpr_write_b32 a115, 0\n\t"
                 "v_accvgpr_write_b32 a122, 0\n\tv_accvgpr_write_b32 a123, 0\n\tv_accvgpr_write_b32 a130, 0\n\tv_accvgpr_write_b32 a131, 0");
    STAMP(9);
    f32x4 s;
    SF(4, q)
      WAITV(12 - 4 * q);
      SF(4, k) pin(g[4 * q + k]); SE
      const f32x4 t4 = (__builtin_bit_cast(f32x4, g[4 * q]) + __builtin_bit_cast(f32x4, g[4 * q + 1])) + (__builtin_bit_cast(f32x4, g[4 * q + 2]) + __builtin_bit_cast(f32x4, g[4 * q + 3]));
      if constexpr (q == 0) s = t4; else s += t4;
      pin(s);
    SE
    *(LAS v2u*)(L + kw) = v2u{cvtpk(s[0], s[1]), cvtpk(s[2], s[3])};
    BARRIER_L();
    st32c(vtag, fl_r, ws, flag_off(F_W4, c));
    STAMP(10);
    SF(4, t1) constexpr int t = (t1 + 3) & 3; SF(3, p) dsr64a<100 + 8 * t + 2 * p, (t == 1 ? 4 * 96 : 0) + p * 32>(t < 2 ? k0 : t == 2 ? k2 : k3); SE SE
    f32x4 od[2][4];
    SF(4, t1)
      constexpr int t = (t1 + 3) & 3;
      WL(9 - 3 * t1);
      SF(2, m) mf16_av<true, 100 + 8 * t>(od[m][t], pq[m][0]); SE
      SF(2, m) mf16_av<false, 104 + 8 * t>(od[m][t], pq[m][1]); SE
    SE
    asm volatile("s_nop 7");
    FENCE();
    SF(2, m)
      SF(4, t) pin(od[m][t]); SE
      const float r0 = __builtin_amdgcn_rcpf(od[m][3][0] + 1e-6f), r1 = __builtin_amdgcn_rcpf(od[m][3][1] + 1e-6f);
      const float ra = lg < 3 ? r0 : r1, rb = lg < 2 ? r0 : r1;
      const v4u o = {cvtpk(od[m][0][0] * ra, od[m][0][1] * ra), cvtpk(od[m][0][2] * ra, od[m][0][3] * ra),
                     cvtpk(od[m][1][0] * rb, od[m][1][1] * rb), cvtpk(od[m][1][2] * rb, od[m][1][3] * rb)};
      const v2u o2 = {cvtpk(od[m][2][0] * r1, od[m][2][1] * r1), cvtpk(od[m][2][2] * r1, od[m][2][3] * r1)};
      const unsigned ob = WS_O + (8 * r + 2 * w + m) * 20480;
      st128c(o, lane16, ws, ob + c * 1024);
      st64c(o2, lg >= 2 ? (unsigned)(li * 16 + (lg - 2) * 8) : OOB, ws, ob + (16 + (c >> 2)) * 1024 + (c & 3) * 256);
    SE
    ld32c<0>(pf, pv2, ws, F_W4 * 4096);
    SF(5, t)
      const unsigned x0 = t < 4 ? xr[(t >> 1) & 1][(t & 1) * 2] : xr4[0], x1 = t < 4 ? xr[(t >> 1) & 1][(t & 1) * 2 + 1] : xr4[1];
      acc2[3][t] = f32x4{bflo(x0), bfhi(x0), bflo(x1), bfhi(x1)};
      pin(acc2[3][t]);
    SE
    WAITV(1);
    BARRIER();
    st32c(vtag, fl_c, ws, flag_off(F_O, r));
    STAMP(11);
    poll_chk<1>(pf, ws, pv2, F_W4 * 4096, tag, budget);
    SF(14, k) pfx(IC<k>{}); SE
    poll_w0(ws, flag_off(F_O, r) + li * 4, tag, budget, w);
    st32c(vtag, (b == 0 && tid == 0) ? 0u : OOB, ws, 0);
    BARRIER();
  }
  STAMP(12);
  const int mt0 = 8 * r + 4 * hf;
  const bool dead = w >= 2;
  auto ldw4a = [&](auto i_) __attribute__((always_inline)) {
    constexpr int i = decltype(i_)::value;
    ld_a<160 + 4 * i, (i % 5 < 4 ? i % 5 : 3) * 1024>(i % 5 < 4 ? lane16 : lane16b, ws, (i >= 15 && dead) ? OOB : w4src + (10 + i / 5) * SLOT);
  };
  v4u hp[2]; v2u hp4;
  const unsigned pb0 = lb0 + ldsw, pb1 = lb1 + ldsw, pb2 = lb2 + ldsw;
  {
    unsigned cs3p[16];
    auto ldo = [&](auto i_) __attribute__((always_inline)) {
      constexpr int i = decltype(i_)::value, mm = i / 5, kk = i % 5;
      ld_a<4 + i * 4, (kk < 4 ? kk : 3) * 1024>(kk < 4 ? lane16 : lane16b, ws, WS_O + (mt0 + ((w + mm + 1) & 3)) * 20480 + ldsw);
    };
    constexpr int UP = 10;
    SF(UP, i) ldo(IC<i>{}); SE
    f32x4 pr[3][5];
    unsigned rb[3];
    SF(3, mm) rb[mm] = slot_of(12 + mm) + ((w + 3 - mm) & 3) * 5120 + lane16; pin(rb[mm]); SE
    SF(20, i)
      constexpr int mm = i / 5, kk = i % 5;
      WAITV(i + UP <= 20 ? UP - 1 : 19 - i + (i - 10));
      if constexpr (i == 17) BARRIER_L();
      SF(5, t)
        constexpr int fb = kk * 5 + t;
        mf16_aa<(kk == 0 && mm < 3), (fb < 24 ? 160 + 4 * fb : 0), 4 + i * 4>(acc2[mm][t]);
        if constexpr (t == 0 && i + UP < 20) ldo(IC<i + UP>{});
        if constexpr (i >= 10 && t == 0) pfx(IC<(i < 16 ? 14 + i - 10 : 30 + i - 16)>{});
        if constexpr (mm > 0 && kk == 1) {
          constexpr int sl = (12 + mm - 1) % 7;
          dsw128f<(sl % 3) * SLOT + t * 1024>(acc2[mm - 1][t], sl < 3 ? pb0 : sl < 6 ? pb1 : pb2);
        }
        if constexpr (i == 17) { dsr128f<t * 1024>(pr[0][t], rb[0]); dsr128f<t * 1024>(pr[1][t], rb[1]); }
        if constexpr (i == 18) dsr128f<t * 1024>(pr[2][t], rb[2]);
        if constexpr (i == 19) { WL(4 - t); pin(pr[0][t]); pin(pr[1][t]); pin(pr[2][t]); pr[0][t] = (pr[0][t] + pr[1][t]) + pr[2][t]; pin(pr[0][t]); }
      SE
    SE
    STAMP(13);
    asm volatile("s_nop 3");
    f32x4 v[5];
    SF(5, t) pin(acc2[3][t]); v[t] = pr[0][t] + acc2[3][t]; SE
    SF(2, k) hp[k] = v4u{cvtpk(v[2 * k][0], v[2 * k][1]), cvtpk(v[2 * k][2], v[2 * k][3]), cvtpk(v[2 * k + 1][0], v[2 * k + 1][1]), cvtpk(v[2 * k + 1][2], v[2 * k + 1][3])}; SE
    hp4 = v2u{cvtpk(v[4][0], v[4][1]), cvtpk(v[4][2], v[4][3])};
    const v4u h4 = {hp4.x, hp4.y, 0u, 0u};
    f32x4 g2, s2;
    mf16_vv<true>(g2, hp[0], hp[0]); mf16_vv<true>(s2, ones, hp[0]);
    mf16_vv<false>(g2, hp[1], hp[1]); mf16_vv<false>(s2, ones, hp[1]);
    mf16_vv<false>(g2, h4, h4); mf16_vv<false>(s2, ones, h4);
    const int mt = mt0 + w;
    const unsigned hb = WS_H + (mt >> 1) * 40960 + 5 * j * 1024 + (mt & 1) * 256;
    const unsigned hv = (lg >> 1) * 1024 + (lg & 1) * 512 + li * 16;
    st128c(hp[0], hv, ws, hb); st128c(hp[1], hv, ws, hb + 2048);
    st64c(hp4, (lg >> 1) * 512 + li * 16 + (lg & 1) * 8, ws, hb + 4096);
    asm volatile("s_nop 1" : "+v"(g2), "+v"(s2));
    const float sq = sel4(g2, tp), sv = s2[0];
    st64c(v2u{__builtin_bit_cast(unsigned, sv), __builtin_bit_cast(unsigned, sq)}, lg == tq ? (unsigned)li * 8 : OOB, ws, WS_HS + (j * 2048 + 16 * mt) * 8);
    SF(16, k) ld32c<(k & 3) * 1024>(cs3p[k], (unsigned)tid * 4, ws, WS_CS3 + c * 16384 + (k >> 2) * 4096); SE
    SF(4, k) pfx(IC<34 + k>{}); SE
    WAITV(20);
    BARRIER();
    st32c(vtag, fl_c, ws, flag_off(F_H, r));
    STAMP(14);
    SF(10, k) pfx(IC<20 + k>{}); SE
    SF(2, k) pfx(IC<38 + k>{}); SE
    {
      WAITV(17);
      SF(16, k) pin(cs3p[k]); SE
      float q4[4];
      SF(4, k) q4[k] = (asf(cs3p[4 * k]) + asf(cs3p[4 * k + 1])) + (asf(cs3p[4 * k + 2]) + asf(cs3p[4 * k + 3])); SE
      *(LAS float*)(L + L_CS3 + ((tid & 7) * 32 + (tid >> 3)) * 4) = (q4[0] + q4[1]) + (q4[2] + q4[3]);
    }
    poll_w0(ws, flag_off(F_H, r) + li * 4, tag, budget, w);
    BARRIER();
  }
  STAMP(15);
  unsigned va[4];
  auto lda = [&](auto u_, auto mm_) __attribute__((always_inline)) {
    constexpr int u = decltype(u_)::value, mm = decltype(mm_)::value;
    ld_a<areg(u, mm), 0>(va[mm], ws, (u == 13 && dead) ? OOB : WS_ACT + u * 4096);
  };
  unsigned pa = flag_off(F_A0, r) + li * 4;
  {
    v2u hs[4];
    const unsigned hsrc = WS_H + (4 * r + w) * 40960;
    SF(4, s) ld_a<4 * s, (s & 3) * 1024>(lane16, ws, hsrc); SE
    v4u bf[6];
    SF(4, k) rd(IC<160 + k>{}, bf[k]); SE
    SF(4, k) ld64c<0>(hs[k], (unsigned)(((lane >> 5) * 4 * 2048 + (lane & 31)) * 8), ws, WS_HS + (k * 2048 + 128 * r + 32 * w) * 8); SE
    SF(P3_HUP - 4, s) ld_a<4 * (s + 4), (s & 3) * 1024>(lane16, ws, hsrc + ((s + 4) >> 2) * 4096); SE
    f32x16 acc[3];
    f32x4 cs[4];
    float nmean, c1, kk2, eG[2], eU[2], eT[2], eO[8];
    unsigned ov[4];
    unsigned csa = L_CS3 + (lane >> 5) * 16;
    unsigned av = ((lane >> 4) & 1) * 57344 + (lane >> 5) * 256 + li * 16;
    SF(4, mm) va[mm] = lane16 + (mt0 + ((w + mm + 1) & 3)) * 57344 + w * 1024; pin(va[mm]); SE
    pin(csa); pin(av); pin(pa);
    const unsigned ab = WS_ACT + (8 * r + 2 * w) * 57344;
    auto epi = [&](auto p_, auto q_) __attribute__((always_inline)) {
      constexpr int p = decltype(p_)::value, q = decltype(q_)::value;
      f32x16& a = acc[p % 3];
      if constexpr (q < 24) {
        constexpr int e = q / 3, ph = q % 3, x = e & 1, y = x ^ 1;
        if constexpr (ph == 0) {
          const float ag = a[e], au = a[8 + e], cg = cs[e >> 2][e & 3], cu = cs[2 + (e >> 2)][e & 3];
          if constexpr (e == 0) asm volatile("v_fma_f32 %0, %2, %3, %4\n\tv_fma_f32 %1, %2, %5, %6" : "=&v"(eG[x]), "=v"(eU[x]) : "v"(nmean), "v"(cg), "v"(ag), "v"(cu), "v"(au));
          else asm volatile("v_rcp_f32 %2, %2\n\tv_fma_f32 %0, %3, %4, %5\n\tv_fma_f32 %1, %3, %6, %7" : "=&v"(eG[x]), "=v"(eU[x]), "+v"(eT[y]) : "v"(nmean), "v"(cg), "v"(ag), "v"(cu), "v"(au));
        } else if constexpr (ph == 1) {
          if constexpr (e == 0) asm volatile("v_mul_f32 %0, %1, %2\n\tv_exp_f32 %0, %0" : "=v"(eT[x]) : "v"(eG[x]), "v"(c1));
          else asm volatile("v_mul_f32 %0, %2, %3\n\tv_mul_f32 %1, %4, %5\n\tv_exp_f32 %0, %0" : "=&v"(eT[x]), "=v"(eO[e - 1]) : "v"(eG[x]), "v"(c1), "v"(eU[y]), "v"(eT[y]));
        } else {
          if constexpr (e >= 2 && !(e & 1)) asm volatile("v_mul_f32 %0, %3, %0\n\tv_fma_f32 %1, %1, %4, %4\n\tv_cvt_pk_bf16_f32 %2, %5, %6" : "+v"(eU[x]), "+v"(eT[x]), "=v"(ov[(e - 2) >> 1]) : "v"(eG[x]), "v"(kk2), "v"(eO[e - 2]), "v"(eO[e - 1]));
          else asm volatile("v_mul_f32 %0, %2, %0\n\tv_fma_f32 %1, %1, %3, %3" : "+v"(eU[x]), "+v"(eT[x]) : "v"(eG[x]), "v"(kk2));
        }
      } else if constexpr (q == 24) asm volatile("v_rcp_f32 %0, %0\n\ts_nop 0" : "+v"(eT[1]));
      else if constexpr (q == 25) asm volatile("v_mul_f32 %1, %1, %2\n\tv_cvt_pk_bf16_f32 %0, %3, %1" : "=v"(ov[3]), "+v"(eU[1]) : "v"(eT[1]), "v"(eO[6]));
      else if constexpr (q == P3_EPI_ST) {
        unsigned so;
        if constexpr (p == 6) so = c < 12 ? ab + (48 + (c >> 1)) * 1024 + (c & 1) * 512 : OOB;
        else so = ab + (16 * (p >> 1) + c) * 1024 + (p & 1) * 512;
        st128c(v4u{ov[0], ov[1], ov[2], ov[3]}, av, ws, so);
      }
    };
    SF(280, g)
      constexpr int p = g < 80 ? (g & 1) : g / 40, s = g < 80 ? (g >> 1) : g % 40;
      if constexpr (g == P3_CHK_A0) poll_chk<p3_cnt(P3_POLL_A0, g)>(pf, ws, pa, 0, tag, budget);
      if constexpr (g % 20 == 16) {
        constexpr int nu = 8 + g / 20 + 1;
        if constexpr (g == 176) WAITV(p3_cnt(p3_epi_start(1) + P3_EPI_ST, g));
        else if constexpr (g == 236) WAITV(p3_cnt(p3_epi_start(3) + P3_EPI_ST, g));
        else if constexpr (nu >= 14 && nu < 22) WAITV(p3_cnt(p3_dma_start(nu) + 12, g));
        BARRIER();
      }
      if constexpr (g < 80 && !(g & 1)) {
        if constexpr (s < 4) WAITV(P3_HUP + 3 - s + p3_cnt(-1, g));
        else if constexpr (s < P3_HUP) WAITV(P3_HUP - 1 - s + p3_cnt(-1, g));
        else WAITV(p3_cnt(2 * (s - P3_HUP) + 1, g));
      }
      WL(g + 4 < 280 ? 3 : 279 - g);
      mf32_va<s == 0, 4 * s>(acc[p % 3], bf[g % 6]);
      SF(7, ep) if constexpr (g >= p3_epi_start(ep) - 8 && g < p3_epi_start(ep) - 4) {
        constexpr int k = g - (p3_epi_start(ep) - 8);
        constexpr int kk = k == 0 ? 0 : k == 1 ? 2 : k == 2 ? 1 : 3;
        dsr128f<ep * 128 + kk * 32>(cs[kk], csa);
      } SE
      if constexpr (g + 4 < 280) rd(IC<160 + g + 4>{}, bf[(g + 4) % 6]);
      if constexpr (g == 176) st32c(vtag, fl_c, ws, flag_off(F_A0, r));
      if constexpr (g == 236) st32c(vtag, fl_c, ws, flag_off(F_A1, r));
      if constexpr (p3_hload(g)) { constexpr int hsx = s + P3_HUP; ld_a<4 * hsx, (hsx & 3) * 1024>(lane16, ws, hsrc + (hsx >> 2) * 4096); }
      if constexpr (p3_dma_at(g) >= 0) {
        constexpr int nu = p3_dma_at(g) / 8, f = p3_dma_at(g) % 8;
        unit_dma<nu, f, slot_of(nu + 1)>(ws, lane16, lane16b, nu < 22 ? w3src + (nu - 8) * SLOT : w4src + (nu - 18) * SLOT, ldsw);
      }
      if constexpr (p3_w4a_at(g) >= 10) ldw4a(IC<p3_w4a_at(g) - 10>{});
      else if constexpr (p3_w4a_at(g) >= 0) pfx(IC<40 + p3_w4a_at(g)>{});
      if constexpr (p3_aload(g)) lda(IC<(g - P3_AL0) / 8>{}, IC<((g - P3_AL0) / 2) % 4>{});
      if constexpr (g == P3_POLL_A0) ld32c<0>(pf, pa, ws, 0);
      if constexpr (g == P3_POLL_A1) ld32c<0>(pf, pa, ws, 4096);
      if constexpr (g == 40) {
        SF(4, k) pin(hs[k]); SE
        const float s1 = xsum32((asf(hs[0].x) + asf(hs[1].x)) + (asf(hs[2].x) + asf(hs[3].x)));
        const float s2 = xsum32((asf(hs[0].y) + asf(hs[1].y)) + (asf(hs[2].y) + asf(hs[3].y)));
        const float mean = s1 * (1.f / 640.f);
        kk2 = fmaxf(s2 * (1.f / 640.f) - mean * mean, 0.f) + 1e-5f;
        nmean = -mean;
        c1 = -1.44269504f * __builtin_amdgcn_rsqf(kk2);
        pin(kk2); pin(nmean); pin(c1);
      }
      SF(6, ep) if constexpr (g >= p3_epi_start(ep) && g < p3_epi_start(ep) + 28) epi(IC<ep>{}, IC<g - p3_epi_start(ep)>{}); SE
    SE
    STAMP(16);
    asm volatile("s_nop 11");
    {
      f32x16& a = acc[0];
      pin(a);
      SF(4, k) pin(cs[k]); SE
      float o[8];
      SF(8, e)
        const float G = fmaf(nmean, cs[e >> 2][e & 3], a[e]), U = fmaf(nmean, cs[2 + (e >> 2)][e & 3], a[8 + e]);
        o[e] = G * U * __builtin_amdgcn_rcpf(fmaf(__builtin_amdgcn_exp2f(G * c1), kk2, kk2));
      SE
      SF(4, k) ov[k] = cvtpk(o[2 * k], o[2 * k + 1]); SE
      epi(IC<6>{}, IC<P3_EPI_ST>{});
    }
    poll_chk<1>(pf, ws, pa, 4096, tag, budget);
    BARRIER();
    st32c(vtag, fl_c, ws, flag_off(F_A2, r));
    STAMP(17);
    STAMP(18);
  }
  {
    f32x4 acc[4][5];
    v4u bf[2][5];
    auto rdw = [&](auto u_, auto t_) __attribute__((always_inline)) {
      constexpr int u = decltype(u_)::value, t = decltype(t_)::value, sl = (18 + u) % 7;
      dsr128<(sl % 3) * SLOT + t * 1024>(bf[u & 1][t], sl < 3 ? pb0 : sl < 6 ? pb1 : pb2);
    };
    SF(5, t)
      const unsigned x0 = t < 4 ? hp[(t >> 1) & 1][(t & 1) * 2] : hp4[0], x1 = t < 4 ? hp[(t >> 1) & 1][(t & 1) * 2 + 1] : hp4[1];
      acc[3][t] = f32x4{bflo(x0), bfhi(x0), bflo(x1), bfhi(x1)};
      pin(acc[3][t]);
    SE
    SF(11, u)
      if constexpr (u == 2) { WAITV(13); BARRIER(); st32c(vtag, fl_c, ws, flag_off(F_A3, r)); }
      if constexpr (u == 4) WAITV(18);
      if constexpr (u == 5) WAITV(17);
      if constexpr (u == 6) poll_chk<5>(pf, ws, pa, 8192, tag, budget);
      if constexpr (u == 8) WAITV(12);
      if constexpr (u == 9) WAITV(8);
      if constexpr (u == 10) poll_chk<0>(pf, ws, pa, 12288, tag, budget);
      if constexpr (u >= 4 && u < 10) WL(0);
      SF(4, mm) SF(5, t)
        if constexpr (u == 0 && mm < 3) mf16_lva0<VRES + 4 * t, areg(u, mm)>(acc[mm][t]);
        else if constexpr (u < 4) mf16_lva<VRES + 20 * u + 4 * t, areg(u, mm)>(acc[mm][t]);
        else if constexpr (u < 10) mf16_va<false, areg(u, mm)>(acc[mm][t], bf[u & 1][t]);
        else mf16_aa<false, 160 + 20 * (u - 10) + 4 * t, areg(u, mm)>(acc[mm][t]);
        constexpr int i = mm * 5 + t;
        if constexpr (u >= 3 && u < 9 && i < 10 && (i & 1)) rdw(IC<u + 1>{}, IC<i / 2>{});
        if constexpr (i == 0 && u == 5) ld32c<0>(pf, pa, ws, 8192);
        if constexpr (i == 0 && u == 9) ld32c<0>(pf, pa, ws, 12288);
        if constexpr (u < 4 && i < 12 && i % 3 == 0) lda(IC<u + 4>{}, IC<i / 3>{});
        if constexpr ((u == 6 || u == 7 || u == 10) && i < 16 && !(i & 1)) lda(IC<(u == 10 ? 12 : 8 + 2 * (u - 6)) + i / 8>{}, IC<(i / 2) % 4>{});
        if constexpr (u < 3 && (i == 12 || i == 16) && 2 * u + (i - 12) / 4 < 5) unit_dma<25, 2 * u + (i - 12) / 4, slot_of(26)>(ws, lane16, lane16b, w4src + 7 * SLOT, ldsw);
        if constexpr ((u == 4 || u == 5) && i % 4 == 2) unit_dma<22 + u, i / 4, slot_of(23 + u)>(ws, lane16, lane16b, w4src + (4 + u) * SLOT, ldsw);
      SE SE
    SE
    f32x4 pr[3][5];
    unsigned rb[3];
    SF(3, mm) rb[mm] = mm * SLOT + ((w + 3 - mm) & 3) * 5120 + lane16; pin(rb[mm]); SE
    SF(4, mm)
      SF(5, t)
        mf16_aa<false, 180 + 4 * t, areg(11, mm)>(acc[mm][t]);
        if constexpr (mm > 0 && t >= 3) dsw128f<(mm - 1) * SLOT + (t - 3) * 1024>(acc[mm - 1][t - 3], pb0);
      SE
      if constexpr (mm == 0) WAITV(4);
      SF(5, t)
        mf16_aa<false, 200 + 4 * t, areg(12, mm)>(acc[mm][t]);
        if constexpr (mm > 0 && t < 3) dsw128f<(mm - 1) * SLOT + (t + 2) * 1024>(acc[mm - 1][t + 2], pb0);
      SE
      if constexpr (mm == 0) WAITV(0);
      if constexpr (mm == 3) BARRIER_L();
      SF(5, t)
        mf16_aa<false, 220 + 4 * t, areg(13, mm)>(acc[mm][t]);
        if constexpr (mm == 3) { SF(3, k) dsr128f<t * 1024>(pr[k][t], rb[k]); SE }
      SE
    SE
    STAMP(19);
    f32x4 v[5];
    SF(5, t)
      WL(12 - 3 * t);
      pin(pr[0][t]); pin(pr[1][t]); pin(pr[2][t]); pin(acc[3][t]);
      v[t] = ((pr[0][t] + pr[1][t]) + pr[2][t]) + acc[3][t];
    SE
    const rsrc_t ro = mk(ptr2(ka[12], ka[13]), 2048 * 640 * 2);
    const unsigned oo = ((128 * r + 64 * hf + 16 * w + li) * 640 + 80 * j) * 2;
    SF(2, k) st128p(v4u{cvtpk(v[2 * k][0], v[2 * k][1]), cvtpk(v[2 * k][2], v[2 * k][3]), cvtpk(v[2 * k + 1][0], v[2 * k + 1][1]), cvtpk(v[2 * k + 1][2], v[2 * k + 1][3])}, oo + lg * 16, ro, k * 64); SE
    st64p(v2u{cvtpk(v[4][0], v[4][1]), cvtpk(v[4][2], v[4][3])}, oo + lg * 8, ro, 128);
  }
  STAMP(20);
  asm volatile("fbk_end:");
}

extern "C" void kernel_launch(void* const* d_in, const int* in_sizes, int n_in, void* d_out, int out_size, void* d_ws, size_t ws_size, hipStream_t stream) {
  (void)in_sizes; (void)n_in; (void)out_size; (void)ws_size;
  (void)hipFuncSetAttribute((const void*)fused_block, hipFuncAttributeMaxDynamicSharedMemorySize, (int)LDS_BYTES);
  fused_block<<<256, 256, LDS_BYTES, stream>>>(d_in[0], d_in[1], d_in[2], d_in[4], d_in[5], d_in[6], d_out, (char*)d_ws);
}
```

```cpp
#include <hip/hip_runtime.h>
#include <utility>
#include <type_traits>

#define PROF 0

#define DI __device__ __forceinline__
#define LAS __attribute__((address_space(3)))
typedef unsigned v4u __attribute__((ext_vector_type(4)));
typedef unsigned v2u __attribute__((ext_vector_type(2)));
typedef float f32x2 __attribute__((ext_vector_type(2)));
typedef float f32x4 __attribute__((ext_vector_type(4)));
typedef float f32x16 __attribute__((ext_vector_type(16)));
typedef __bf16 bf16x2 __attribute__((ext_vector_type(2)));
typedef __amdgpu_buffer_rsrc_t rsrc_t;

template <int N> using IC = std::integral_constant<int, N>;
template <class Fn, int... Is> DI void sfor_impl(Fn&& f, std::integer_sequence<int, Is...>) { (f(IC<Is>{}), ...); }
template <int N, class Fn> DI void sfor(Fn&& f) { sfor_impl(f, std::make_integer_sequence<int, N>{}); }
#define SF(N, i) sfor<N>([&](auto i##_) __attribute__((always_inline)) { constexpr int i = decltype(i##_)::value;
#define SE });

constexpr unsigned WS_FLAG = 4096;
constexpr unsigned WS_PROF = 65536;
constexpr unsigned WS_W1T = 262144;
constexpr unsigned WS_CS1 = WS_W1T + 16 * 163840;
constexpr unsigned WS_W2T = WS_CS1 + 16 * 16 * 512;
constexpr unsigned WS_W3T = WS_W2T + 8 * 102400;
constexpr unsigned WS_CS3 = WS_W3T + 16 * 286720;
constexpr unsigned WS_W4T = WS_CS3 + 16 * 16 * 1024;
constexpr unsigned WS_KVP = WS_W4T + 8 * 286720;
constexpr unsigned WS_O = WS_KVP + 16 * 16 * 4096;
constexpr unsigned WS_H = WS_O + 128 * 20480;
constexpr unsigned WS_HS = WS_H + 64 * 40960;
constexpr unsigned WS_ACT = WS_HS + 8 * 2048 * 8;
constexpr unsigned WS_END = WS_ACT + 128 * 57344;
constexpr unsigned OOB = 0x40000000u;
enum { F_W1 = 0, F_W4 = 1, F_KV = 2, F_O = 3, F_H = 4, F_A0 = 5, F_A1 = 6, F_A2 = 7, F_A3 = 8 };
DI unsigned flag_off(int kind, int grp) { return WS_FLAG + (kind * 16 + grp) * 256; }

constexpr unsigned SLOT = 20480;
constexpr unsigned L_XST = 3 * SLOT;
constexpr unsigned L_MISC = 143360;
constexpr unsigned L_RED = L_MISC;
constexpr unsigned L_MEAN = L_MISC + 16384, L_RSTD = L_MEAN + 512, L_CS1 = L_RSTD + 512, L_CS3 = L_CS1 + 512;
constexpr unsigned LDS_BYTES = 163840;

#define WAITV(n) asm volatile("s_waitcnt vmcnt(%0)" ::"i"(n) : "memory")
#define WAITL(n) asm volatile("s_waitcnt lgkmcnt(%0)" ::"i"(n) : "memory")
#define BARRIER() asm volatile("s_barrier" ::: "memory")
#define BARRIER_L() asm volatile("s_waitcnt lgkmcnt(0)\n\ts_barrier" ::: "memory")
#define FENCE() __builtin_amdgcn_sched_barrier(0)

template <int C> DI void set_m0(unsigned base) { asm volatile("s_add_u32 m0, %0, %c1\n\ts_nop 0" ::"s"(base), "i"(C) : "scc", "memory"); }
template <int IMM, bool SC1, int NEXT> DI void dma(unsigned voff, rsrc_t rs, unsigned soff, unsigned nb) {
  if constexpr (SC1) {
    if constexpr (NEXT >= 0) asm volatile("buffer_load_dwordx4 %0, %1, %2 offen offset:%c3 sc1 lds\n\ts_add_u32 m0, %4, %c5" ::"v"(voff), "s"(rs), "s"(soff), "i"(IMM), "s"(nb), "i"(NEXT) : "scc", "memory");
    else asm volatile("buffer_load_dwordx4 %0, %1, %2 offen offset:%c3 sc1 lds" ::"v"(voff), "s"(rs), "s"(soff), "i"(IMM) : "memory");
  } else {
    if constexpr (NEXT >= 0) asm volatile("buffer_load_dwordx4 %0, %1, %2 offen offset:%c3 lds\n\ts_add_u32 m0, %4, %c5" ::"v"(voff), "s"(rs), "s"(soff), "i"(IMM), "s"(nb), "i"(NEXT) : "scc", "memory");
    else asm volatile("buffer_load_dwordx4 %0, %1, %2 offen offset:%c3 lds" ::"v"(voff), "s"(rs), "s"(soff), "i"(IMM) : "memory");
  }
}
#define NOP1() asm volatile("s_nop 0")
template <int R, int IMM> DI void ld_a(unsigned voff, rsrc_t rs, unsigned soff) {
  asm volatile("buffer_load_dwordx4 a[%c0:%c1], %2, %3, %4 offen offset:%c5 sc1" ::"i"(R), "i"(R + 3), "v"(voff), "s"(rs), "s"(soff), "i"(IMM) : "memory");
}
template <int IMM> DI void ld128c(v4u& d, unsigned voff, rsrc_t rs, unsigned soff) {
  asm volatile("buffer_load_dwordx4 %0, %1, %2, %3 offen offset:%c4 sc1" : "=v"(d) : "v"(voff), "s"(rs), "s"(soff), "i"(IMM) : "memory");
}
template <int IMM> DI void ld64c(v2u& d, unsigned voff, rsrc_t rs, unsigned soff) {
  asm volatile("buffer_load_dwordx2 %0, %1, %2, %3 offen offset:%c4 sc1" : "=v"(d) : "v"(voff), "s"(rs), "s"(soff), "i"(IMM) : "memory");
}
template <int IMM> DI void ld32c(unsigned& d, unsigned voff, rsrc_t rs, unsigned soff) {
  asm volatile("buffer_load_dword %0, %1, %2, %3 offen offset:%c4 sc1" : "=v"(d) : "v"(voff), "s"(rs), "s"(soff), "i"(IMM) : "memory");
}
DI void st128c(v4u d, unsigned voff, rsrc_t rs, unsigned soff) {
  asm volatile("buffer_store_dwordx4 %0, %1, %2, %3 offen sc1\n\ts_nop 1" ::"v"(d), "v"(voff), "s"(rs), "s"(soff) : "memory");
}
DI void st64c(v2u d, unsigned voff, rsrc_t rs, unsigned soff) {
  asm volatile("buffer_store_dwordx2 %0, %1, %2, %3 offen sc1" ::"v"(d), "v"(voff), "s"(rs), "s"(soff) : "memory");
}
DI void st32c(unsigned d, unsigned voff, rsrc_t rs, unsigned soff) {
  asm volatile("buffer_store_dword %0, %1, %2, %3 offen sc1" ::"v"(d), "v"(voff), "s"(rs), "s"(soff) : "memory");
}
DI void st128p(v4u d, unsigned voff, rsrc_t rs, unsigned soff) {
  asm volatile("buffer_store_dwordx4 %0, %1, %2, %3 offen\n\ts_nop 1" ::"v"(d), "v"(voff), "s"(rs), "s"(soff) : "memory");
}
DI void st64p(v2u d, unsigned voff, rsrc_t rs, unsigned soff) {
  asm volatile("buffer_store_dwordx2 %0, %1, %2, %3 offen" ::"v"(d), "v"(voff), "s"(rs), "s"(soff) : "memory");
}
template <int OFF> DI void dsr128(v4u& d, unsigned addr) { asm volatile("ds_read_b128 %0, %1 offset:%c2" : "=v"(d) : "v"(addr), "i"(OFF) : "memory"); }
template <int OFF> DI void dsr128f(f32x4& d, unsigned addr) { asm volatile("ds_read_b128 %0, %1 offset:%c2" : "=v"(d) : "v"(addr), "i"(OFF) : "memory"); }
template <int R, int OFF> DI void dsr128a(unsigned addr) { asm volatile("ds_read_b128 a[%c0:%c1], %2 offset:%c3" ::"i"(R), "i"(R + 3), "v"(addr), "i"(OFF) : "memory"); }
template <int OFF> DI v4u tr_piece(unsigned addr) {
  v2u lo, hi;
  asm volatile("ds_read_b64_tr_b16 %0, %2 offset:%c3\n\tds_read_b64_tr_b16 %1, %2 offset:%c4\n\ts_waitcnt lgkmcnt(0)" : "=&v"(lo), "=&v"(hi) : "v"(addr), "i"(OFF), "i"(OFF + 512) : "memory");
  return v4u{lo.x, lo.y, hi.x, hi.y};
}
template <bool Z, int RA> DI void mf16_av(f32x4& acc, const v4u& b) {
  if constexpr (Z) asm volatile("v_mfma_f32_16x16x32_bf16 %0, a[%c1:%c2], %3, 0" : "=&v"(acc) : "i"(RA), "i"(RA + 3), "v"(b));
  else asm volatile("v_mfma_f32_16x16x32_bf16 %0, a[%c1:%c2], %3, %0" : "+v"(acc) : "i"(RA), "i"(RA + 3), "v"(b));
}
template <bool Z, int RB> DI void mf16_va(f32x4& acc, const v4u& a) {
  if constexpr (Z) asm volatile("v_mfma_f32_16x16x32_bf16 %0, %3, a[%c1:%c2], 0" : "=&v"(acc) : "i"(RB), "i"(RB + 3), "v"(a));
  else asm volatile("v_mfma_f32_16x16x32_bf16 %0, %3, a[%c1:%c2], %0" : "+v"(acc) : "i"(RB), "i"(RB + 3), "v"(a));
}
template <bool Z, int RA, int RB> DI void mf16_aa(f32x4& acc) {
  if constexpr (Z) asm volatile("v_mfma_f32_16x16x32_bf16 %0, a[%c1:%c2], a[%c3:%c4], 0" : "=&v"(acc) : "i"(RA), "i"(RA + 3), "i"(RB), "i"(RB + 3));
  else asm volatile("v_mfma_f32_16x16x32_bf16 %0, a[%c1:%c2], a[%c3:%c4], %0" : "+v"(acc) : "i"(RA), "i"(RA + 3), "i"(RB), "i"(RB + 3));
}
template <bool Z> DI void mf16_vv(f32x4& acc, const v4u& a, const v4u& b) {
  if constexpr (Z) asm volatile("s_nop 1\n\tv_mfma_f32_16x16x32_bf16 %0, %1, %2, 0" : "=&v"(acc) : "v"(a), "v"(b));
  else asm volatile("s_nop 1\n\tv_mfma_f32_16x16x32_bf16 %0, %1, %2, %0" : "+v"(acc) : "v"(a), "v"(b));
}
template <bool Z, int RB> DI void mf32_va(f32x16& acc, const v4u& a) {
  if constexpr (Z) asm volatile("v_mfma_f32_32x32x16_bf16 %0, %3, a[%c1:%c2], 0" : "=&v"(acc) : "i"(RB), "i"(RB + 3), "v"(a));
  else asm volatile("v_mfma_f32_32x32x16_bf16 %0, %3, a[%c1:%c2], %0" : "+v"(acc) : "i"(RB), "i"(RB + 3), "v"(a));
}
template <int RA, int RB> DI void mf16_lva(f32x4& acc) {
  asm volatile("v_mfma_f32_16x16x32_bf16 %0, v[%c1:%c2], a[%c3:%c4], %0" : "+v"(acc) : "i"(RA), "i"(RA + 3), "i"(RB), "i"(RB + 3));
}
template <int RA, int RB> DI void mf16_lva0(f32x4& acc) {
  asm volatile("v_mfma_f32_16x16x32_bf16 %0, v[%c1:%c2], a[%c3:%c4], 0" : "=&v"(acc) : "i"(RA), "i"(RA + 3), "i"(RB), "i"(RB + 3));
}
template <int R, int IMM> DI void ld_v(unsigned voff, rsrc_t rs, unsigned soff) {
  asm volatile("buffer_load_dwordx4 v[%c0:%c1], %2, %3, %4 offen offset:%c5 sc1" ::"i"(R), "i"(R + 3), "v"(voff), "s"(rs), "s"(soff), "i"(IMM) : "memory");
}
#define MFMA_DRAIN() do { asm volatile("s_nop 15" ::: "memory"); FENCE(); } while (0)
template <class T> DI void pin(T& x) { asm volatile("" : "+v"(x)); }

DI unsigned cvtpk(float lo, float hi) { f32x2 v = {lo, hi}; bf16x2 b = __builtin_convertvector(v, bf16x2); return __builtin_bit_cast(unsigned, b); }
DI float asf(unsigned u) { return __builtin_bit_cast(float, u); }
DI float bflo(unsigned u) { return __builtin_bit_cast(float, u << 16); }
DI float bfhi(unsigned u) { return __builtin_bit_cast(float, u & 0xffff0000u); }
DI float sum8(v4u v, float acc) {
  acc += (bflo(v.x) + bfhi(v.x)) + (bflo(v.y) + bfhi(v.y));
  acc += (bflo(v.z) + bfhi(v.z)) + (bflo(v.w) + bfhi(v.w));
  return acc;
}
DI float phi1(float t1) {
  const float e = __builtin_amdgcn_fmed3f(__builtin_amdgcn_exp2f(fmaf(t1, 1.44269504f, -1.44269504e-6f)), 0.f, 1.f);
  return fmaxf(t1, 1e-6f) + e;
}
template <int R, int OFF> DI void tr_a(unsigned addr) {
  asm volatile("ds_read_b64_tr_b16 a[%c0:%c1], %4 offset:%c5\n\tds_read_b64_tr_b16 a[%c2:%c3], %4 offset:%c6" ::"i"(R), "i"(R + 1), "i"(R + 2), "i"(R + 3), "v"(addr), "i"(OFF), "i"(OFF + 512) : "memory");
}
template <int R> DI void st_a(unsigned voff, rsrc_t rs, unsigned soff) {
  asm volatile("buffer_store_dwordx4 a[%c0:%c1], %2, %3, %4 offen sc1" ::"i"(R), "i"(R + 3), "v"(voff), "s"(rs), "s"(soff) : "memory");
}
template <int OFF> DI void dsw128f(const f32x4& d, unsigned addr) { asm volatile("ds_write_b128 %0, %1 offset:%c2" ::"v"(addr), "v"(d), "i"(OFF) : "memory"); }
DI float v_fma(float a, float b, float c) { float r; asm volatile("v_fma_f32 %0, %1, %2, %3" : "=v"(r) : "v"(a), "v"(b), "v"(c)); return r; }
DI float v_mul(float a, float b) { float r; asm volatile("v_mul_f32 %0, %1, %2" : "=v"(r) : "v"(a), "v"(b)); return r; }
DI float v_exp(float a) { float r; asm volatile("v_exp_f32 %0, %1" : "=v"(r) : "v"(a)); return r; }
DI float v_rcp(float a) { float r; asm volatile("v_rcp_f32 %0, %1" : "=v"(r) : "v"(a)); return r; }
DI unsigned v_cvtpk(float a, float b) { unsigned r; asm volatile("v_cvt_pk_bf16_f32 %0, %1, %2" : "=v"(r) : "v"(a), "v"(b)); return r; }
DI rsrc_t mk(const void* p, unsigned bytes) { return __builtin_amdgcn_make_buffer_rsrc((void*)p, 0, (int)bytes, 0x00020000); }

#define ACLOB "a0","a1","a2","a3","a4","a5","a6","a7","a8","a9","a10","a11","a12","a13","a14","a15","a16","a17","a18","a19","a20","a21","a22","a23","a24","a25","a26","a27","a28","a29","a30","a31","a32","a33","a34","a35","a36","a37","a38","a39","a40","a41","a42","a43","a44","a45","a46","a47","a48","a49","a50","a51","a52","a53","a54","a55","a56","a57","a58","a59","a60","a61","a62","a63","a64","a65","a66","a67","a68","a69","a70","a71","a72","a73","a74","a75","a76","a77","a78","a79","a80","a81","a82","a83","a84","a85","a86","a87","a88","a89","a90","a91","a92","a93","a94","a95","a96","a97","a98","a99","a100","a101","a102","a103","a104","a105","a106","a107","a108","a109","a110","a111","a112","a113","a114","a115","a116","a117","a118","a119","a120","a121","a122","a123","a124","a125","a126","a127","a128","a129","a130","a131","a132","a133","a134","a135","a136","a137","a138","a139","a140","a141","a142","a143","a144","a145","a146","a147","a148","a149","a150","a151","a152","a153","a154","a155","a156","a157","a158","a159","a160","a161","a162","a163","a164","a165","a166","a167","a168","a169","a170","a171","a172","a173","a174","a175","a176","a177","a178","a179","a180","a181","a182","a183","a184","a185","a186","a187","a188","a189","a190","a191","a192","a193","a194","a195","a196","a197","a198","a199","a200","a201","a202","a203","a204","a205","a206","a207","a208","a209","a210","a211","a212","a213","a214","a215","a216","a217","a218","a219","a220","a221","a222","a223","a224","a225","a226","a227","a228","a229","a230","a231","a232","a233","a234","a235","a236","a237","a238","a239","a240","a241","a242","a243","a244","a245","a246","a247","a248","a249","a250","a251","a252","a253","a254","a255"

DI void poll_w0(rsrc_t ws, unsigned voff, unsigned tag, int& budget, int w) {
  unsigned f;
  asm volatile("s_cmp_lg_u32 %5, 0\n\ts_cbranch_scc1 2f\n"
               "1:\n\tbuffer_load_dword %0, %2, %3, 0 offen sc1\n\ts_waitcnt vmcnt(0)\n\tv_cmp_ne_u32 vcc, %4, %0\n\ts_nop 1\n\ts_cbranch_vccz 2f\n\t"
               "s_sub_u32 %1, %1, 1\n\ts_cmp_gt_i32 %1, 0\n\ts_cbranch_scc1 1b\n2:"
               : "=&v"(f), "+s"(budget) : "v"(voff), "s"(ws), "s"(tag), "s"(w) : "vcc", "scc", "memory");
}
template <int N> DI void poll_chk(unsigned f, rsrc_t ws, unsigned voff, unsigned soff, unsigned tag, int& budget) {
  asm volatile("s_waitcnt vmcnt(%c6)\n"
               "1:\n\tv_cmp_ne_u32 vcc, %4, %0\n\ts_nop 1\n\ts_cbranch_vccz 2f\n\t"
               "s_sub_u32 %1, %1, 1\n\ts_cmp_gt_i32 %1, 0\n\ts_cbranch_scc0 2f\n\t"
               "buffer_load_dword %0, %2, %3, %5 offen sc1\n\ts_waitcnt vmcnt(0)\n\ts_branch 1b\n2:"
               : "+v"(f), "+s"(budget) : "v"(voff), "s"(ws), "s"(tag), "s"(soff), "i"(N) : "vcc", "scc", "memory");
}

#define STAMP(i) asm volatile("; MARK %0" ::"i"(i))

constexpr int slot_of(int u) { return (u % 7) * (int)SLOT; }
template <int U, int FR, int NEXT> DI void unit_dma(rsrc_t ws, unsigned lane16, unsigned lane16b, unsigned soff, unsigned ldsw) {
  if constexpr (FR < 3) dma<FR * 1024, true, -1>(lane16, ws, soff, ldsw);
  else if constexpr (FR == 3) dma<3072, true, slot_of(U) + 1024>(lane16, ws, soff, ldsw);
  else dma<3072, true, NEXT>(lane16b, ws, soff, ldsw);
}

#define WL(n) __builtin_amdgcn_s_waitcnt(0xC07F | ((n) << 8))
template <int R> DI void dsr64a(unsigned addr) { asm volatile("ds_read_b64 a[%c0:%c1], %2" ::"i"(R), "i"(R + 1), "v"(addr) : "memory"); }
DI float xsum32(float s) {
  const unsigned u = __builtin_bit_cast(unsigned, s);
  const auto r = __builtin_amdgcn_permlane32_swap(u, u, false, false);
  return asf(r[0]) + asf(r[1]);
}
constexpr int VRES = 176;
constexpr int P3_HUP = 10;
constexpr int P3_LASTU = 24;
constexpr int p3_dma_start(int nu) { int d = nu - 15; int a = 76 + 15 * d, b = 36 + 20 * d; return a > b ? a : b; }
constexpr int p3_dma_at(int g) {
  for (int nu = 14; nu <= P3_LASTU; ++nu) { int s = p3_dma_start(nu); if (g >= s && g < s + 15 && (g - s) % 3 == 0) return nu * 8 + (g - s) / 3; }
  return -1;
}
constexpr int W4A_PRE = 8;
constexpr int p3_w4a_at(int g) { return (g >= 92 && (g - 92) % 6 == 0 && W4A_PRE + (g - 92) / 6 < 20) ? W4A_PRE + (g - 92) / 6 : -1; }
constexpr int p3_epi_start(int p) { return p < 5 ? 83 + 30 * p : p == 5 ? 243 : 280; }
constexpr int P3_EPI_ST = 27;
constexpr int P3_POLL_A0 = 218, P3_CHK_A0 = 235, P3_POLL_A1 = 275, P3_AL0 = 236;
constexpr bool p3_hload(int g) { return g < 80 && (g & 1) && (g >> 1) + P3_HUP < 40; }
constexpr bool p3_aload(int g) { return g >= P3_AL0 && g < P3_AL0 + 32 && !(g & 1); }
constexpr bool p3_store(int g) { for (int p = 0; p < 6; ++p) if (g == p3_epi_start(p) + P3_EPI_ST) return true; return false; }
constexpr int p3_nvm(int g) { return p3_hload(g) + (p3_dma_at(g) >= 0) + (p3_w4a_at(g) >= 0) + p3_aload(g) + p3_store(g) + (g == P3_POLL_A0) + (g == P3_POLL_A1) + (g == 176) + (g == 236); }
constexpr int p3_cnt(int gx, int g) { int n = 0; for (int k = gx + 1; k < g; ++k) n += p3_nvm(k); return n > 63 ? 63 : n; }
constexpr int areg(int u, int mm) { return (u == 0 ? 240 : u < 11 ? 16 * (u - 1) : 16 * (u - 11)) + 4 * mm; }
typedef unsigned v16u __attribute__((ext_vector_type(16)));
DI const void* ptr2(unsigned lo, unsigned hi) { return (const void*)(((unsigned long long)hi << 32) | lo); }

__global__ void __launch_bounds__(256, 1) __attribute__((amdgpu_num_vgpr(VRES)))
fused_block(const void* __restrict__ xp_, const void* __restrict__ wqkvp_, const void* __restrict__ wop_, const void* __restrict__ wgp_,
            const void* __restrict__ wup_, const void* __restrict__ wdp_, void* __restrict__ outp_, char* __restrict__ wsp_) {
  extern __shared__ __attribute__((aligned(16))) char smem_[];
  LAS char* L = (LAS char*)smem_;
  unsigned long long pc0; unsigned codelen; v16u ka;
  asm volatile("s_load_dwordx16 %2, %3, 0\nfbk_begin:\n\ts_getpc_b64 %0\n\ts_mov_b32 %1, fbk_end-fbk_begin"
               : "=&s"(pc0), "=&s"(codelen), "=&s"(ka) : "s"(__builtin_amdgcn_kernarg_segment_ptr()) : ACLOB, "v255");
  FENCE();
  const int tid = threadIdx.x, lane = tid & 63;
  const int w = __builtin_amdgcn_readfirstlane(tid >> 6);
  const int b = blockIdx.x, xcd = b & 7, bi = b >> 3;
  const int r = (xcd >> 1) * 4 + (bi & 3), c = (xcd & 1) * 8 + (bi >> 2);
  const int j = (c >> 3) * 4 + (c & 3), hf = (c >> 2) & 1;
  const int li = lane & 15, lg = lane >> 4;
  const unsigned lane16 = lane * 16, lane16b = lane16 + 1024;
  const unsigned ldsw = w * 5120;
  int budget = 200000;
  const int drow = lane >> 3, dch = lane & 7, tq = li >> 2, tp = li & 3;
  const int pp = 2 * r + hf;
  const unsigned xst = L_XST + w * 20480;
  unsigned vo1, vo1b, vo2, vct;
  {
    const int z = 4 * w + (dch & 3);
    int col = 640 + 8 * z;
    col = z >= 5 ? 1232 + 8 * z : col; col = z == 5 ? 1312 : col; col = z >= 10 ? 8 * z - 80 : col; col = z == 15 ? 32 : col;
    vo1 = ((40 * r + 8 * (dch >> 2) + drow) * 1920 + 40 * c + col) * 2;
    vo1b = (dch >> 2) ? OOB : vo1;
    pin(vo1); pin(vo1b);
  }
  set_m0<2 * SLOT>(ldsw);
  asm volatile("s_waitcnt lgkmcnt(0)" : "+s"(ka));
  char* const wsp = (char*)ptr2(ka[14], ka[15]);
  const rsrc_t ws = mk(wsp, WS_END);
  const rsrc_t rx = mk(ptr2(ka[0], ka[1]), 2048 * 640 * 2);
  STAMP(0);
  unsigned epoch, cpf;
  {
    const rsrc_t rw = mk(ptr2(ka[2], ka[3]), 640 * 1920 * 2);
    dma<0, false, -1>(vo1, rw, 0, 0);
    dma<1024, false, -1>(vo1, rw, 16 * 3840 - 1024, 0);
    dma<2048, false, -1>(vo1b, rw, 32 * 3840 - 2048, 0);
    const int oc = 32 * (w < 3 ? w : dch >> 1) + pp, n = oc >> 2, g = oc & 3;
    const int ra = 40 * n + 8 * g, rb = 160 * (n - 16) + 40 * g + 32;
    vo2 = ((n < 16 ? ra : rb) + drow) * 1280 + 160 * j + (w < 3 ? dch * 16 : 128 + (dch & 1) * 16);
    vo2 = oc < 80 ? vo2 : OOB;
    vct = tid < 16 ? (unsigned)(bi * 16 + tid) * 128 : OOB;
    const rsrc_t rw2 = mk((const char*)ptr2(ka[4], ka[5]) - 3072, 640 * 640 * 2 + 3072);
    dma<3072, false, 0>(vo2, rw2, 0, xst);
    unsigned z = 0; ld32c<0>(epoch, z, ws, 0);
    const rsrc_t rc = mk((const void*)pc0, codelen);
    asm volatile("buffer_load_dword %0, %1, %2, 0 offen" : "=v"(cpf) : "v"(vct), "s"(rc) : "memory");
  }
  unsigned xv[4];
  SF(4, pc) xv[pc] = drow * 1280 + ((dch ^ (4 * (pc & 1) + lg)) * 16) + pc * (10240 - 1024); SE
  const unsigned xrow = (128 * r + 32 * w) * 1280;
  auto x_piece = [&](auto ch_, auto pc_) __attribute__((always_inline)) {
    constexpr int ch = decltype(ch_)::value, pc = decltype(pc_)::value;
    dma<pc * 1024, false, (pc == 3 ? ((ch + 1) % 5) * 4096 : -1)>(xv[pc], rx, xrow + ch * 128, xst);
  };
  auto x_chunk = [&](auto ch_) __attribute__((always_inline)) { SF(4, pc) x_piece(ch_, IC<pc>{}); SE NOP1(); };
  STAMP(21);
  x_chunk(IC<0>{});
  const unsigned trY = tq * 128 + lg * 32 + tp * 8, trX = tq * 128 + (lg >> 1) * 64 + tp * 16 + (lg & 1) * 8;
  unsigned dst1[3], d2, ta, tb, dc; v4u sel;
  {
    const unsigned s1 = (li == (lg & 1)) ? 0x3f803f80u : 0u;
    sel = v4u{s1, s1, s1, s1};
    const int cg = 2 * w + (lg & 1);
    SF(3, k)
      const int ol = 2 * k + (lg >> 1), oc = 5 * r + ol, ks = oc >> 2;
      const int fr = cg < 5 ? ks * 5 + cg : 95 + ks * 3 + cg;
      dst1[k] = ol < 5 ? (unsigned)(fr * 1024 + (oc & 3) * 256 + li * 16) : OOB;
      pin(dst1[k]);
    SE
    const int oc = 32 * (w < 3 ? w : lg) + pp;
    d2 = oc < 80 ? (unsigned)((oc >> 2) * 5120 + (oc & 3) * 256 + (w < 3 ? lg : 4) * 1024 + li * 16) : OOB;
    ta = 2 * SLOT + ldsw + trY; tb = 2 * SLOT + ldsw + (w < 3 ? trX : trY); dc = lane < 16 ? (unsigned)(16 * w + li) * 8 : OOB;
    pin(sel); pin(d2); pin(ta); pin(tb); pin(dc);
  }
  x_chunk(IC<1>{});
  STAMP(22);
  unsigned fl_r = tid == 0 ? (unsigned)(4 * r) : OOB, fl_c = tid == 0 ? (unsigned)(4 * c) : OOB;
  pin(fl_r); pin(fl_c);
  unsigned tag, vtag;
  {
    STAMP(23);
    asm volatile("s_waitcnt vmcnt(8)" : "+v"(epoch), "+v"(cpf)::"memory");
    STAMP(24);
    SF(3, k) tr_a<160 + 4 * k, k * 1024>(ta); SE
    tr_a<172, 3072>(tb);
    tag = __builtin_amdgcn_readfirstlane(epoch) * 1664525u + 1013904223u;
    vtag = tag; pin(vtag);
    f32x4 cs;
    WAITL(2);
    SF(3, k) mf16_va<k == 0, 160 + 4 * k>(cs, sel); SE
    SF(3, k) st_a<160 + 4 * k>(dst1[k], ws, WS_W1T + c * 163840); SE
    WAITL(0);
    st_a<172>(d2, ws, WS_W2T + j * 102400);
    asm volatile("s_nop 3" : "+v"(cs));
    const float c0 = cs[0], c1 = cs[1];
    st64c(v2u{__builtin_bit_cast(unsigned, c0), __builtin_bit_cast(unsigned, c1)}, dc, ws, WS_CS1 + (c * 16 + r) * 512);
  }
  STAMP(25);
  x_chunk(IC<2>{}); x_chunk(IC<3>{});
  STAMP(26);
  WAITV(8);
  BARRIER();
  st32c(vtag, fl_r, ws, flag_off(F_W1, c));
  STAMP(1);
  unsigned xa0 = xst + li * 128 + ((lg ^ (li >> 1)) * 16), xa1 = xst + li * 128 + (((4 + lg) ^ (li >> 1)) * 16);
  auto x_read = [&](auto ch_) __attribute__((always_inline)) {
    constexpr int ch = decltype(ch_)::value;
    dsr128a<(4 * ch + 0) * 4, (ch % 5) * 4096>(xa0);
    dsr128a<(4 * ch + 1) * 4, (ch % 5) * 4096 + 2048>(xa0);
    dsr128a<(4 * ch + 2) * 4, (ch % 5) * 4096>(xa1);
    dsr128a<(4 * ch + 3) * 4, (ch % 5) * 4096 + 2048>(xa1);
  };
  x_read(IC<0>{}); x_read(IC<1>{});
  x_chunk(IC<4>{});
  v4u ones = {0x3f803f80u, 0x3f803f80u, 0x3f803f80u, 0x3f803f80u};
  unsigned pv2 = flag_off(0, (lane & 16) ? (c ^ 4) : c) + li * 4;
  pin(ones); pin(pv2);
  unsigned ix[10], kx[4][3];
  unsigned v3, s4[2], d4[2], v4x, v4y, d3[5], t3, d4x, d4y, t4x, t4y, dc3; v4u sel3;
  auto lane_consts = [&](auto n_) __attribute__((always_inline)) {
    constexpr int n = decltype(n_)::value;
      if constexpr (n == 0) {
        const int ps = 4 * (w >> 1) + (dch >> 1), ft = ps < 6 ? 6 * c + ps : 96 + c;
        v3 = ((40 * r + drow) * 1728 + ft * 16 + 8 * (dch & 1)) * 2;
        v3 = (ps < 7 && ft < 108) ? v3 : OOB;
        pin(v3);
      }
      if constexpr (n == 1) {
        SF(2, k)
          const int oc = 32 * (2 * w + k) + pp, nn = oc >> 2, g = oc & 3;
          const int fa = 6 * (nn & 15) + 2 * (nn >> 4), fb = 2 * nn, ft = (nn < 48 ? fa : fb) + (g >> 1);
          const unsigned sv = (16 * ft + 8 * (g & 1)) * 1280 + 160 * j, dv = WS_W4T + j * 286720 + nn * 5120 + g * 256;
          s4[k] = oc < 216 ? sv : OOB; d4[k] = oc < 216 ? dv : OOB;
        SE
        v4x = drow * 1280 + dch * 16;
        v4y = (dch < 2 ? s4[0] : dch < 4 ? s4[1] : OOB) + drow * 1280 + 128 + (dch & 1) * 16;
        pin(v4x); pin(v4y);
      }
      if constexpr (n == 2) {
        const int pd = 4 * (w >> 1) + lg;
        const unsigned d30 = pd < 7 ? (unsigned)(pd * (pd < 2 ? 1024 : 40960) + (w & 1) * 256 + li * 16) : OOB;
        SF(5, k) d3[k] = d30 + (pd < 2 ? (unsigned)(((5 * r + k) >> 1) * 1024) : 0u); pin(d3[k]); SE
      }
      if constexpr (n == 3) {
        t3 = SLOT + ldsw + tq * 128 + lg * 32 + (2 * (tp & 1) + (tp >> 1)) * 8;
        const unsigned s1 = li == lg ? 0x3f803f80u : 0u;
        sel3 = v4u{s1, s1, s1, s1};
        dc3 = lane < 16 ? (unsigned)(((w & 1) * 16 + li) * 32 + (w >> 1) * 16) : OOB;
        pin(t3); pin(sel3); pin(dc3);
      }
      if constexpr (n == 3) {
        d4x = lg * 1024 + li * 16; d4y = (lg == 0 ? d4[0] : lg == 1 ? d4[1] : OOB) + 4096 + li * 16;
        t4x = 2 * SLOT + ldsw + trX; t4y = 2 * SLOT + ldsw + trY;
        pin(d4x); pin(d4y); pin(t4x); pin(t4y);
      }
      if constexpr (n == 4 || n == 5) {
        SF(5, i0)
          constexpr int i = 5 * (n - 4) + i0;
          constexpr int a = i < 2 ? 0 : i < 5 ? 1 : i < 7 ? 2 : i - 7;
          constexpr int bb = i == 0 || i == 2 ? 0 : i == 1 || i == 3 || i == 5 ? 1 : i < 7 ? 2 : 3;
          const int dq = 4 * a + lg, hd = dq >= 5;
          int e; bool ok = dq < 10;
          if constexpr (bb < 2) { e = 16 * bb + li - 20 * hd; ok = ok & (e >= 0) & (e < 20); }
          else if constexpr (bb == 2) { e = 24 + li - 20 * hd; ok = ok & (li >= 8) & (hd != 0); }
          else { e = 20; ok = ok & (li == 0); }
          ix[i] = L_RED + w * 4096 + (ok ? hd * 100 + e * 5 + dq : 255) * 16;
          pin(ix[i]);
        SE
      }
      if constexpr (n == 6 || n == 7) {
        SF(2, t0) SF(3, p)
          constexpr int t = 2 * (n - 6) + t0;
          const int dq = 4 * p + lg, hd = dq >= 5;
          int e; bool ok = dq < 10;
          if constexpr (t < 2) { e = 8 * tq + 4 * t + tp - 20 * hd; ok = ok & (e >= 0) & (e < 20); }
          else if constexpr (t == 2) { e = 24 + li - 20 * hd; ok = ok & (li >= 8) & (hd != 0); }
          else { e = 20; ok = ok & (tp == hd); }
          kx[t][p] = L_RED + (ok ? hd * 100 + e * 5 + dq : 255) * 8;
          pin(kx[t][p]);
        SE SE
      }
  };
  f32x4 gr[2], sm[2];
  auto x_stat1 = [&](auto ch_, auto q_) __attribute__((always_inline)) {
    constexpr int ch = decltype(ch_)::value, q = decltype(q_)::value;
    mf16_aa<(ch == 0 && q < 2), (4 * ch + q) * 4, (4 * ch + q) * 4>(gr[q & 1]);
    mf16_va<(ch == 0 && q < 2), (4 * ch + q) * 4>(sm[q & 1], ones);
  };
  const unsigned w1src = WS_W1T + c * 163840 + ldsw, w3src = WS_W3T + c * 286720 + ldsw;
  unsigned pf;
  SF(5, k)
    if constexpr (k >= 1) { WAITV(k < 3 ? 13 : 12); x_read(IC<k + 1>{}); }
    WAITL(4);
    SF(4, q) x_stat1(IC<k>{}, IC<q>{}); x_piece(IC<5 + k>{}, IC<q>{}); SE
    lane_consts(IC<k>{});
  SE
  ld32c<0>(pf, pv2, ws, F_W1 * 4096);
  STAMP(2);
  STAMP(27);
  SF(3, k)
    WAITV(13 - 4 * k); x_read(IC<6 + k>{});
    WAITL(4);
    SF(4, q) x_stat1(IC<5 + k>{}, IC<q>{}); SE
    lane_consts(IC<5 + k>{});
  SE
  set_m0<0>(ldsw);
  poll_chk<0>(pf, ws, pv2, F_W1 * 4096, tag, budget);
  STAMP(3);
  x_read(IC<9>{});
  SF(5, f) unit_dma<0, f, slot_of(1)>(ws, lane16, lane16b, w1src, ldsw); if constexpr (f >= 3) NOP1(); SE
  WAITL(0);
  SF(2, k) SF(4, q)
    x_stat1(IC<8 + k>{}, IC<q>{});
    constexpr int i = 4 * k + q;
    if constexpr (i < 5) unit_dma<1, i, slot_of(2)>(ws, lane16, lane16b, w1src + SLOT, ldsw);
    else unit_dma<2, i - 5, -1>(ws, lane16, lane16b, w1src + 2 * SLOT, ldsw);
  SE SE
  unit_dma<2, 3, -1>(ws, lane16, lane16b, w1src + 2 * SLOT, ldsw); NOP1();
  unit_dma<2, 4, slot_of(3)>(ws, lane16, lane16b, w1src + 2 * SLOT, ldsw);
  const unsigned lb0 = lane16, lb1 = lane16 + 3 * SLOT, lb2 = lane16 + 6 * SLOT;
  auto rd = [&](auto fr_, v4u& d) __attribute__((always_inline)) {
    constexpr int fr = decltype(fr_)::value, sl = (fr / 20) % 7, off = (sl % 3) * SLOT + (fr % 20) * 1024;
    dsr128<off>(d, sl < 3 ? lb0 : sl < 6 ? lb1 : lb2);
  };
  f32x4 akv[5][2], aq[3][2];
  const rsrc_t rw3 = mk((w & 1) ? ptr2(ka[8], ka[9]) : ptr2(ka[6], ka[7]), 640 * 1728 * 2);
  const rsrc_t rw4 = mk((const char*)ptr2(ka[10], ka[11]) - 2048, 1728 * 640 * 2 + 2048);
  {
    v4u bf[8];
    unsigned cs1p[8];
    unsigned vc1 = (lane >> 5) * 4096 + (32 * w + (lane & 31)) * 4;
    asm volatile("s_nop 3");
    SF(2, m)
      pin(gr[m]); pin(sm[m]);
      const float sq = tp == 0 ? gr[m][0] : tp == 1 ? gr[m][1] : tp == 2 ? gr[m][2] : gr[m][3];
      const float mean = sm[m][0] * (1.f / 640.f);
      const float rs = __builtin_amdgcn_rsqf(fmaxf(sq * (1.f / 640.f) - mean * mean, 0.f) + 1e-5f);
      const unsigned sa = lg == tq ? (unsigned)((32 * w + 16 * m + li) * 4) : 2560u;
      *(LAS float*)(L + L_MEAN + sa) = -mean * rs;
      *(LAS float*)(L + L_RSTD + sa) = rs;
    SE
    pin(vc1);
    WAITV(10);
    BARRIER();
    STAMP(4);
    SF(6, f) rd(IC<f>{}, bf[f]); SE
    SF(100, f)
      constexpr int ks = f / 5, t = f % 5;
      if constexpr (f % 20 == 14 && f < 80) { WAITV(f == 14 ? 14 : f == 34 ? 19 : f == 54 ? 16 : 15); BARRIER(); }
      if constexpr (!(f & 1)) WL(f + 6 <= 100 ? 4 : 98 - f);
      mf16_av<ks == 0, (2 * ks) * 4>(akv[t][0], bf[f & 7]);
      if constexpr (f + 6 < 100) rd(IC<f + 6>{}, bf[(f + 6) & 7]);
      mf16_av<ks == 0, (2 * ks + 1) * 4>(akv[t][1], bf[f & 7]);
      if constexpr (f % 3 == 1 && f < 75) { constexpr int i = f / 3; unit_dma<3 + i / 5, i % 5, slot_of(4 + i / 5)>(ws, lane16, lane16b, w1src + (3 + i / 5) * SLOT, ldsw); }
      if constexpr (f >= 76 && f % 3 == 1) {
        constexpr int k = (f - 76) / 3;
        if constexpr (k < 5) dma<(k < 4 ? k : 3) * 1024, false, (k == 3 ? (int)SLOT + 1024 : k == 4 ? 2 * (int)SLOT : -1)>(v3, rw3, k * 8 * 3456 - (k < 4 ? k : 3) * 1024, ldsw);
        else if constexpr (k == 5) dma<0, false, -1>(v4x, rw4, s4[0] + 2048, 0);
        else if constexpr (k == 6) dma<1024, false, -1>(v4x, rw4, s4[1] + 1024, 0);
        else dma<2048, false, slot_of(8)>(v4y, rw4, 0, ldsw);
      }
      if constexpr (f % 3 == 2 && f < 24) ld32c<(f / 3) * 512>(cs1p[f / 3], vc1, ws, WS_CS1 + c * 8192);
      if constexpr (f == 56) {
        WAITV(11);
        SF(8, k) pin(cs1p[k]); SE
        const float s = xsum32(((asf(cs1p[0]) + asf(cs1p[1])) + (asf(cs1p[2]) + asf(cs1p[3]))) + ((asf(cs1p[4]) + asf(cs1p[5])) + (asf(cs1p[6]) + asf(cs1p[7]))));
        *(LAS float*)(L + L_CS1 + (32 * w + (lane & 1) * 16 + ((lane & 31) >> 1)) * 4) = s;
      }
    SE
  }
  STAMP(5);
  const unsigned w2src = WS_W2T + j * 102400 + w * 25600;
  auto ldw2 = [&](auto i_) __attribute__((always_inline)) {
    constexpr int i = decltype(i_)::value;
    ld_a<(i < 24 ? 160 + 4 * i : 0), (i & 3) * 1024>(lane16, ws, w2src + (i >> 2) * 4096);
  };
  {
    v4u fr[5];
    f32x4 nb[2], ra[2];
    float cs[5];
    SF(2, m)
      nb[m] = *(LAS f32x4*)(L + L_MEAN + (32 * w + 16 * m + 4 * lg) * 4);
      ra[m] = *(LAS f32x4*)(L + L_RSTD + (32 * w + 16 * m + 4 * lg) * 4);
    SE
    SF(5, t) cs[t] = *(LAS float*)(L + L_CS1 + (16 * t + li) * 4); SE
    asm volatile("s_nop 7" ::: "memory");
    FENCE();
    SF(5, t)
      float v[2][4];
      const float eps = t < 2 ? 1e-6f : (t == 2 && li < 8) ? 1e-6f : 0.f;
      SF(2, m)
        if constexpr (t < 3) ldw2(IC<2 * t + m>{});
        pin(akv[t][m]);
        SF(4, e)
          float x = fmaf(akv[t][m][e], ra[m][e], fmaf(nb[m][e], cs[t], eps));
          if constexpr (t < 2) x = phi1(x);
          if constexpr (t == 2) { const float y = phi1(x); x = li < 8 ? y : x; }
          v[m][e] = x;
        SE
        fr[t][2 * m] = cvtpk(v[m][0], v[m][1]); fr[t][2 * m + 1] = cvtpk(v[m][2], v[m][3]);
      SE
      if constexpr (t < 3) pin(fr[t]);
    SE
    f32x4 dd[10];
    mf16_vv<true>(dd[7], fr[0], ones); mf16_vv<true>(dd[8], fr[1], ones); mf16_vv<true>(dd[9], fr[2], ones);
    mf16_vv<true>(dd[4], fr[1], fr[2]); mf16_vv<true>(dd[6], fr[2], fr[2]);
    mf16_vv<true>(dd[0], fr[0], fr[3]); mf16_vv<true>(dd[2], fr[1], fr[3]);
    mf16_vv<true>(dd[1], fr[0], fr[4]); mf16_vv<true>(dd[3], fr[1], fr[4]); mf16_vv<true>(dd[5], fr[2], fr[4]);
    dsw128f<0>(dd[7], ix[7]); dsw128f<0>(dd[8], ix[8]); dsw128f<0>(dd[9], ix[9]); dsw128f<0>(dd[4], ix[4]); dsw128f<0>(dd[6], ix[6]);
    dsw128f<0>(dd[0], ix[0]); dsw128f<0>(dd[2], ix[2]);
    asm volatile("s_nop 1");
    dsw128f<0>(dd[1], ix[1]); dsw128f<0>(dd[3], ix[3]); dsw128f<0>(dd[5], ix[5]);
    ldw2(IC<6>{}); ldw2(IC<7>{});
    WAITV(16);
    BARRIER_L();
    f32x4 s = *(LAS f32x4*)(L + L_RED + tid * 16);
    SF(3, k) s += *(LAS f32x4*)(L + L_RED + (k + 1) * 4096 + tid * 16); SE
    st128c(__builtin_bit_cast(v4u, s), tid < 210 ? (unsigned)tid * 16 : OOB, ws, WS_KVP + (c * 16 + r) * 4096);
  }
  STAMP(6);
  v4u pq[2][2];
  v4u xr[2]; v2u xr4;
  unsigned pvk = flag_off(F_KV, c) + li * 4;
  {
    v4u bf[8];
    SF(6, f) rd(IC<100 + f>{}, bf[f]); SE
    unsigned xo = ((128 * r + 64 * hf + 16 * w + li) * 640 + 80 * j) * 2;
    unsigned xo16 = xo + lg * 16, xo8 = xo + lg * 8;
    pin(xo16); pin(xo8); pin(pvk);
    f32x4 cs3;
    WAITV(9);
    SF(60, f)
      constexpr int ks = f / 3, t = f % 3;
      if constexpr (f == 16) { STAMP(30); WAITV(10); BARRIER(); st32c(vtag, fl_r, ws, flag_off(F_KV, c)); STAMP(31); }
      if constexpr (f == 40) { WAITV(6); BARRIER(); st32c(vtag, fl_r, ws, flag_off(F_W4, c)); }
      if constexpr (!(f & 1)) WL(f + 6 <= 60 ? 4 : 58 - f);
      mf16_va<ks == 0, (2 * ks) * 4>(aq[t][0], bf[f & 7]);
      if constexpr (f < 16 && !(f & 1)) {
        constexpr int k = f / 2;
        if constexpr (k < 5) tr_a<224 + 4 * k, k * 1024>(t3);
        else tr_a<224 + 4 * k, (k - 5) * 1024>(k < 7 ? t4x : t4y);
      }
      if constexpr (f + 6 < 60) rd(IC<100 + f + 6>{}, bf[(f + 6) & 7]);
      mf16_va<ks == 0, (2 * ks + 1) * 4>(aq[t][1], bf[f & 7]);
      if constexpr (f >= 7 && f < 23 && (f & 1)) {
        constexpr int k = (f - 7) / 2;
        if constexpr (k < 5) { st_a<224 + 4 * k>(d3[k], ws, WS_W3T + c * 286720 + (5 * r + k) * 512); mf16_va<k == 0, 224 + 4 * k>(cs3, sel3); }
        else if constexpr (k < 7) st_a<224 + 4 * k>(d4x, ws, d4[(k - 5) & 1]);
        else st_a<224 + 4 * k>(d4y, ws, 0);
      }
      if constexpr (f == 24) {
        pin(cs3);
        st128c(__builtin_bit_cast(v4u, cs3), dc3, ws, WS_CS3 + (c * 16 + r) * 1024);
      }
      if constexpr (f % 3 == 1 && f < 52) ldw2(IC<8 + f / 3>{});
      if constexpr (f == 32) ld128c<0>(xr[0], xo16, rx, 0);
      if constexpr (f == 41) ld128c<64>(xr[1], xo16, rx, 0);
      if constexpr (f == 50) ld64c<128>(xr4, xo8, rx, 0);
      if constexpr (f == 58) ld32c<0>(pf, pvk, ws, 0);
    SE
  }
  STAMP(7);
  const unsigned w4src = WS_W4T + j * 286720 + ldsw;
  auto pfx = [&](auto i_) __attribute__((always_inline)) {
    constexpr int i = decltype(i_)::value;
    if constexpr (i < 20) { unit_dma<8 + i / 5, i % 5, slot_of(9 + i / 5)>(ws, lane16, lane16b, w3src + (i / 5) * SLOT, ldsw); if constexpr (i % 5 >= 3) NOP1(); }
    else { constexpr int k = i - 20; ld_v<VRES + 4 * k, (k % 5 < 4 ? k % 5 : 3) * 1024>(k % 5 < 4 ? lane16 : lane16b, ws, w4src + (k / 5) * SLOT); }
  };
  f32x4 acc2[4][5];
  {
    float nb[2], ra[2]; f32x4 cs[3];
    SF(2, m) nb[m] = *(LAS float*)(L + L_MEAN + (32 * w + 16 * m + li) * 4); ra[m] = *(LAS float*)(L + L_RSTD + (32 * w + 16 * m + li) * 4); SE
    SF(3, t) cs[t] = *(LAS f32x4*)(L + L_CS1 + (80 + 16 * t + 4 * lg) * 4); SE
    asm volatile("s_nop 7" ::: "memory");
    FENCE();
    unsigned pk[2][3][2];
    auto qepi = [&](auto m_, auto t_) __attribute__((always_inline)) {
      constexpr int m = decltype(m_)::value, t = decltype(t_)::value;
      pin(aq[t][m]);
      float v[4];
      SF(4, e) v[e] = phi1(fmaf(aq[t][m][e], ra[m], fmaf(nb[m], cs[t][e], 1e-6f))); SE
      pk[m][t][0] = cvtpk(v[0], v[1]); pk[m][t][1] = cvtpk(v[2], v[3]);
      pin(pk[m][t][0]); pin(pk[m][t][1]);
    };
    SF(3, t) qepi(IC<0>{}, IC<t>{}); SE
    unsigned gv = tid < 210 ? (unsigned)tid * 16 : OOB;
    pin(gv);
    poll_chk<0>(pf, ws, pvk, 0, tag, budget);
    pin(xr[0]); pin(xr[1]); pin(xr4);
    STAMP(8);
    v4u g[16];
    SF(16, k)
      ld128c<0>(g[k], gv, ws, WS_KVP + (c * 16 + k) * 4096);
      if constexpr (k == 3 || k == 7 || k == 11) qepi(IC<1>{}, IC<k / 4>{});
    SE
    SF(2, m)
      pq[m][0] = v4u{pk[m][0][0], pk[m][0][1], pk[m][1][0], pk[m][1][1]};
      pq[m][1] = v4u{lg < 2 ? pk[m][2][0] : 0u, lg < 2 ? pk[m][2][1] : 0u, 0u, 0u};
      pin(pq[m][0]); pin(pq[m][1]);
    SE
    asm volatile("v_accvgpr_write_b32 a106, 0\n\tv_accvgpr_write_b32 a107, 0\n\tv_accvgpr_write_b32 a114, 0\n\tv_accvgpr_write_b32 a115, 0\n\t"
                 "v_accvgpr_write_b32 a122, 0\n\tv_accvgpr_write_b32 a123, 0\n\tv_accvgpr_write_b32 a130, 0\n\tv_accvgpr_write_b32 a131, 0");
    STAMP(9);
    ld32c<0>(pf, pv2, ws, F_W4 * 4096);
    f32x4 s;
    SF(4, q)
      WAITV(13 - 4 * q);
      SF(4, k) pin(g[4 * q + k]); SE
      const f32x4 t4 = (__builtin_bit_cast(f32x4, g[4 * q]) + __builtin_bit_cast(f32x4, g[4 * q + 1])) + (__builtin_bit_cast(f32x4, g[4 * q + 2]) + __builtin_bit_cast(f32x4, g[4 * q + 3]));
      if constexpr (q == 0) s = t4; else s += t4;
      pin(s);
    SE
    *(LAS v2u*)(L + L_RED + tid * 8) = v2u{cvtpk(s[0], s[1]), cvtpk(s[2], s[3])};
    BARRIER_L();
    STAMP(10);
    SF(4, t) SF(3, p) dsr64a<100 + 8 * t + 2 * p>(kx[t][p]); SE SE
    poll_chk<0>(pf, ws, pv2, F_W4 * 4096, tag, budget);
    f32x4 od[2][4];
    SF(4, t1)
      constexpr int t = (t1 + 3) & 3;
      if constexpr (t1 == 0) WAITL(0);
      SF(2, m) mf16_av<true, 100 + 8 * t>(od[m][t], pq[m][0]); SE
      SF(2, m) mf16_av<false, 104 + 8 * t>(od[m][t], pq[m][1]); SE
      pfx(IC<t1>{});
    SE
    asm volatile("s_nop 7");
    FENCE();
    SF(2, m)
      SF(4, t) pin(od[m][t]); SE
      const float r0 = __builtin_amdgcn_rcpf(od[m][3][0] + 1e-6f), r1 = __builtin_amdgcn_rcpf(od[m][3][1] + 1e-6f);
      const float ra = lg < 3 ? r0 : r1, rb = lg < 2 ? r0 : r1;
      const v4u o = {cvtpk(od[m][0][0] * ra, od[m][0][1] * ra), cvtpk(od[m][0][2] * ra, od[m][0][3] * ra),
                     cvtpk(od[m][1][0] * rb, od[m][1][1] * rb), cvtpk(od[m][1][2] * rb, od[m][1][3] * rb)};
      const v2u o2 = {cvtpk(od[m][2][0] * r1, od[m][2][1] * r1), cvtpk(od[m][2][2] * r1, od[m][2][3] * r1)};
      const unsigned ob = WS_O + (8 * r + 2 * w + m) * 20480;
      st128c(o, lane16, ws, ob + c * 1024);
      st64c(o2, lg >= 2 ? (unsigned)(li * 16 + (lg - 2) * 8) : OOB, ws, ob + (16 + (c >> 2)) * 1024 + (c & 3) * 256);
    SE
    SF(8, k) pfx(IC<4 + k>{}); SE
    SF(5, t)
      const unsigned x0 = t < 4 ? xr[(t >> 1) & 1][(t & 1) * 2] : xr4[0], x1 = t < 4 ? xr[(t >> 1) & 1][(t & 1) * 2 + 1] : xr4[1];
      acc2[3][t] = f32x4{bflo(x0), bfhi(x0), bflo(x1), bfhi(x1)};
      pin(acc2[3][t]);
    SE
    WAITV(8);
    BARRIER();
    st32c(vtag, fl_c, ws, flag_off(F_O, r));
    STAMP(11);
    SF(14, k) pfx(IC<12 + k>{}); SE
    poll_w0(ws, flag_off(F_O, r) + li * 4, tag, budget, w);
    st32c(vtag, (b == 0 && tid == 0) ? 0u : OOB, ws, 0);
    BARRIER();
  }
  STAMP(12);
  const int mt0 = 8 * r + 4 * hf;
  const bool dead = w >= 2;
  auto ldw4a = [&](auto i_) __attribute__((always_inline)) {
    constexpr int i = decltype(i_)::value;
    ld_a<160 + 4 * i, (i % 5 < 4 ? i % 5 : 3) * 1024>(i % 5 < 4 ? lane16 : lane16b, ws, (i >= 15 && dead) ? OOB : w4src + (10 + i / 5) * SLOT);
  };
  v4u hp[2]; v2u hp4;
  const unsigned pb0 = lb0 + ldsw, pb1 = lb1 + ldsw, pb2 = lb2 + ldsw;
  {
    unsigned cs3p[16];
    auto ldo = [&](auto i_) __attribute__((always_inline)) {
      constexpr int i = decltype(i_)::value, mm = i / 5, kk = i % 5;
      ld_a<4 + i * 4, (kk < 4 ? kk : 3) * 1024>(kk < 4 ? lane16 : lane16b, ws, WS_O + (mt0 + ((w + mm + 1) & 3)) * 20480 + ldsw);
    };
    constexpr int UP = 8;
    SF(UP, i) ldo(IC<i>{}); SE
    SF(20, i)
      constexpr int mm = i / 5, kk = i % 5;
      WAITV(i + UP <= 20 ? UP - 1 : 19 - i + 3 * (i - 12));
      SF(5, t)
        constexpr int fb = kk * 5 + t;
        mf16_aa<(kk == 0 && mm < 3), (fb < 24 ? 160 + 4 * fb : 0), 4 + i * 4>(acc2[mm][t]);
        if constexpr (t == 0 && i + UP < 20) ldo(IC<i + UP>{});
        if constexpr (i >= 12 && t == 0) pfx(IC<26 + i - 12>{});
        if constexpr (i >= 12 && (t == 1 || t == 3)) { constexpr int k = 2 * (i - 12) + (t >> 1); ld32c<(k & 3) * 1024>(cs3p[k], (unsigned)tid * 4, ws, WS_CS3 + c * 16384 + (k >> 2) * 4096); }
        if constexpr (mm > 0 && kk == 2) {
          constexpr int sl = (12 + mm - 1) % 7;
          dsw128f<(sl % 3) * SLOT + t * 1024>(acc2[mm - 1][t], sl < 3 ? pb0 : sl < 6 ? pb1 : pb2);
        }
      SE
    SE
    STAMP(13);
    pfx(IC<34>{}); pfx(IC<35>{});
    BARRIER_L();
    f32x4 v[5];
    SF(5, t)
      v[t] = *(LAS f32x4*)(L + slot_of(12) + ((w + 3) & 3) * 5120 + t * 1024 + lane16);
      SF(2, mm) v[t] += *(LAS f32x4*)(L + slot_of(13 + mm) + ((w + 2 - mm) & 3) * 5120 + t * 1024 + lane16); SE
    SE
    SF(5, t) pin(acc2[3][t]); v[t] += acc2[3][t]; SE
    SF(2, k) hp[k] = v4u{cvtpk(v[2 * k][0], v[2 * k][1]), cvtpk(v[2 * k][2], v[2 * k][3]), cvtpk(v[2 * k + 1][0], v[2 * k + 1][1]), cvtpk(v[2 * k + 1][2], v[2 * k + 1][3])}; SE
    hp4 = v2u{cvtpk(v[4][0], v[4][1]), cvtpk(v[4][2], v[4][3])};
    const v4u h4 = {hp4.x, hp4.y, 0u, 0u};
    f32x4 g2, s2;
    mf16_vv<true>(g2, hp[0], hp[0]); mf16_vv<true>(s2, ones, hp[0]);
    mf16_vv<false>(g2, hp[1], hp[1]); mf16_vv<false>(s2, ones, hp[1]);
    mf16_vv<false>(g2, h4, h4); mf16_vv<false>(s2, ones, h4);
    const int mt = mt0 + w;
    const unsigned hb = WS_H + (mt >> 1) * 40960 + 5 * j * 1024 + (mt & 1) * 256;
    const unsigned hv = (lg >> 1) * 1024 + (lg & 1) * 512 + li * 16;
    st128c(hp[0], hv, ws, hb); st128c(hp[1], hv, ws, hb + 2048);
    st64c(hp4, (lg >> 1) * 512 + li * 16 + (lg & 1) * 8, ws, hb + 4096);
    asm volatile("s_nop 1" : "+v"(g2), "+v"(s2));
    const float sq = tp == 0 ? g2[0] : tp == 1 ? g2[1] : tp == 2 ? g2[2] : g2[3], sv = s2[0];
    st64c(v2u{__builtin_bit_cast(unsigned, sv), __builtin_bit_cast(unsigned, sq)}, lg == tq ? (unsigned)li * 8 : OOB, ws, WS_HS + (j * 2048 + 16 * mt) * 8);
    SF(4, k) pfx(IC<36 + k>{}); SE
    SF(4, k) ldw4a(IC<k>{}); SE
    {
      WAITV(12);
      SF(16, k) pin(cs3p[k]); SE
      float s = 0.f;
      SF(16, k) s += asf(cs3p[k]); SE
      *(LAS float*)(L + L_CS3 + ((tid & 7) * 32 + (tid >> 3)) * 4) = s;
    }
    WAITV(8);
    BARRIER_L();
    st32c(vtag, fl_c, ws, flag_off(F_H, r));
    STAMP(14);
    SF(2, u) SF(5, f) unit_dma<12 + u, f, slot_of(13 + u)>(ws, lane16, lane16b, w3src + (4 + u) * SLOT, ldsw); if constexpr (f >= 3) NOP1(); SE SE
    SF(W4A_PRE - 4, k) ldw4a(IC<4 + k>{}); SE
    poll_w0(ws, flag_off(F_H, r) + li * 4, tag, budget, w);
    BARRIER();
  }
  STAMP(15);
  unsigned va[4];
  auto lda = [&](auto u_, auto mm_) __attribute__((always_inline)) {
    constexpr int u = decltype(u_)::value, mm = decltype(mm_)::value;
    ld_a<areg(u, mm), 0>(va[mm], ws, (u == 13 && dead) ? OOB : WS_ACT + u * 4096);
  };
  unsigned pa = flag_off(F_A0, r) + li * 4;
  {
    v2u hs[4];
    const unsigned hsrc = WS_H + (4 * r + w) * 40960;
    SF(4, s) ld_a<4 * s, (s & 3) * 1024>(lane16, ws, hsrc); SE
    v4u bf[6];
    SF(4, k) rd(IC<160 + k>{}, bf[k]); SE
    SF(4, k) ld64c<0>(hs[k], (unsigned)(((lane >> 5) * 4 * 2048 + (lane & 31)) * 8), ws, WS_HS + (k * 2048 + 128 * r + 32 * w) * 8); SE
    SF(P3_HUP - 4, s) ld_a<4 * (s + 4), (s & 3) * 1024>(lane16, ws, hsrc + ((s + 4) >> 2) * 4096); SE
    f32x16 acc[3];
    f32x4 cs[4];
    float nmean, c1, kk2, eG[2], eU[2], eT[2], eO[8];
    unsigned ov[4];
    unsigned csa = L_CS3 + (lane >> 5) * 16;
    unsigned av = ((lane >> 4) & 1) * 57344 + (lane >> 5) * 256 + li * 16;
    SF(4, mm) va[mm] = lane16 + (mt0 + ((w + mm + 1) & 3)) * 57344 + w * 1024; pin(va[mm]); SE
    pin(csa); pin(av); pin(pa);
    const unsigned ab = WS_ACT + (8 * r + 2 * w) * 57344;
    auto epi = [&](auto p_, auto q_) __attribute__((always_inline)) {
      constexpr int p = decltype(p_)::value, q = decltype(q_)::value;
      f32x16& a = acc[p % 3];
      if constexpr (q < 24) {
        constexpr int e = q / 3, ph = q % 3, x = e & 1, y = x ^ 1;
        if constexpr (ph == 0) {
          const float ag = a[e], au = a[8 + e], cg = cs[e >> 2][e & 3], cu = cs[2 + (e >> 2)][e & 3];
          if constexpr (e == 0) asm volatile("v_fma_f32 %0, %2, %3, %4\n\tv_fma_f32 %1, %2, %5, %6" : "=&v"(eG[x]), "=v"(eU[x]) : "v"(nmean), "v"(cg), "v"(ag), "v"(cu), "v"(au));
          else asm volatile("v_rcp_f32 %2, %2\n\tv_fma_f32 %0, %3, %4, %5\n\tv_fma_f32 %1, %3, %6, %7" : "=&v"(eG[x]), "=v"(eU[x]), "+v"(eT[y]) : "v"(nmean), "v"(cg), "v"(ag), "v"(cu), "v"(au));
        } else if constexpr (ph == 1) {
          if constexpr (e == 0) asm volatile("v_mul_f32 %0, %1, %2\n\tv_exp_f32 %0, %0" : "=v"(eT[x]) : "v"(eG[x]), "v"(c1));
          else asm volatile("v_mul_f32 %0, %2, %3\n\tv_mul_f32 %1, %4, %5\n\tv_exp_f32 %0, %0" : "=&v"(eT[x]), "=v"(eO[e - 1]) : "v"(eG[x]), "v"(c1), "v"(eU[y]), "v"(eT[y]));
        } else {
          if constexpr (e >= 2 && !(e & 1)) asm volatile("v_mul_f32 %0, %3, %0\n\tv_fma_f32 %1, %1, %4, %4\n\tv_cvt_pk_bf16_f32 %2, %5, %6" : "+v"(eU[x]), "+v"(eT[x]), "=v"(ov[(e - 2) >> 1]) : "v"(eG[x]), "v"(kk2), "v"(eO[e - 2]), "v"(eO[e - 1]));
          else asm volatile("v_mul_f32 %0, %2, %0\n\tv_fma_f32 %1, %1, %3, %3" : "+v"(eU[x]), "+v"(eT[x]) : "v"(eG[x]), "v"(kk2));
        }
      } else if constexpr (q == 24) asm volatile("v_rcp_f32 %0, %0\n\ts_nop 0" : "+v"(eT[1]));
      else if constexpr (q == 25) asm volatile("v_mul_f32 %1, %1, %2\n\tv_cvt_pk_bf16_f32 %0, %3, %1" : "=v"(ov[3]), "+v"(eU[1]) : "v"(eT[1]), "v"(eO[6]));
      else if constexpr (q == P3_EPI_ST) {
        unsigned so;
        if constexpr (p == 6) so = c < 12 ? ab + (48 + (c >> 1)) * 1024 + (c & 1) * 512 : OOB;
        else so = ab + (16 * (p >> 1) + c) * 1024 + (p & 1) * 512;
        st128c(v4u{ov[0], ov[1], ov[2], ov[3]}, av, ws, so);
      }
    };
    SF(280, g)
      constexpr int p = g < 80 ? (g & 1) : g / 40, s = g < 80 ? (g >> 1) : g % 40;
      if constexpr (g == P3_CHK_A0) poll_chk<p3_cnt(P3_POLL_A0, g)>(pf, ws, pa, 0, tag, budget);
      if constexpr (g % 20 == 16) {
        constexpr int nu = 8 + g / 20 + 1;
        if constexpr (g == 176) WAITV(p3_cnt(p3_epi_start(1) + P3_EPI_ST, g));
        else if constexpr (g == 236) WAITV(p3_cnt(p3_epi_start(3) + P3_EPI_ST, g));
        else if constexpr (nu >= 14 && nu < 22) WAITV(p3_cnt(p3_dma_start(nu) + 12, g));
        BARRIER();
      }
      if constexpr (g < 80 && !(g & 1)) {
        if constexpr (s < 4) WAITV(P3_HUP + 3 - s + p3_cnt(-1, g));
        else if constexpr (s < P3_HUP) WAITV(P3_HUP - 1 - s + p3_cnt(-1, g));
        else WAITV(p3_cnt(2 * (s - P3_HUP) + 1, g));
      }
      WL(g + 4 < 280 ? 3 : 279 - g);
      mf32_va<s == 0, 4 * s>(acc[p % 3], bf[g % 6]);
      SF(7, ep) if constexpr (g >= p3_epi_start(ep) - 8 && g < p3_epi_start(ep) - 4) {
        constexpr int k = g - (p3_epi_start(ep) - 8);
        constexpr int kk = k == 0 ? 0 : k == 1 ? 2 : k == 2 ? 1 : 3;
        dsr128f<ep * 128 + kk * 32>(cs[kk], csa);
      } SE
      if constexpr (g + 4 < 280) rd(IC<160 + g + 4>{}, bf[(g + 4) % 6]);
      if constexpr (g == 176) st32c(vtag, fl_c, ws, flag_off(F_A0, r));
      if constexpr (g == 236) st32c(vtag, fl_c, ws, flag_off(F_A1, r));
      if constexpr (p3_hload(g)) { constexpr int hsx = s + P3_HUP; ld_a<4 * hsx, (hsx & 3) * 1024>(lane16, ws, hsrc + (hsx >> 2) * 4096); }
      if constexpr (p3_dma_at(g) >= 0) {
        constexpr int nu = p3_dma_at(g) / 8, f = p3_dma_at(g) % 8;
        unit_dma<nu, f, slot_of(nu + 1)>(ws, lane16, lane16b, nu < 22 ? w3src + (nu - 8) * SLOT : w4src + (nu - 18) * SLOT, ldsw);
      }
      if constexpr (p3_w4a_at(g) >= 0) ldw4a(IC<p3_w4a_at(g)>{});
      if constexpr (p3_aload(g)) lda(IC<(g - P3_AL0) / 8>{}, IC<((g - P3_AL0) / 2) % 4>{});
      if constexpr (g == P3_POLL_A0) ld32c<0>(pf, pa, ws, 0);
      if constexpr (g == P3_POLL_A1) ld32c<0>(pf, pa, ws, 4096);
      if constexpr (g == 40) {
        SF(4, k) pin(hs[k]); SE
        const float s1 = xsum32((asf(hs[0].x) + asf(hs[1].x)) + (asf(hs[2].x) + asf(hs[3].x)));
        const float s2 = xsum32((asf(hs[0].y) + asf(hs[1].y)) + (asf(hs[2].y) + asf(hs[3].y)));
        const float mean = s1 * (1.f / 640.f);
        kk2 = fmaxf(s2 * (1.f / 640.f) - mean * mean, 0.f) + 1e-5f;
        nmean = -mean;
        c1 = -1.44269504f * __builtin_amdgcn_rsqf(kk2);
        pin(kk2); pin(nmean); pin(c1);
      }
      SF(6, ep) if constexpr (g >= p3_epi_start(ep) && g < p3_epi_start(ep) + 28) epi(IC<ep>{}, IC<g - p3_epi_start(ep)>{}); SE
    SE
    STAMP(16);
    asm volatile("s_nop 11");
    {
      f32x16& a = acc[0];
      pin(a);
      SF(4, k) pin(cs[k]); SE
      float o[8];
      SF(8, e)
        const float G = fmaf(nmean, cs[e >> 2][e & 3], a[e]), U = fmaf(nmean, cs[2 + (e >> 2)][e & 3], a[8 + e]);
        o[e] = G * U * __builtin_amdgcn_rcpf(fmaf(__builtin_amdgcn_exp2f(G * c1), kk2, kk2));
      SE
      SF(4, k) ov[k] = cvtpk(o[2 * k], o[2 * k + 1]); SE
      epi(IC<6>{}, IC<P3_EPI_ST>{});
    }
    poll_chk<1>(pf, ws, pa, 4096, tag, budget);
    BARRIER();
    st32c(vtag, fl_c, ws, flag_off(F_A2, r));
    STAMP(17);
    STAMP(18);
  }
  {
    f32x4 acc[4][5];
    v4u bf[2][5];
    auto rdw = [&](auto u_, auto t_) __attribute__((always_inline)) {
      constexpr int u = decltype(u_)::value, t = decltype(t_)::value, sl = (18 + u) % 7;
      dsr128<(sl % 3) * SLOT + t * 1024>(bf[u & 1][t], sl < 3 ? pb0 : sl < 6 ? pb1 : pb2);
    };
    SF(5, t)
      const unsigned x0 = t < 4 ? hp[(t >> 1) & 1][(t & 1) * 2] : hp4[0], x1 = t < 4 ? hp[(t >> 1) & 1][(t & 1) * 2 + 1] : hp4[1];
      acc[3][t] = f32x4{bflo(x0), bfhi(x0), bflo(x1), bfhi(x1)};
      pin(acc[3][t]);
    SE
    SF(12, u)
      if constexpr (u == 2) { WAITV(13); BARRIER(); st32c(vtag, fl_c, ws, flag_off(F_A3, r)); }
      if constexpr (u == 4) WAITV(18);
      if constexpr (u == 5) WAITV(17);
      if constexpr (u == 6) poll_chk<5>(pf, ws, pa, 8192, tag, budget);
      if constexpr (u == 8) WAITV(12);
      if constexpr (u == 9) WAITV(8);
      if constexpr (u == 10) poll_chk<0>(pf, ws, pa, 12288, tag, budget);
      if constexpr (u >= 4 && u < 10) WL(0);
      SF(4, mm) SF(5, t)
        if constexpr (u == 0 && mm < 3) mf16_lva0<VRES + 4 * t, areg(u, mm)>(acc[mm][t]);
        else if constexpr (u < 4) mf16_lva<VRES + 20 * u + 4 * t, areg(u, mm)>(acc[mm][t]);
        else if constexpr (u < 10) mf16_va<false, areg(u, mm)>(acc[mm][t], bf[u & 1][t]);
        else mf16_aa<false, 160 + 20 * (u - 10) + 4 * t, areg(u, mm)>(acc[mm][t]);
        constexpr int i = mm * 5 + t;
        if constexpr (u >= 3 && u < 9 && i < 10 && (i & 1)) rdw(IC<u + 1>{}, IC<i / 2>{});
        if constexpr (i == 0 && u == 5) ld32c<0>(pf, pa, ws, 8192);
        if constexpr (i == 0 && u == 9) ld32c<0>(pf, pa, ws, 12288);
        if constexpr (u < 4 && i < 12 && i % 3 == 0) lda(IC<u + 4>{}, IC<i / 3>{});
        if constexpr ((u == 6 || u == 7 || u == 10) && i < 16 && !(i & 1)) lda(IC<(u == 10 ? 12 : 8 + 2 * (u - 6)) + i / 8>{}, IC<(i / 2) % 4>{});
        if constexpr (u < 3 && (i == 12 || i == 16) && 2 * u + (i - 12) / 4 < 5) unit_dma<25, 2 * u + (i - 12) / 4, slot_of(26)>(ws, lane16, lane16b, w4src + 7 * SLOT, ldsw);
        if constexpr ((u == 4 || u == 5) && i % 4 == 2) unit_dma<22 + u, i / 4, slot_of(23 + u)>(ws, lane16, lane16b, w4src + (4 + u) * SLOT, ldsw);
      SE SE
    SE
    WAITV(0);
    SF(4, mm)
      SF(5, t) mf16_aa<false, 200 + 4 * t, areg(12, mm)>(acc[mm][t]); SE
      SF(5, t)
        mf16_aa<false, 220 + 4 * t, areg(13, mm)>(acc[mm][t]);
        if constexpr (mm > 0) dsw128f<(mm - 1) * SLOT + t * 1024>(acc[mm - 1][t], pb0);
      SE
    SE
    STAMP(19);
    BARRIER_L();
    f32x4 v[5];
    SF(5, t)
      v[t] = *(LAS f32x4*)(L + ((w + 3) & 3) * 5120 + t * 1024 + lane16);
      SF(2, mm) v[t] += *(LAS f32x4*)(L + (1 + mm) * SLOT + ((w + 2 - mm) & 3) * 5120 + t * 1024 + lane16); SE
    SE
    SF(5, t) pin(acc[3][t]); v[t] += acc[3][t]; SE
    const rsrc_t ro = mk(ptr2(ka[12], ka[13]), 2048 * 640 * 2);
    const unsigned oo = ((128 * r + 64 * hf + 16 * w + li) * 640 + 80 * j) * 2;
    SF(2, k) st128p(v4u{cvtpk(v[2 * k][0], v[2 * k][1]), cvtpk(v[2 * k][2], v[2 * k][3]), cvtpk(v[2 * k + 1][0], v[2 * k + 1][1]), cvtpk(v[2 * k + 1][2], v[2 * k + 1][3])}, oo + lg * 16, ro, k * 64); SE
    st64p(v2u{cvtpk(v[4][0], v[4][1]), cvtpk(v[4][2], v[4][3])}, oo + lg * 8, ro, 128);
  }
  STAMP(20);
  asm volatile("fbk_end:");
}

extern "C" void kernel_launch(void* const* d_in, const int* in_sizes, int n_in, void* d_out, int out_size, void* d_ws, size_t ws_size, hipStream_t stream) {
  (void)in_sizes; (void)n_in; (void)out_size; (void)ws_size;
  (void)hipFuncSetAttribute((const void*)fused_block, hipFuncAttributeMaxDynamicSharedMemorySize, (int)LDS_BYTES);
  fused_block<<<256, 256, LDS_BYTES, stream>>>(d_in[0], d_in[1], d_in[2], d_in[4], d_in[5], d_in[6], d_out, (char*)d_ws);
}
```

```cpp
#include <hip/hip_runtime.h>
#include <utility>
#include <type_traits>

#define PROF 0

#define DI __device__ __forceinline__
#define LAS __attribute__((address_space(3)))
typedef unsigned v4u __attribute__((ext_vector_type(4)));
typedef unsigned v2u __attribute__((ext_vector_type(2)));
typedef float f32x2 __attribute__((ext_vector_type(2)));
typedef float f32x4 __attribute__((ext_vector_type(4)));
typedef float f32x16 __attribute__((ext_vector_type(16)));
typedef __bf16 bf16x2 __attribute__((ext_vector_type(2)));
typedef __amdgpu_buffer_rsrc_t rsrc_t;

template <int N> using IC = std::integral_constant<int, N>;
template <class Fn, int... Is> DI void sfor_impl(Fn&& f, std::integer_sequence<int, Is...>) { (f(IC<Is>{}), ...); }
template <int N, class Fn> DI void sfor(Fn&& f) { sfor_impl(f, std::make_integer_sequence<int, N>{}); }
#define SF(N, i) sfor<N>([&](auto i##_) __attribute__((always_inline)) { constexpr int i = decltype(i##_)::value;
#define SE });

constexpr unsigned WS_FLAG = 4096;
constexpr unsigned WS_PROF = 65536;
constexpr unsigned WS_W1T = 262144;
constexpr unsigned WS_CS1 = WS_W1T + 16 * 163840;
constexpr unsigned WS_W2T = WS_CS1 + 16 * 16 * 512;
constexpr unsigned WS_W3T = WS_W2T + 8 * 102400;
constexpr unsigned WS_CS3 = WS_W3T + 16 * 286720;
constexpr unsigned WS_W4T = WS_CS3 + 16 * 16 * 1024;
constexpr unsigned WS_KVP = WS_W4T + 8 * 286720;
constexpr unsigned WS_O = WS_KVP + 16 * 16 * 4096;
constexpr unsigned WS_H = WS_O + 128 * 20480;
constexpr unsigned WS_HS = WS_H + 64 * 40960;
constexpr unsigned WS_ACT = WS_HS + 8 * 2048 * 8;
constexpr unsigned WS_END = WS_ACT + 128 * 57344;
constexpr unsigned OOB = 0x40000000u;
enum { F_W1 = 0, F_W4 = 1, F_KV = 2, F_O = 3, F_H = 4, F_A0 = 5, F_A1 = 6, F_A2 = 7, F_A3 = 8 };
DI unsigned flag_off(int kind, int grp) { return WS_FLAG + (kind * 16 + grp) * 256; }

constexpr unsigned SLOT = 20480;
constexpr unsigned L_XST = 3 * SLOT;
constexpr unsigned L_MISC = 143360;
constexpr unsigned L_RED = L_MISC;
constexpr unsigned L_KVT = L_MISC + 13440;
constexpr unsigned L_MEAN = L_MISC + 17792, L_RSTD = L_MEAN + 512, L_CS1 = L_RSTD + 512, L_CS3 = L_CS1 + 512;
constexpr unsigned LDS_BYTES = 163840;

#define WAITV(n) asm volatile("s_waitcnt vmcnt(%0)" ::"i"(n) : "memory")
#define WAITL(n) asm volatile("s_waitcnt lgkmcnt(%0)" ::"i"(n) : "memory")
#define BARRIER() asm volatile("s_barrier" ::: "memory")
#define BARRIER_L() asm volatile("s_waitcnt lgkmcnt(0)\n\ts_barrier" ::: "memory")
#define FENCE() __builtin_amdgcn_sched_barrier(0)

template <int C> DI void set_m0(unsigned base) { asm volatile("s_add_u32 m0, %0, %c1\n\ts_nop 0" ::"s"(base), "i"(C) : "scc", "memory"); }
template <int IMM, bool SC1, int NEXT> DI void dma(unsigned voff, rsrc_t rs, unsigned soff, unsigned nb) {
  if constexpr (SC1) {
    if constexpr (NEXT >= 0) asm volatile("buffer_load_dwordx4 %0, %1, %2 offen offset:%c3 sc1 lds\n\ts_add_u32 m0, %4, %c5" ::"v"(voff), "s"(rs), "s"(soff), "i"(IMM), "s"(nb), "i"(NEXT) : "scc", "memory");
    else asm volatile("buffer_load_dwordx4 %0, %1, %2 offen offset:%c3 sc1 lds" ::"v"(voff), "s"(rs), "s"(soff), "i"(IMM) : "memory");
  } else {
    if constexpr (NEXT >= 0) asm volatile("buffer_load_dwordx4 %0, %1, %2 offen offset:%c3 lds\n\ts_add_u32 m0, %4, %c5" ::"v"(voff), "s"(rs), "s"(soff), "i"(IMM), "s"(nb), "i"(NEXT) : "scc", "memory");
    else asm volatile("buffer_load_dwordx4 %0, %1, %2 offen offset:%c3 lds" ::"v"(voff), "s"(rs), "s"(soff), "i"(IMM) : "memory");
  }
}
#define NOP1() asm volatile("s_nop 0")
template <int R, int IMM> DI void ld_a(unsigned voff, rsrc_t rs, unsigned soff) {
  asm volatile("buffer_load_dwordx4 a[%c0:%c1], %2, %3, %4 offen offset:%c5 sc1" ::"i"(R), "i"(R + 3), "v"(voff), "s"(rs), "s"(soff), "i"(IMM) : "memory");
}
template <int IMM> DI void ld128c(v4u& d, unsigned voff, rsrc_t rs, unsigned soff) {
  asm volatile("buffer_load_dwordx4 %0, %1, %2, %3 offen offset:%c4 sc1" : "=v"(d) : "v"(voff), "s"(rs), "s"(soff), "i"(IMM) : "memory");
}
template <int IMM> DI void ld64c(v2u& d, unsigned voff, rsrc_t rs, unsigned soff) {
  asm volatile("buffer_load_dwordx2 %0, %1, %2, %3 offen offset:%c4 sc1" : "=v"(d) : "v"(voff), "s"(rs), "s"(soff), "i"(IMM) : "memory");
}
template <int IMM> DI void ld32c(unsigned& d, unsigned voff, rsrc_t rs, unsigned soff) {
  asm volatile("buffer_load_dword %0, %1, %2, %3 offen offset:%c4 sc1" : "=v"(d) : "v"(voff), "s"(rs), "s"(soff), "i"(IMM) : "memory");
}
DI void st128c(v4u d, unsigned voff, rsrc_t rs, unsigned soff) {
  asm volatile("buffer_store_dwordx4 %0, %1, %2, %3 offen sc1\n\ts_nop 1" ::"v"(d), "v"(voff), "s"(rs), "s"(soff) : "memory");
}
DI void st64c(v2u d, unsigned voff, rsrc_t rs, unsigned soff) {
  asm volatile("buffer_store_dwordx2 %0, %1, %2, %3 offen sc1" ::"v"(d), "v"(voff), "s"(rs), "s"(soff) : "memory");
}
DI void st32c(unsigned d, unsigned voff, rsrc_t rs, unsigned soff) {
  asm volatile("buffer_store_dword %0, %1, %2, %3 offen sc1" ::"v"(d), "v"(voff), "s"(rs), "s"(soff) : "memory");
}
DI void st128p(v4u d, unsigned voff, rsrc_t rs, unsigned soff) {
  asm volatile("buffer_store_dwordx4 %0, %1, %2, %3 offen\n\ts_nop 1" ::"v"(d), "v"(voff), "s"(rs), "s"(soff) : "memory");
}
DI void st64p(v2u d, unsigned voff, rsrc_t rs, unsigned soff) {
  asm volatile("buffer_store_dwordx2 %0, %1, %2, %3 offen" ::"v"(d), "v"(voff), "s"(rs), "s"(soff) : "memory");
}
template <int OFF> DI void dsr128(v4u& d, unsigned addr) { asm volatile("ds_read_b128 %0, %1 offset:%c2" : "=v"(d) : "v"(addr), "i"(OFF) : "memory"); }
template <int OFF> DI void dsr128f(f32x4& d, unsigned addr) { asm volatile("ds_read_b128 %0, %1 offset:%c2" : "=v"(d) : "v"(addr), "i"(OFF) : "memory"); }
template <int R, int OFF> DI void dsr128a(unsigned addr) { asm volatile("ds_read_b128 a[%c0:%c1], %2 offset:%c3" ::"i"(R), "i"(R + 3), "v"(addr), "i"(OFF) : "memory"); }
template <int OFF> DI v4u tr_piece(unsigned addr) {
  v2u lo, hi;
  asm volatile("ds_read_b64_tr_b16 %0, %2 offset:%c3\n\tds_read_b64_tr_b16 %1, %2 offset:%c4\n\ts_waitcnt lgkmcnt(0)" : "=&v"(lo), "=&v"(hi) : "v"(addr), "i"(OFF), "i"(OFF + 512) : "memory");
  return v4u{lo.x, lo.y, hi.x, hi.y};
}
template <bool Z, int RA> DI void mf16_av(f32x4& acc, const v4u& b) {
  if constexpr (Z) asm volatile("v_mfma_f32_16x16x32_bf16 %0, a[%c1:%c2], %3, 0" : "=&v"(acc) : "i"(RA), "i"(RA + 3), "v"(b));
  else asm volatile("v_mfma_f32_16x16x32_bf16 %0, a[%c1:%c2], %3, %0" : "+v"(acc) : "i"(RA), "i"(RA + 3), "v"(b));
}
template <bool Z, int RB> DI void mf16_va(f32x4& acc, const v4u& a) {
  if constexpr (Z) asm volatile("v_mfma_f32_16x16x32_bf16 %0, %3, a[%c1:%c2], 0" : "=&v"(acc) : "i"(RB), "i"(RB + 3), "v"(a));
  else asm volatile("v_mfma_f32_16x16x32_bf16 %0, %3, a[%c1:%c2], %0" : "+v"(acc) : "i"(RB), "i"(RB + 3), "v"(a));
}
template <bool Z, int RA, int RB> DI void mf16_aa(f32x4& acc) {
  if constexpr (Z) asm volatile("v_mfma_f32_16x16x32_bf16 %0, a[%c1:%c2], a[%c3:%c4], 0" : "=&v"(acc) : "i"(RA), "i"(RA + 3), "i"(RB), "i"(RB + 3));
  else asm volatile("v_mfma_f32_16x16x32_bf16 %0, a[%c1:%c2], a[%c3:%c4], %0" : "+v"(acc) : "i"(RA), "i"(RA + 3), "i"(RB), "i"(RB + 3));
}
template <bool Z> DI void mf16_vv(f32x4& acc, const v4u& a, const v4u& b) {
  if constexpr (Z) asm volatile("s_nop 1\n\tv_mfma_f32_16x16x32_bf16 %0, %1, %2, 0" : "=&v"(acc) : "v"(a), "v"(b));
  else asm volatile("s_nop 1\n\tv_mfma_f32_16x16x32_bf16 %0, %1, %2, %0" : "+v"(acc) : "v"(a), "v"(b));
}
template <bool Z, int RB> DI void mf32_va(f32x16& acc, const v4u& a) {
  if constexpr (Z) asm volatile("v_mfma_f32_32x32x16_bf16 %0, %3, a[%c1:%c2], 0" : "=&v"(acc) : "i"(RB), "i"(RB + 3), "v"(a));
  else asm volatile("v_mfma_f32_32x32x16_bf16 %0, %3, a[%c1:%c2], %0" : "+v"(acc) : "i"(RB), "i"(RB + 3), "v"(a));
}
DI void mf32_vv(f32x16& acc, const v4u& a, const v4u& b) { asm volatile("v_mfma_f32_32x32x16_bf16 %0, %1, %2, %0" : "+v"(acc) : "v"(a), "v"(b)); }
template <int RA, int RB> DI void mf16_lva(f32x4& acc) {
  asm volatile("v_mfma_f32_16x16x32_bf16 %0, v[%c1:%c2], a[%c3:%c4], %0" : "+v"(acc) : "i"(RA), "i"(RA + 3), "i"(RB), "i"(RB + 3));
}
template <int RA, int RB> DI void mf16_lva0(f32x4& acc) {
  asm volatile("v_mfma_f32_16x16x32_bf16 %0, v[%c1:%c2], a[%c3:%c4], 0" : "=&v"(acc) : "i"(RA), "i"(RA + 3), "i"(RB), "i"(RB + 3));
}
template <int R, int IMM> DI void ld_v(unsigned voff, rsrc_t rs, unsigned soff) {
  asm volatile("buffer_load_dwordx4 v[%c0:%c1], %2, %3, %4 offen offset:%c5 sc1" ::"i"(R), "i"(R + 3), "v"(voff), "s"(rs), "s"(soff), "i"(IMM) : "memory");
}
#define MFMA_DRAIN() do { asm volatile("s_nop 15" ::: "memory"); FENCE(); } while (0)
template <class T> DI void pin(T& x) { asm volatile("" : "+v"(x)); }

DI unsigned cvtpk(float lo, float hi) { f32x2 v = {lo, hi}; bf16x2 b = __builtin_convertvector(v, bf16x2); return __builtin_bit_cast(unsigned, b); }
DI float asf(unsigned u) { return __builtin_bit_cast(float, u); }
DI float bflo(unsigned u) { return __builtin_bit_cast(float, u << 16); }
DI float bfhi(unsigned u) { return __builtin_bit_cast(float, u & 0xffff0000u); }
DI float sum8(v4u v, float acc) {
  acc += (bflo(v.x) + bfhi(v.x)) + (bflo(v.y) + bfhi(v.y));
  acc += (bflo(v.z) + bfhi(v.z)) + (bflo(v.w) + bfhi(v.w));
  return acc;
}
DI float phi1(float t1) {
  const float e = __builtin_amdgcn_fmed3f(__builtin_amdgcn_exp2f(fmaf(t1, 1.44269504f, -1.44269504e-6f)), 0.f, 1.f);
  return fmaxf(t1, 1e-6f) + e;
}
template <int R, int OFF, int P4 = 512> DI void tr_a(unsigned addr) {
  asm volatile("ds_read_b64_tr_b16 a[%c0:%c1], %4 offset:%c5\n\tds_read_b64_tr_b16 a[%c2:%c3], %4 offset:%c6" ::"i"(R), "i"(R + 1), "i"(R + 2), "i"(R + 3), "v"(addr), "i"(OFF), "i"(OFF + P4) : "memory");
}
template <int R> DI void st_a(unsigned voff, rsrc_t rs, unsigned soff) {
  asm volatile("buffer_store_dwordx4 a[%c0:%c1], %2, %3, %4 offen sc1" ::"i"(R), "i"(R + 3), "v"(voff), "s"(rs), "s"(soff) : "memory");
}
template <int OFF> DI void dsw128f(const f32x4& d, unsigned addr) { asm volatile("ds_write_b128 %0, %1 offset:%c2" ::"v"(addr), "v"(d), "i"(OFF) : "memory"); }
DI float v_fma(float a, float b, float c) { float r; asm volatile("v_fma_f32 %0, %1, %2, %3" : "=v"(r) : "v"(a), "v"(b), "v"(c)); return r; }
DI float v_mul(float a, float b) { float r; asm volatile("v_mul_f32 %0, %1, %2" : "=v"(r) : "v"(a), "v"(b)); return r; }
DI float v_exp(float a) { float r; asm volatile("v_exp_f32 %0, %1" : "=v"(r) : "v"(a)); return r; }
DI float v_rcp(float a) { float r; asm volatile("v_rcp_f32 %0, %1" : "=v"(r) : "v"(a)); return r; }
DI unsigned v_cvtpk(float a, float b) { unsigned r; asm volatile("v_cvt_pk_bf16_f32 %0, %1, %2" : "=v"(r) : "v"(a), "v"(b)); return r; }
DI rsrc_t mk(const void* p, unsigned bytes) { return __builtin_amdgcn_make_buffer_rsrc((void*)p, 0, (int)bytes, 0x00020000); }

#define ACLOB "a0","a1","a2","a3","a4","a5","a6","a7","a8","a9","a10","a11","a12","a13","a14","a15","a16","a17","a18","a19","a20","a21","a22","a23","a24","a25","a26","a27","a28","a29","a30","a31","a32","a33","a34","a35","a36","a37","a38","a39","a40","a41","a42","a43","a44","a45","a46","a47","a48","a49","a50","a51","a52","a53","a54","a55","a56","a57","a58","a59","a60","a61","a62","a63","a64","a65","a66","a67","a68","a69","a70","a71","a72","a73","a74","a75","a76","a77","a78","a79","a80","a81","a82","a83","a84","a85","a86","a87","a88","a89","a90","a91","a92","a93","a94","a95","a96","a97","a98","a99","a100","a101","a102","a103","a104","a105","a106","a107","a108","a109","a110","a111","a112","a113","a114","a115","a116","a117","a118","a119","a120","a121","a122","a123","a124","a125","a126","a127","a128","a129","a130","a131","a132","a133","a134","a135","a136","a137","a138","a139","a140","a141","a142","a143","a144","a145","a146","a147","a148","a149","a150","a151","a152","a153","a154","a155","a156","a157","a158","a159","a160","a161","a162","a163","a164","a165","a166","a167","a168","a169","a170","a171","a172","a173","a174","a175","a176","a177","a178","a179","a180","a181","a182","a183","a184","a185","a186","a187","a188","a189","a190","a191","a192","a193","a194","a195","a196","a197","a198","a199","a200","a201","a202","a203","a204","a205","a206","a207","a208","a209","a210","a211","a212","a213","a214","a215","a216","a217","a218","a219","a220","a221","a222","a223","a224","a225","a226","a227","a228","a229","a230","a231","a232","a233","a234","a235","a236","a237","a238","a239","a240","a241","a242","a243","a244","a245","a246","a247","a248","a249","a250","a251","a252","a253","a254","a255"

DI void poll_w0(rsrc_t ws, unsigned voff, unsigned tag, int& budget, int w) {
  unsigned f;
  asm volatile("s_cmp_lg_u32 %5, 0\n\ts_cbranch_scc1 2f\n"
               "1:\n\tbuffer_load_dword %0, %2, %3, 0 offen sc1\n\ts_waitcnt vmcnt(0)\n\tv_cmp_ne_u32 vcc, %4, %0\n\ts_nop 1\n\ts_cbranch_vccz 2f\n\t"
               "s_sub_u32 %1, %1, 1\n\ts_cmp_gt_i32 %1, 0\n\ts_cbranch_scc1 1b\n2:"
               : "=&v"(f), "+s"(budget) : "v"(voff), "s"(ws), "s"(tag), "s"(w) : "vcc", "scc", "memory");
}
template <int N> DI void poll_chk(unsigned f, rsrc_t ws, unsigned voff, unsigned soff, unsigned tag, int& budget) {
  asm volatile("s_waitcnt vmcnt(%c6)\n"
               "1:\n\tv_cmp_ne_u32 vcc, %4, %0\n\ts_nop 1\n\ts_cbranch_vccz 2f\n\t"
               "s_sub_u32 %1, %1, 1\n\ts_cmp_gt_i32 %1, 0\n\ts_cbranch_scc0 2f\n\t"
               "buffer_load_dword %0, %2, %3, %5 offen sc1\n\ts_waitcnt vmcnt(0)\n\ts_branch 1b\n2:"
               : "+v"(f), "+s"(budget) : "v"(voff), "s"(ws), "s"(tag), "s"(soff), "i"(N) : "vcc", "scc", "memory");
}

#define STAMP(i) asm volatile("; MARK %0" ::"i"(i))

constexpr int slot_of(int u) { return (u % 7) * (int)SLOT; }
template <int U, int FR, int NEXT> DI void unit_dma(rsrc_t ws, unsigned lane16, unsigned lane16b, unsigned soff, unsigned ldsw) {
  if constexpr (FR < 3) dma<FR * 1024, true, -1>(lane16, ws, soff, ldsw);
  else if constexpr (FR == 3) dma<3072, true, slot_of(U) + 1024>(lane16, ws, soff, ldsw);
  else dma<3072, true, NEXT>(lane16b, ws, soff, ldsw);
}

#define WL(n) __builtin_amdgcn_s_waitcnt(0xC07F | ((n) << 8))
template <int R, int OFF> DI void dsr64a(unsigned addr) { asm volatile("ds_read_b64 a[%c0:%c1], %2 offset:%c3" ::"i"(R), "i"(R + 1), "v"(addr), "i"(OFF) : "memory"); }
template <int OFF> DI void dsr32f(float& d, unsigned addr) { asm volatile("ds_read_b32 %0, %1 offset:%c2" : "=v"(d) : "v"(addr), "i"(OFF) : "memory"); }
constexpr unsigned long long kv_mask(int a, int bb) {
  unsigned long long m = 0;
  for (int l = 0; l < 64; ++l) {
    const int li = l & 15, dq = 4 * a + (l >> 4), hd = dq >= 5, eg = bb < 2 ? 16 * bb + li : 24 + li;
    const bool ok = dq < 10 && (bb == 3 ? li == 0 : (bb < 2 || li >= 8) && eg / 20 == hd);
    if (ok) m |= 1ull << l;
  }
  return m;
}
template <int A, int BB, int OFF> DI void dsw_kv(const f32x4& d, unsigned addr) {
  asm volatile("s_mov_b32 exec_lo, %c2\n\ts_mov_b32 exec_hi, %c3\n\tds_write_b128 %0, %1 offset:%c4" ::"v"(addr), "v"(d), "i"((int)(unsigned)kv_mask(A, BB)), "i"((int)(unsigned)(kv_mask(A, BB) >> 32)), "i"(OFF) : "memory");
}
DI float sel4(const f32x4& v, int i) { const float a = (i & 1) ? v[1] : v[0], b = (i & 1) ? v[3] : v[2]; return (i & 2) ? b : a; }
DI float xsum32(float s) {
  const unsigned u = __builtin_bit_cast(unsigned, s);
  const auto r = __builtin_amdgcn_permlane32_swap(u, u, false, false);
  return asf(r[0]) + asf(r[1]);
}
constexpr int VRES = 176;
constexpr int P3_HUP = 6;
constexpr int P3_LASTU = 24;
constexpr int p3_dma_start(int nu) { int d = nu - 15; int a = 85 + 15 * d, b = 36 + 20 * d; return a > b ? a : b; }
constexpr int p3_dma_at(int g) {
  for (int nu = 14; nu <= P3_LASTU; ++nu) { int s = p3_dma_start(nu); if (g >= s && g < s + 15 && (g - s) % 3 == 0) return nu * 8 + (g - s) / 3; }
  return -1;
}
constexpr int p3_w4a_at(int g) { return (g >= 72 && (g - 72) % 6 == 0 && (g - 72) / 6 < 30) ? (g - 72) / 6 : -1; }
constexpr int p3_epi_start(int p) { return p < 5 ? 123 + 24 * p : 246; }
constexpr int P3_EPI_ST = 26;
constexpr int p3_st_step(int p) { return p < 5 ? p3_epi_start(p) + P3_EPI_ST : p3_epi_start(5) + P3_EPI_ST / 2; }
constexpr int P3_FL0 = 196, P3_FL1 = 236, P3_FL2 = 276;
constexpr int P3_POLL_A0 = 232, P3_CHK_A0 = 246, P3_POLL_A1 = 278, P3_AL0 = 247;
constexpr bool p3_hload(int g) { return (g & 1) && (g >> 1) + P3_HUP < 40; }
constexpr bool p3_aload(int g) { return g >= P3_AL0 && g < P3_AL0 + 32 && (g - P3_AL0) % 2 == 0; }
constexpr bool p3_store(int g) { for (int p = 0; p < 6; ++p) if (g == p3_st_step(p)) return true; return false; }
constexpr int p3_nvm(int g) { return p3_hload(g) + (p3_dma_at(g) >= 0) + (p3_w4a_at(g) >= 0) + p3_aload(g) + p3_store(g) + (g == P3_POLL_A0) + (g == P3_POLL_A1) + (g == P3_FL0) + (g == P3_FL1) + (g == P3_FL2); }
constexpr int p3_cnt(int gx, int g) { int n = 0; for (int k = gx + 1; k < g; ++k) n += p3_nvm(k); return n > 63 ? 63 : n; }
DI void split3(float x, unsigned& a, unsigned& b, unsigned& c) {
  a = __builtin_bit_cast(unsigned, x) & 0xffff0000u;
  const float r1 = x - asf(a);
  b = __builtin_bit_cast(unsigned, r1) & 0xffff0000u;
  c = __builtin_bit_cast(unsigned, r1 - asf(b)) & 0xffff0000u;
}
constexpr int areg(int u, int mm) { return (u == 0 ? 240 : u < 11 ? 16 * (u - 1) : 16 * (u - 11)) + 4 * mm; }
typedef unsigned v16u __attribute__((ext_vector_type(16)));
DI const void* ptr2(unsigned lo, unsigned hi) { return (const void*)(((unsigned long long)hi << 32) | lo); }

__global__ void __launch_bounds__(256, 1) __attribute__((amdgpu_num_vgpr(VRES)))
fused_block(const void* __restrict__ xp_, const void* __restrict__ wqkvp_, const void* __restrict__ wop_, const void* __restrict__ wgp_,
            const void* __restrict__ wup_, const void* __restrict__ wdp_, void* __restrict__ outp_, char* __restrict__ wsp_) {
  extern __shared__ __attribute__((aligned(16))) char smem_[];
  LAS char* L = (LAS char*)smem_;
  unsigned long long pc0; unsigned codelen; v16u ka;
  asm volatile("s_load_dwordx16 %2, %3, 0\nfbk_begin:\n\ts_getpc_b64 %0\n\ts_mov_b32 %1, fbk_end-fbk_begin"
               : "=&s"(pc0), "=&s"(codelen), "=&s"(ka) : "s"(__builtin_amdgcn_kernarg_segment_ptr()) : ACLOB, "v255");
  FENCE();
  const int tid = threadIdx.x, lane = tid & 63;
  const int w = __builtin_amdgcn_readfirstlane(tid >> 6);
  const int b = blockIdx.x, xcd = b & 7, bi = b >> 3;
  const int r = (xcd >> 1) * 4 + (bi & 3), c = (xcd & 1) * 8 + (bi >> 2);
  const int j = (c >> 3) * 4 + (c & 3), hf = (c >> 2) & 1;
  const int li = lane & 15, lg = lane >> 4;
  const unsigned lane16 = lane * 16, lane16b = lane16 + 1024;
  const unsigned ldsw = w * 5120;
  int budget = 200000;
  const int drow = lane >> 3, dch = lane & 7, tq = li >> 2, tp = li & 3;
  const int pp = 2 * r + hf;
  const unsigned xst = L_XST + w * 20480;
  unsigned vo1, vo1b, vo2, vct;
  {
    int col = 640 + 8 * li;
    col = li >= 5 ? 1232 + 8 * li : col; col = li == 5 ? 1312 : col; col = li >= 10 ? 8 * li - 80 : col; col = li == 15 ? 32 : col;
    vo1 = ((40 * r + 12 * w + lg) * 1920 + 40 * c + col) * 2;
    vo1b = w == 3 ? OOB : vo1;
    pin(vo1); pin(vo1b);
  }
  set_m0<SLOT>(3072 * w);
  asm volatile("s_waitcnt lgkmcnt(0)" : "+s"(ka));
  char* const wsp = (char*)ptr2(ka[14], ka[15]);
  const rsrc_t ws = mk(wsp, WS_END);
  const rsrc_t rx = mk(ptr2(ka[0], ka[1]), 2048 * 640 * 2);
  STAMP(0);
  unsigned epoch, cpf, xpf;
  {
    const rsrc_t rw = mk(ptr2(ka[2], ka[3]), 640 * 1920 * 2);
    dma<0, false, -1>(vo1, rw, 0, 0);
    dma<1024, false, -1>(vo1b, rw, 4 * 3840 - 1024, 0);
    dma<2048, false, 2 * SLOT>(vo1b, rw, 8 * 3840 - 2048, ldsw);
    const int oc = 32 * (w < 3 ? w : dch >> 1) + pp, n = oc >> 2, g = oc & 3;
    const int ra = 40 * n + 8 * g, rb = 160 * (n - 16) + 40 * g + 32;
    vo2 = ((n < 16 ? ra : rb) + drow) * 1280 + 160 * j + (w < 3 ? dch * 16 : 128 + (dch & 1) * 16);
    vo2 = oc < 80 ? vo2 : OOB;
    vct = tid < 16 ? (unsigned)(bi * 16 + tid) * 128 : OOB;
    const rsrc_t rw2 = mk((const char*)ptr2(ka[4], ka[5]) - 3072, 640 * 640 * 2 + 3072);
    dma<3072, false, 0>(vo2, rw2, 0, xst);
    unsigned z = 0; ld32c<0>(epoch, z, ws, 0);
    { unsigned tv = (tid >> 3) * 1280 + (4 + dch) * 128; tv = (tid < 128 && dch < 6) ? tv : OOB;
      asm volatile("buffer_load_dword %0, %1, %2, %3 offen" : "=v"(xpf) : "v"(tv), "s"(rx), "s"((128 * r + 16 * (c & 7)) * 1280) : "memory"); }
    const rsrc_t rc = mk((const void*)pc0, codelen);
    asm volatile("buffer_load_dword %0, %1, %2, 0 offen" : "=v"(cpf) : "v"(vct), "s"(rc) : "memory");
  }
  unsigned xv[4];
  SF(4, pc) xv[pc] = drow * 1280 + ((dch ^ (4 * (pc & 1) + lg)) * 16) + pc * (10240 - 1024); SE
  const unsigned xrow = (128 * r + 32 * w) * 1280;
  auto x_piece = [&](auto ch_, auto pc_) __attribute__((always_inline)) {
    constexpr int ch = decltype(ch_)::value, pc = decltype(pc_)::value;
    dma<pc * 1024, false, (pc == 3 ? ((ch + 1) % 5) * 4096 : -1)>(xv[pc], rx, xrow + ch * 128, xst);
  };
  auto x_chunk = [&](auto ch_) __attribute__((always_inline)) { SF(4, pc) x_piece(ch_, IC<pc>{}); SE NOP1(); };
  STAMP(21);
  x_chunk(IC<0>{}); x_chunk(IC<1>{});
  const unsigned trY = tq * 128 + lg * 32 + tp * 8, trX = tq * 128 + (lg >> 1) * 64 + tp * 16 + (lg & 1) * 8;
  unsigned dst1[3], d2, ta, tb, dc; v4u sel;
  {
    const unsigned s1 = (li == (lg & 1)) ? 0x3f803f80u : 0u;
    sel = v4u{s1, s1, s1, s1};
    const int cg = 2 * w + (lg & 1);
    SF(3, k)
      const int ol = 2 * k + (lg >> 1), oc = 5 * r + ol, ks = oc >> 2;
      const int fr = cg < 5 ? ks * 5 + cg : 95 + ks * 3 + cg;
      dst1[k] = ol < 5 ? (unsigned)(fr * 1024 + (oc & 3) * 256 + li * 16) : OOB;
      pin(dst1[k]);
    SE
    const int oc = 32 * (w < 3 ? w : lg) + pp;
    d2 = oc < 80 ? (unsigned)((oc >> 2) * 5120 + (oc & 3) * 256 + (w < 3 ? lg : 4) * 1024 + li * 16) : OOB;
    ta = SLOT + ((lg >> 1) * 8 + tq) * 256 + (2 * w + (lg & 1)) * 32 + tp * 8; tb = 2 * SLOT + ldsw + (w < 3 ? trX : trY); dc = lane < 16 ? (unsigned)(16 * w + li) * 8 : OOB;
    pin(sel); pin(d2); pin(ta); pin(tb); pin(dc);
  }
  x_chunk(IC<2>{});
  STAMP(22);
  unsigned fl_r = tid == 0 ? (unsigned)(4 * r) : OOB, fl_c = tid == 0 ? (unsigned)(4 * c) : OOB;
  pin(fl_r); pin(fl_c);
  unsigned v3, s4[2], d4[2], v4y, d3, d3s, d4y;
  {
    const int ps = 4 * (w >> 1) + (dch >> 1);
    const unsigned fa = (6 * c + ps) * 32, fb = c < 12 ? (96 + c) * 32 : OOB;
    v3 = (ps < 6 ? fa : ps == 6 ? fb : OOB) + (40 * r + drow) * 3456 + 16 * (dch & 1);
    const int pd = 4 * (w >> 1) + lg;
    d3s = pd < 3 ? 2048u : 0u;
    d3 = (pd < 7 ? (unsigned)(pd * (pd < 3 ? 1024 : 40960)) : OOB) + (w & 1) * 256 + li * 16;
    pin(v3); pin(d3); pin(d3s);
  }
  x_chunk(IC<3>{});
  {
    SF(2, k)
      const int oc = 32 * (2 * w + k) + pp, nn = oc >> 2, g = oc & 3;
      const int ft = (w < 3 ? 6 * (nn & 15) + 2 * w : 2 * nn) + (g >> 1);
      const unsigned sv = (16 * ft + 8 * (g & 1)) * 1280 + 160 * j, dv = WS_W4T + j * 286720 + nn * 5120 + g * 256;
      s4[k] = oc < 216 ? sv : OOB; d4[k] = oc < 216 ? dv : OOB;
    SE
    v4y = (dch < 2 ? s4[0] : dch < 4 ? s4[1] : OOB) + drow * 1280 + 128 + (dch & 1) * 16;
    d4y = (lg == 0 ? d4[0] : lg == 1 ? d4[1] : OOB) + 4096 + li * 16;
    pin(v4y); pin(d4y);
  }
  unsigned tag, vtag;
  {
    STAMP(23);
    asm volatile("s_waitcnt vmcnt(16)" : "+v"(epoch), "+v"(cpf), "+v"(xpf)::"memory");
    BARRIER();
    STAMP(24);
    SF(3, k) tr_a<160 + 4 * k, k * 4096, 1024>(ta); SE
    tr_a<172, 3072>(tb);
    tag = __builtin_amdgcn_readfirstlane(epoch) * 1664525u + 1013904223u;
    vtag = tag; pin(vtag);
    f32x4 cs;
    WAITL(2);
    SF(3, k) mf16_va<k == 0, 160 + 4 * k>(cs, sel); SE
    SF(3, k) st_a<160 + 4 * k>(dst1[k], ws, WS_W1T + c * 163840); SE
    WAITL(0);
    st_a<172>(d2, ws, WS_W2T + j * 102400);
    asm volatile("s_nop 3" : "+v"(cs));
    const float c0 = cs[0], c1 = cs[1];
    st64c(v2u{__builtin_bit_cast(unsigned, c0), __builtin_bit_cast(unsigned, c1)}, dc, ws, WS_CS1 + (c * 16 + r) * 512);
  }
  STAMP(25);
  x_chunk(IC<4>{});
  unsigned xa0 = xst + li * 128 + ((lg ^ (li >> 1)) * 16), xa1 = xst + li * 128 + (((4 + lg) ^ (li >> 1)) * 16);
  auto x_read = [&](auto ch_) __attribute__((always_inline)) {
    constexpr int ch = decltype(ch_)::value;
    dsr128a<(4 * ch + 0) * 4, (ch % 5) * 4096>(xa0);
    dsr128a<(4 * ch + 1) * 4, (ch % 5) * 4096 + 2048>(xa0);
    dsr128a<(4 * ch + 2) * 4, (ch % 5) * 4096>(xa1);
    dsr128a<(4 * ch + 3) * 4, (ch % 5) * 4096 + 2048>(xa1);
  };
  v4u ones = {0x3f803f80u, 0x3f803f80u, 0x3f803f80u, 0x3f803f80u};
  unsigned pv2 = flag_off(0, (lane & 16) ? (c ^ 4) : c) + li * 4;
  pin(ones); pin(pv2);
  f32x4 gr[2], sm[2];
  auto x_stat1 = [&](auto ch_, auto q_) __attribute__((always_inline)) {
    constexpr int ch = decltype(ch_)::value, q = decltype(q_)::value;
    mf16_aa<(ch == 0 && q < 2), (4 * ch + q) * 4, (4 * ch + q) * 4>(gr[q & 1]);
    mf16_va<(ch == 0 && q < 2), (4 * ch + q) * 4>(sm[q & 1], ones);
  };
  const unsigned w1src = WS_W1T + c * 163840 + ldsw, w3src = WS_W3T + c * 286720 + ldsw;
  unsigned pf;
  auto x_step = [&](auto k_) __attribute__((always_inline)) {
    constexpr int k = decltype(k_)::value;
    WAITL(4);
    SF(4, q) x_stat1(IC<k>{}, IC<q>{}); if constexpr (k < 5) x_piece(IC<5 + k>{}, IC<q>{}); SE
  };
  WAITV(17); x_read(IC<0>{}); x_read(IC<1>{});
  x_step(IC<0>{});
  STAMP(26);
  WAITV(8);
  BARRIER();
  st32c(vtag, fl_r, ws, flag_off(F_W1, c));
  STAMP(1);
  x_read(IC<2>{}); x_step(IC<1>{});
  x_read(IC<3>{}); x_step(IC<2>{});
  WAITV(13); x_read(IC<4>{}); x_step(IC<3>{});
  WAITV(13); x_read(IC<5>{}); x_step(IC<4>{});
  ld32c<0>(pf, pv2, ws, F_W1 * 4096);
  {
    const v4u z4 = {0u, 0u, 0u, 0u};
    *(LAS v4u*)(L + L_KVT + tid * 16) = z4;
    if (tid < 16) *(LAS v4u*)(L + L_KVT + 4096 + tid * 16) = z4;
  }
  STAMP(2);
  WAITV(13); x_read(IC<6>{}); x_step(IC<5>{});
  WAITV(9); x_read(IC<7>{});
  set_m0<0>(ldsw);
  x_step(IC<6>{});
  WAITV(5); x_read(IC<8>{}); x_step(IC<7>{});
  poll_chk<0>(pf, ws, pv2, F_W1 * 4096, tag, budget);
  STAMP(3);
  x_read(IC<9>{});
  SF(5, f) unit_dma<0, f, slot_of(1)>(ws, lane16, lane16b, w1src, ldsw); if constexpr (f >= 3) NOP1(); SE
  WAITL(0);
  SF(2, k) SF(4, q)
    x_stat1(IC<8 + k>{}, IC<q>{});
    constexpr int i = 4 * k + q;
    if constexpr (i < 5) unit_dma<1, i, slot_of(2)>(ws, lane16, lane16b, w1src + SLOT, ldsw);
    else unit_dma<2, i - 5, -1>(ws, lane16, lane16b, w1src + 2 * SLOT, ldsw);
  SE SE
  unit_dma<2, 3, -1>(ws, lane16, lane16b, w1src + 2 * SLOT, ldsw); NOP1();
  unit_dma<2, 4, slot_of(3)>(ws, lane16, lane16b, w1src + 2 * SLOT, ldsw);
  const unsigned lb0 = lane16, lb1 = lane16 + 3 * SLOT, lb2 = lane16 + 6 * SLOT;
  auto rd = [&](auto fr_, v4u& d) __attribute__((always_inline)) {
    constexpr int fr = decltype(fr_)::value, sl = (fr / 20) % 7, off = (sl % 3) * SLOT + (fr % 20) * 1024;
    dsr128<off>(d, sl < 3 ? lb0 : sl < 6 ? lb1 : lb2);
  };
  f32x4 akv[5][2], aq[3][2], enb[2], era[2];
  float ecs[5];
  const unsigned v4x = drow * 1280 + dch * 16, d4x = lg * 1024 + li * 16;
  const rsrc_t rw3 = mk((w & 1) ? ptr2(ka[8], ka[9]) : ptr2(ka[6], ka[7]), 640 * 1728 * 2);
  const rsrc_t rw4 = mk((const char*)ptr2(ka[10], ka[11]) - 2048, 1728 * 640 * 2 + 2048);
  {
    v4u bf[8];
    unsigned cs1p[8];
    unsigned vc1 = (lane >> 5) * 4096 + (32 * w + (lane & 31)) * 4;
    unsigned ea = L_MEAN + (32 * w + 4 * lg) * 4, ec = L_CS1 + li * 4;
    pin(vc1); pin(ea); pin(ec);
    WAITV(10);
    BARRIER();
    STAMP(4);
    SF(6, f) rd(IC<f>{}, bf[f]); SE
    SF(100, f)
      constexpr int ks = f / 5, t = f % 5;
      if constexpr (f % 20 == 14 && f < 80) { WAITV(f == 14 ? 14 : f == 34 ? 19 : f == 54 ? 16 : 15); BARRIER(); }
      if constexpr (!(f & 1)) WL(f + 6 <= 100 ? 4 : f == 96 ? 6 : 9);
      mf16_av<ks == 0, (2 * ks) * 4>(akv[t][0], bf[f & 7]);
      if constexpr (f + 6 < 100) rd(IC<f + 6>{}, bf[(f + 6) & 7]);
      mf16_av<ks == 0, (2 * ks + 1) * 4>(akv[t][1], bf[f & 7]);
      if constexpr (f % 3 == 1 && f < 75) { constexpr int i = f / 3; unit_dma<3 + i / 5, i % 5, slot_of(4 + i / 5)>(ws, lane16, lane16b, w1src + (3 + i / 5) * SLOT, ldsw); }
      if constexpr (f >= 76 && f % 3 == 1) {
        constexpr int k = (f - 76) / 3;
        if constexpr (k < 5) dma<(k < 4 ? k : 3) * 1024, false, (k == 3 ? (int)SLOT + 1024 : k == 4 ? 2 * (int)SLOT : -1)>(v3, rw3, k * 8 * 3456 - (k < 4 ? k : 3) * 1024, ldsw);
        else if constexpr (k == 5) dma<0, false, -1>(v4x, rw4, s4[0] + 2048, 0);
        else if constexpr (k == 6) dma<1024, false, -1>(v4x, rw4, s4[1] + 1024, 0);
        else dma<2048, false, slot_of(8)>(v4y, rw4, 0, ldsw);
      }
      if constexpr (f % 3 == 2 && f < 24) ld32c<(f / 3) * 512>(cs1p[f / 3], vc1, ws, WS_CS1 + c * 8192);
      if constexpr (f == 27) {
    float sta[2], stb[2];
    SF(2, m)
      pin(gr[m]); pin(sm[m]);
      const float sq = sel4(gr[m], tp);
      const float mean = sm[m][0] * (1.f / 640.f);
      const float rs = __builtin_amdgcn_rsqf(fmaxf(sq * (1.f / 640.f) - mean * mean, 0.f) + 1e-5f);
      stb[m] = -mean * rs; sta[m] = rs;
    SE
    if (lg == tq) {
      SF(2, m) *(LAS float*)(L + L_MEAN + (32 * w + 16 * m + li) * 4) = stb[m]; *(LAS float*)(L + L_RSTD + (32 * w + 16 * m + li) * 4) = sta[m]; SE
    }
      }
      if constexpr (f == 94) { dsr128f<0>(enb[0], ea); dsr128f<512>(era[0], ea); }
      if constexpr (f == 95) { dsr128f<64>(enb[1], ea); dsr128f<576>(era[1], ea); }
      if constexpr (f == 96) { dsr32f<0>(ecs[0], ec); dsr32f<64>(ecs[1], ec); dsr32f<128>(ecs[2], ec); }
      if constexpr (f == 97) { dsr32f<192>(ecs[3], ec); dsr32f<256>(ecs[4], ec); }
      if constexpr (f == 56) {
        WAITV(11);
        SF(8, k) pin(cs1p[k]); SE
        const float s = xsum32(((asf(cs1p[0]) + asf(cs1p[1])) + (asf(cs1p[2]) + asf(cs1p[3]))) + ((asf(cs1p[4]) + asf(cs1p[5])) + (asf(cs1p[6]) + asf(cs1p[7]))));
        *(LAS float*)(L + L_CS1 + (32 * w + (lane & 1) * 16 + ((lane & 31) >> 1)) * 4) = s;
      }
    SE
  }
  STAMP(5);
  const unsigned w2src = WS_W2T + j * 102400 + w * 25600;
  auto ldw2 = [&](auto i_) __attribute__((always_inline)) {
    constexpr int i = decltype(i_)::value;
    ld_a<(i < 24 ? 160 + 4 * i : 0), (i & 3) * 1024>(lane16, ws, w2src + (i >> 2) * 4096);
  };
  {
    v4u fr[5];
    unsigned ib = L_RED + w * 3360 + (5 * li + lg) * 16, iz = L_RED + w * 3360 + (lg ? 204 + lg : 104) * 16;
    pin(ib); pin(iz);
    asm volatile("s_waitcnt lgkmcnt(0)\n\ts_nop 5" : "+v"(enb[0]), "+v"(enb[1]), "+v"(era[0]), "+v"(era[1]), "+v"(ecs[0]), "+v"(ecs[1]), "+v"(ecs[2]), "+v"(ecs[3]), "+v"(ecs[4])::"memory");
    FENCE();
    SF(5, t)
      float v[2][4];
      const float eps = t < 2 ? 1e-6f : (t == 2 && li < 8) ? 1e-6f : 0.f;
      SF(2, m)
        if constexpr (t < 3) ldw2(IC<2 * t + m>{});
        pin(akv[t][m]);
        SF(4, e)
          float x = fmaf(akv[t][m][e], era[m][e], fmaf(enb[m][e], ecs[t], eps));
          if constexpr (t < 2) x = phi1(x);
          if constexpr (t == 2) { const float y = phi1(x); x = li < 8 ? y : x; }
          v[m][e] = x;
        SE
        fr[t][2 * m] = cvtpk(v[m][0], v[m][1]); fr[t][2 * m + 1] = cvtpk(v[m][2], v[m][3]);
      SE
      if constexpr (t < 3) pin(fr[t]);
    SE
    f32x4 dd[10];
    asm volatile("s_nop 1\n\t"
                 "v_mfma_f32_16x16x32_bf16 %7, %10, %15, 0\n\tv_mfma_f32_16x16x32_bf16 %8, %11, %15, 0\n\tv_mfma_f32_16x16x32_bf16 %9, %12, %15, 0\n\t"
                 "v_mfma_f32_16x16x32_bf16 %4, %11, %12, 0\n\tv_mfma_f32_16x16x32_bf16 %6, %12, %12, 0\n\t"
                 "v_mfma_f32_16x16x32_bf16 %0, %10, %13, 0\n\tv_mfma_f32_16x16x32_bf16 %2, %11, %13, 0\n\t"
                 "v_mfma_f32_16x16x32_bf16 %1, %10, %14, 0\n\tv_mfma_f32_16x16x32_bf16 %3, %11, %14, 0\n\tv_mfma_f32_16x16x32_bf16 %5, %12, %14, 0"
                 : "=&v"(dd[0]), "=&v"(dd[1]), "=&v"(dd[2]), "=&v"(dd[3]), "=&v"(dd[4]), "=&v"(dd[5]), "=&v"(dd[6]), "=&v"(dd[7]), "=&v"(dd[8]), "=&v"(dd[9])
                 : "v"(fr[0]), "v"(fr[1]), "v"(fr[2]), "v"(fr[3]), "v"(fr[4]), "v"(ones));
    FENCE();
    dsw_kv<0, 3, 100 * 16>(dd[7], ib); dsw_kv<1, 3, 0>(dd[8], iz); dsw_kv<2, 3, 208 * 16>(dd[9], ib);
    dsw_kv<1, 2, 124 * 16>(dd[4], ib); dsw_kv<2, 2, 128 * 16>(dd[6], ib);
    dsw_kv<0, 0, 0>(dd[0], ib); dsw_kv<1, 0, 4 * 16>(dd[2], ib);
    dsw_kv<0, 1, 80 * 16>(dd[1], ib); dsw_kv<1, 1, 84 * 16>(dd[3], ib); dsw_kv<2, 1, 88 * 16>(dd[5], ib);
    asm volatile("s_mov_b64 exec, -1" ::: "memory");
    FENCE();
    ldw2(IC<6>{}); ldw2(IC<7>{});
    unsigned ra = L_RED + tid * 16, gv0 = tid < 210 ? (unsigned)tid * 16 : OOB;
    pin(ra); pin(gv0);
    WAITV(16);
    BARRIER_L();
    f32x4 p[4];
    SF(4, k) dsr128f<k * 3360>(p[k], ra); SE
    asm volatile("s_waitcnt lgkmcnt(0)" : "+v"(p[0]), "+v"(p[1]), "+v"(p[2]), "+v"(p[3])::"memory");
    st128c(__builtin_bit_cast(v4u, (p[0] + p[1]) + (p[2] + p[3])), gv0, ws, WS_KVP + (c * 16 + r) * 4096);
  }
  STAMP(6);
  v4u pq[2][2];
  v4u xr[2]; v2u xr4;
  unsigned pvk = flag_off(F_KV, c) + li * 4;
  {
    v4u bf[8];
    SF(6, f) rd(IC<100 + f>{}, bf[f]); SE
    unsigned xo = ((128 * r + 64 * hf + 16 * w + li) * 640 + 80 * j) * 2;
    unsigned xo16 = xo + lg * 16, xo8 = xo + lg * 8;
    pin(xo16); pin(xo8); pin(pvk);
    f32x4 cs3;
    unsigned t3 = SLOT + ldsw + tq * 128 + lg * 32 + (2 * (tp & 1) + (tp >> 1)) * 8, t4x = 2 * SLOT + ldsw + trX, t4y = 2 * SLOT + ldsw + trY;
    unsigned dc3 = lane < 16 ? (unsigned)(((w & 1) * 16 + li) * 32 + (w >> 1) * 16) : OOB;
    const unsigned s13 = li == lg ? 0x3f803f80u : 0u;
    v4u sel3 = {s13, s13, s13, s13};
    pin(t3); pin(t4x); pin(t4y); pin(dc3); pin(sel3);
    WAITV(9);
    SF(60, f)
      constexpr int ks = f / 3, t = f % 3;
      if constexpr (f == 16) { STAMP(30); WAITV(5); BARRIER(); st32c(vtag, fl_r, ws, flag_off(F_KV, c)); STAMP(31); }
      if constexpr (!(f & 1)) WL(f + 6 <= 60 ? 4 : 58 - f);
      mf16_va<ks == 0, (2 * ks) * 4>(aq[t][0], bf[f & 7]);
      if constexpr (f < 16 && !(f & 1)) {
        constexpr int k = f / 2;
        if constexpr (k < 5) tr_a<224 + 4 * k, k * 1024>(t3);
        else tr_a<224 + 4 * k, (k - 5) * 1024>(k < 7 ? t4x : t4y);
      }
      if constexpr (f + 6 < 60) rd(IC<100 + f + 6>{}, bf[(f + 6) & 7]);
      mf16_va<ks == 0, (2 * ks + 1) * 4>(aq[t][1], bf[f & 7]);
      if constexpr (f >= 7 && f < 23 && (f & 1)) {
        constexpr int k = (f - 7) / 2;
        if constexpr (k < 5) { st_a<224 + 4 * k>(d3 + d3s * ((5 * r + k) >> 1), ws, WS_W3T + c * 286720 + (5 * r + k) * 512); mf16_va<k == 0, 224 + 4 * k>(cs3, sel3); }
        else if constexpr (k < 7) st_a<224 + 4 * k>(d4x, ws, d4[(k - 5) & 1]);
        else st_a<224 + 4 * k>(d4y, ws, 0);
      }
      if constexpr (f == 24) {
        pin(cs3);
        st128c(__builtin_bit_cast(v4u, cs3), dc3, ws, WS_CS3 + (c * 16 + r) * 1024);
      }
      if constexpr (f >= 26 && f % 4 == 2 && f < 58) ldw2(IC<8 + (f - 26) / 4>{});
      if constexpr (f == 32) ld128c<0>(xr[0], xo16, rx, 0);
      if constexpr (f == 41) ld128c<64>(xr[1], xo16, rx, 0);
      if constexpr (f == 50) ld64c<128>(xr4, xo8, rx, 0);
      if constexpr (f == 58) ld32c<0>(pf, pvk, ws, 0);
    SE
  }
  STAMP(7);
  const unsigned w4src = WS_W4T + j * 286720 + ldsw;
  auto pfx = [&](auto i_) __attribute__((always_inline)) {
    constexpr int i = decltype(i_)::value;
    if constexpr (i < 30) { unit_dma<8 + i / 5, i % 5, slot_of(9 + i / 5)>(ws, lane16, lane16b, w3src + (i / 5) * SLOT, ldsw); if constexpr (i % 5 >= 3) NOP1(); }
    else { constexpr int k = i - 30; ld_v<VRES + 4 * k, (k % 5 < 4 ? k % 5 : 3) * 1024>(k % 5 < 4 ? lane16 : lane16b, ws, w4src + (k / 5) * SLOT); }
  };
  f32x4 acc2[4][5];
  {
    float nb[2], ra[2]; f32x4 cs[3];
    SF(2, m) nb[m] = *(LAS float*)(L + L_MEAN + (32 * w + 16 * m + li) * 4); ra[m] = *(LAS float*)(L + L_RSTD + (32 * w + 16 * m + li) * 4); SE
    SF(3, t) cs[t] = *(LAS f32x4*)(L + L_CS1 + (80 + 16 * t + 4 * lg) * 4); SE
    asm volatile("s_nop 7" ::: "memory");
    FENCE();
    unsigned pk[2][3][2];
    auto qepi = [&](auto m_, auto t_) __attribute__((always_inline)) {
      constexpr int m = decltype(m_)::value, t = decltype(t_)::value;
      pin(aq[t][m]);
      float v[4];
      SF(4, e) v[e] = phi1(fmaf(aq[t][m][e], ra[m], fmaf(nb[m], cs[t][e], 1e-6f))); SE
      pk[m][t][0] = cvtpk(v[0], v[1]); pk[m][t][1] = cvtpk(v[2], v[3]);
      pin(pk[m][t][0]); pin(pk[m][t][1]);
    };
    SF(3, t) qepi(IC<0>{}, IC<t>{}); SE
    unsigned gv = tid < 210 ? (unsigned)tid * 16 : OOB;
    unsigned kw, k0 = L_KVT + (8 * tq + tp) * 96 + lg * 8, k2 = L_KVT + (li >= 8 ? 24 + li : 42) * 96 + lg * 8, k3 = L_KVT + (tp < 2 ? 40 + tp : 42) * 96 + lg * 8;
    {
      const int hd = tid >= 105, u = tid - 105 * hd, e = (u * 205) >> 10, dq = u - 5 * e + 5 * hd;
      kw = L_KVT + (tid >= 210 ? 43 * 96 : (e == 20 ? 40 + hd : e + 20 * hd) * 96 + dq * 8);
    }
    pin(gv); pin(kw); pin(k0); pin(k2); pin(k3);
    poll_chk<0>(pf, ws, pvk, 0, tag, budget);
    pin(xr[0]); pin(xr[1]); pin(xr4);
    STAMP(8);
    v4u g[16];
    SF(16, k)
      ld128c<0>(g[k], gv, ws, WS_KVP + (c * 16 + k) * 4096);
      if constexpr (k == 3 || k == 7 || k == 11) qepi(IC<1>{}, IC<k / 4>{});
    SE
    SF(2, m)
      pq[m][0] = v4u{pk[m][0][0], pk[m][0][1], pk[m][1][0], pk[m][1][1]};
      pq[m][1] = v4u{lg < 2 ? pk[m][2][0] : 0u, lg < 2 ? pk[m][2][1] : 0u, 0u, 0u};
      pin(pq[m][0]); pin(pq[m][1]);
    SE
    asm volatile("v_accvgpr_write_b32 a106, 0\n\tv_accvgpr_write_b32 a107, 0\n\tv_accvgpr_write_b32 a114, 0\n\tv_accvgpr_write_b32 a115, 0\n\t"
                 "v_accvgpr_write_b32 a122, 0\n\tv_accvgpr_write_b32 a123, 0\n\tv_accvgpr_write_b32 a130, 0\n\tv_accvgpr_write_b32 a131, 0");
    STAMP(9);
    f32x4 s;
    SF(4, q)
      ldw2(IC<16 + q>{});
      WAITV(13 - 3 * q);
      SF(4, k) pin(g[4 * q + k]); SE
      const f32x4 t4 = (__builtin_bit_cast(f32x4, g[4 * q]) + __builtin_bit_cast(f32x4, g[4 * q + 1])) + (__builtin_bit_cast(f32x4, g[4 * q + 2]) + __builtin_bit_cast(f32x4, g[4 * q + 3]));
      if constexpr (q == 0) s = t4; else s += t4;
      pin(s);
    SE
    *(LAS v2u*)(L + kw) = v2u{cvtpk(s[0], s[1]), cvtpk(s[2], s[3])};
    BARRIER_L();
    st32c(vtag, fl_r, ws, flag_off(F_W4, c));
    STAMP(10);
    SF(4, t1) constexpr int t = (t1 + 3) & 3; SF(3, p) dsr64a<100 + 8 * t + 2 * p, (t == 1 ? 4 * 96 : 0) + p * 32>(t < 2 ? k0 : t == 2 ? k2 : k3); SE SE
    f32x4 od[2][4];
    SF(4, t1)
      constexpr int t = (t1 + 3) & 3;
      WL(9 - 3 * t1);
      SF(2, m) mf16_av<true, 100 + 8 * t>(od[m][t], pq[m][0]); SE
      SF(2, m) mf16_av<false, 104 + 8 * t>(od[m][t], pq[m][1]); SE
      ldw2(IC<20 + t1>{});
    SE
    asm volatile("s_nop 7");
    FENCE();
    SF(2, m)
      SF(4, t) pin(od[m][t]); SE
      const float r0 = __builtin_amdgcn_rcpf(od[m][3][0] + 1e-6f), r1 = __builtin_amdgcn_rcpf(od[m][3][1] + 1e-6f);
      const float ra = lg < 3 ? r0 : r1, rb = lg < 2 ? r0 : r1;
      const v4u o = {cvtpk(od[m][0][0] * ra, od[m][0][1] * ra), cvtpk(od[m][0][2] * ra, od[m][0][3] * ra),
                     cvtpk(od[m][1][0] * rb, od[m][1][1] * rb), cvtpk(od[m][1][2] * rb, od[m][1][3] * rb)};
      const v2u o2 = {cvtpk(od[m][2][0] * r1, od[m][2][1] * r1), cvtpk(od[m][2][2] * r1, od[m][2][3] * r1)};
      const unsigned ob = WS_O + (8 * r + 2 * w + m) * 20480;
      st128c(o, lane16, ws, ob + c * 1024);
      st64c(o2, lg >= 2 ? (unsigned)(li * 16 + (lg - 2) * 8) : OOB, ws, ob + (16 + (c >> 2)) * 1024 + (c & 3) * 256);
    SE
    ld32c<0>(pf, pv2, ws, F_W4 * 4096);
    ldw2(IC<24>{});
    SF(5, t)
      const unsigned x0 = t < 4 ? xr[(t >> 1) & 1][(t & 1) * 2] : xr4[0], x1 = t < 4 ? xr[(t >> 1) & 1][(t & 1) * 2 + 1] : xr4[1];
      acc2[3][t] = f32x4{bflo(x0), bfhi(x0), bflo(x1), bfhi(x1)};
      pin(acc2[3][t]);
    SE
    WAITV(2);
    BARRIER();
    st32c(vtag, fl_c, ws, flag_off(F_O, r));
    STAMP(11);
    poll_chk<2>(pf, ws, pv2, F_W4 * 4096, tag, budget);
    SF(14, k) pfx(IC<k>{}); SE
    poll_w0(ws, flag_off(F_O, r) + li * 4, tag, budget, w);
    st32c(vtag, (b == 0 && tid == 0) ? 0u : OOB, ws, 0);
    BARRIER();
  }
  STAMP(12);
  const int mt0 = 8 * r + 4 * hf;
  const bool dead = w >= 2;
  auto ldw4a = [&](auto i_) __attribute__((always_inline)) {
    constexpr int i = decltype(i_)::value;
    ld_a<160 + 4 * i, (i % 5 < 4 ? i % 5 : 3) * 1024>(i % 5 < 4 ? lane16 : lane16b, ws, (i >= 15 && dead) ? OOB : w4src + (10 + i / 5) * SLOT);
  };
  v4u hp[2]; v2u hp4;
  const unsigned pb0 = lb0 + ldsw, pb1 = lb1 + ldsw, pb2 = lb2 + ldsw;
  {
    unsigned cs3p[16];
    auto ldo = [&](auto i_) __attribute__((always_inline)) {
      constexpr int i = decltype(i_)::value, mm = i / 5, kk = i % 5;
      ld_a<4 + i * 4, (kk < 4 ? kk : 3) * 1024>(kk < 4 ? lane16 : lane16b, ws, WS_O + (mt0 + ((w + mm + 1) & 3)) * 20480 + ldsw);
    };
    constexpr int UP = 10;
    SF(UP, i) ldo(IC<i>{}); SE
    f32x4 pr[3][5];
    unsigned rb[3];
    SF(3, mm) rb[mm] = slot_of(12 + mm) + ((w + 3 - mm) & 3) * 5120 + lane16; pin(rb[mm]); SE
    SF(20, i)
      constexpr int mm = i / 5, kk = i % 5;
      WAITV(i + UP <= 20 ? UP - 1 : 19 - i + (i - 10));
      if constexpr (i == 17) BARRIER_L();
      SF(5, t)
        constexpr int fb = kk * 5 + t;
        mf16_aa<(kk == 0 && mm < 3), (fb < 24 ? 160 + 4 * fb : 0), 4 + i * 4>(acc2[mm][t]);
        if constexpr (t == 0 && i + UP < 20) ldo(IC<i + UP>{});
        if constexpr (i >= 10 && t == 0) pfx(IC<(i < 16 ? 14 + i - 10 : 30 + i - 16)>{});
        if constexpr (mm > 0 && kk == 1) {
          constexpr int sl = (12 + mm - 1) % 7;
          dsw128f<(sl % 3) * SLOT + t * 1024>(acc2[mm - 1][t], sl < 3 ? pb0 : sl < 6 ? pb1 : pb2);
        }
        if constexpr (i == 17) { dsr128f<t * 1024>(pr[0][t], rb[0]); dsr128f<t * 1024>(pr[1][t], rb[1]); }
        if constexpr (i == 18) dsr128f<t * 1024>(pr[2][t], rb[2]);
        if constexpr (i == 19) { WL(4 - t); pin(pr[0][t]); pin(pr[1][t]); pin(pr[2][t]); pr[0][t] = (pr[0][t] + pr[1][t]) + pr[2][t]; pin(pr[0][t]); }
      SE
    SE
    STAMP(13);
    asm volatile("s_nop 3");
    f32x4 v[5];
    SF(5, t) pin(acc2[3][t]); v[t] = pr[0][t] + acc2[3][t]; SE
    SF(2, k) hp[k] = v4u{cvtpk(v[2 * k][0], v[2 * k][1]), cvtpk(v[2 * k][2], v[2 * k][3]), cvtpk(v[2 * k + 1][0], v[2 * k + 1][1]), cvtpk(v[2 * k + 1][2], v[2 * k + 1][3])}; SE
    hp4 = v2u{cvtpk(v[4][0], v[4][1]), cvtpk(v[4][2], v[4][3])};
    const v4u h4 = {hp4.x, hp4.y, 0u, 0u};
    f32x4 g2, s2;
    mf16_vv<true>(g2, hp[0], hp[0]); mf16_vv<true>(s2, ones, hp[0]);
    mf16_vv<false>(g2, hp[1], hp[1]); mf16_vv<false>(s2, ones, hp[1]);
    mf16_vv<false>(g2, h4, h4); mf16_vv<false>(s2, ones, h4);
    const int mt = mt0 + w;
    const unsigned hb = WS_H + (mt >> 1) * 40960 + 5 * j * 1024 + (mt & 1) * 256;
    const unsigned hv = (lg >> 1) * 1024 + (lg & 1) * 512 + li * 16;
    st128c(hp[0], hv, ws, hb); st128c(hp[1], hv, ws, hb + 2048);
    st64c(hp4, (lg >> 1) * 512 + li * 16 + (lg & 1) * 8, ws, hb + 4096);
    asm volatile("s_nop 1" : "+v"(g2), "+v"(s2));
    const float sq = sel4(g2, tp), sv = s2[0];
    st64c(v2u{__builtin_bit_cast(unsigned, sv), __builtin_bit_cast(unsigned, sq)}, lg == tq ? (unsigned)li * 8 : OOB, ws, WS_HS + (j * 2048 + 16 * mt) * 8);
    SF(16, k) ld32c<(k & 3) * 1024>(cs3p[k], (unsigned)tid * 4, ws, WS_CS3 + c * 16384 + (k >> 2) * 4096); SE
    SF(4, k) pfx(IC<34 + k>{}); SE
    WAITV(20);
    BARRIER();
    st32c(vtag, fl_c, ws, flag_off(F_H, r));
    STAMP(14);
    SF(10, k) pfx(IC<20 + k>{}); SE
    SF(2, k) pfx(IC<38 + k>{}); SE
    {
      WAITV(17);
      SF(16, k) pin(cs3p[k]); SE
      float q4[4];
      SF(4, k) q4[k] = (asf(cs3p[4 * k]) + asf(cs3p[4 * k + 1])) + (asf(cs3p[4 * k + 2]) + asf(cs3p[4 * k + 3])); SE
      unsigned c1, c2, c3;
      split3((q4[0] + q4[1]) + (q4[2] + q4[3]), c1, c2, c3);
      *(LAS v4u*)(L + L_RED + ((tid & 7) * 32 + (tid >> 3)) * 16) = v4u{c1 | (c1 >> 16), c2 | (c2 >> 16), c3 | (c1 >> 16), c3 | (c2 >> 16)};
    }
    poll_w0(ws, flag_off(F_H, r) + li * 4, tag, budget, w);
    BARRIER();
  }
  STAMP(15);
  unsigned va[4];
  auto lda = [&](auto u_, auto mm_) __attribute__((always_inline)) {
    constexpr int u = decltype(u_)::value, mm = decltype(mm_)::value;
    ld_a<areg(u, mm), 0>(va[mm], ws, (u == 13 && dead) ? OOB : WS_ACT + u * 4096);
  };
  unsigned pa = flag_off(F_A0, r) + li * 4;
  {
    v2u hs[4];
    const unsigned hsrc = WS_H + (4 * r + w) * 40960;
    SF(4, s) ld_a<4 * s, (s & 3) * 1024>(lane16, ws, hsrc); SE
    v4u bf[6];
    SF(4, k) rd(IC<160 + k>{}, bf[k]); SE
    SF(4, k) ld64c<0>(hs[k], (unsigned)(((lane >> 5) * 4 * 2048 + (lane & 31)) * 8), ws, WS_HS + (k * 2048 + 128 * r + 32 * w) * 8); SE
    SF(P3_HUP - 4, s) ld_a<4 * (s + 4), (s & 3) * 1024>(lane16, ws, hsrc + ((s + 4) >> 2) * 4096); SE
    f32x16 acc[4];
    v4u cf[3], mf;
    float c1, kk2, eP[2], eT[2], eO[8];
    unsigned ov[4];
    unsigned csa = L_RED + (lane & 31) * 16;
    unsigned av = ((lane >> 4) & 1) * 57344 + (lane >> 5) * 256 + li * 16;
    SF(4, mm) va[mm] = lane16 + (mt0 + ((w + mm + 1) & 3)) * 57344 + w * 1024; pin(va[mm]); SE
    pin(csa); pin(av); pin(pa);
    const unsigned ab = WS_ACT + (8 * r + 2 * w) * 57344;
    auto epi = [&](auto p_, auto q_) __attribute__((always_inline)) {
      constexpr int p = decltype(p_)::value, q = decltype(q_)::value;
      f32x16& a = acc[p % 4];
      if constexpr (q < 24) {
        constexpr int e = q / 3, ph = q % 3, x = e & 1, y = x ^ 1;
        const float ag = a[e], au = a[8 + e];
        if constexpr (ph == 0) {
          if constexpr (e == 0) asm volatile("v_mul_f32 %0, %1, %2" : "=v"(eT[x]) : "v"(ag), "v"(c1));
          else asm volatile("v_rcp_f32 %1, %1\n\tv_mul_f32 %0, %2, %3" : "=v"(eT[x]), "+v"(eT[y]) : "v"(ag), "v"(c1));
        } else if constexpr (ph == 1) {
          if constexpr (e == 0) asm volatile("v_exp_f32 %0, %0" : "+v"(eT[x]));
          else asm volatile("v_exp_f32 %0, %0\n\tv_mul_f32 %1, %2, %3" : "+v"(eT[x]), "=v"(eO[e - 1]) : "v"(eP[y]), "v"(eT[y]));
        } else {
          if constexpr (e >= 2 && !(e & 1)) asm volatile("v_mul_f32 %0, %3, %4\n\tv_fma_f32 %1, %1, %5, %5\n\tv_cvt_pk_bf16_f32 %2, %6, %7" : "=&v"(eP[x]), "+v"(eT[x]), "=v"(ov[(e - 2) >> 1]) : "v"(ag), "v"(au), "v"(kk2), "v"(eO[e - 2]), "v"(eO[e - 1]));
          else asm volatile("v_mul_f32 %0, %2, %3\n\tv_fma_f32 %1, %1, %4, %4" : "=&v"(eP[x]), "+v"(eT[x]) : "v"(ag), "v"(au), "v"(kk2));
        }
      } else if constexpr (q == 24) asm volatile("v_rcp_f32 %0, %0\n\ts_nop 0" : "+v"(eT[1]));
      else if constexpr (q == 25) asm volatile("v_mul_f32 %1, %1, %2\n\tv_cvt_pk_bf16_f32 %0, %3, %1" : "=v"(ov[3]), "+v"(eP[1]) : "v"(eT[1]), "v"(eO[6]));
      else if constexpr (q == P3_EPI_ST) {
        unsigned so;
        if constexpr (p == 6) so = c < 12 ? ab + (48 + (c >> 1)) * 1024 + (c & 1) * 512 : OOB;
        else so = ab + (16 * (p >> 1) + c) * 1024 + (p & 1) * 512;
        st128c(v4u{ov[0], ov[1], ov[2], ov[3]}, av, ws, so);
      }
    };
    SF(280, g)
      constexpr int p = g < 120 ? g % 3 : g / 40, s = g < 120 ? g / 3 : g % 40;
      if constexpr (g == 120 || g == 200) STAMP(g == 120 ? 28 : 29);
      if constexpr (g == P3_CHK_A0) poll_chk<p3_cnt(P3_POLL_A0, g)>(pf, ws, pa, 0, tag, budget);
      if constexpr (g % 20 == 16) {
        constexpr int nu = 8 + g / 20 + 1;
        if constexpr (g == P3_FL0) WAITV(p3_cnt(p3_st_step(1), g));
        else if constexpr (g == P3_FL1) WAITV(p3_cnt(p3_st_step(3), g));
        else if constexpr (g == P3_FL2) WAITV(p3_cnt(p3_st_step(5), g));
        else if constexpr (nu >= 14 && nu < 22) WAITV(p3_cnt(p3_dma_start(nu) + 12, g));
        BARRIER();
      }
      if constexpr (g < 120 && g % 3 == 0) {
        if constexpr (s < 4) WAITV(P3_HUP + 3 - s + p3_cnt(-1, g));
        else if constexpr (s < P3_HUP) WAITV(P3_HUP - 1 - s + p3_cnt(-1, g));
        else WAITV(p3_cnt(2 * (s - P3_HUP) + 1, g));
      }
      WL(g + 4 < 280 ? 3 : 279 - g);
      mf32_va<s == 0, 4 * s>(acc[p % 4], bf[g % 6]);
      if constexpr (s == 34) dsr128<p * 512>(cf[p % 3], csa);
      if constexpr (g + 4 < 280) rd(IC<160 + g + 4>{}, bf[(g + 4) % 6]);
      if constexpr (g == P3_FL0) st32c(vtag, fl_c, ws, flag_off(F_A0, r));
      if constexpr (g == P3_FL1) st32c(vtag, fl_c, ws, flag_off(F_A1, r));
      if constexpr (g == P3_FL2) st32c(vtag, fl_c, ws, flag_off(F_A2, r));
      if constexpr (p3_hload(g)) { constexpr int hsx = (g >> 1) + P3_HUP; ld_a<4 * hsx, (hsx & 3) * 1024>(lane16, ws, hsrc + (hsx >> 2) * 4096); }
      if constexpr (p3_dma_at(g) >= 0) {
        constexpr int nu = p3_dma_at(g) / 8, f = p3_dma_at(g) % 8;
        unit_dma<nu, f, slot_of(nu + 1)>(ws, lane16, lane16b, nu < 22 ? w3src + (nu - 8) * SLOT : w4src + (nu - 18) * SLOT, ldsw);
      }
      if constexpr (p3_w4a_at(g) >= 10) ldw4a(IC<p3_w4a_at(g) - 10>{});
      else if constexpr (p3_w4a_at(g) >= 0) pfx(IC<40 + p3_w4a_at(g)>{});
      if constexpr (p3_aload(g)) lda(IC<(g - P3_AL0) / 8>{}, IC<((g - P3_AL0) / 2) % 4>{});
      if constexpr (g == P3_POLL_A0) ld32c<0>(pf, pa, ws, 0);
      if constexpr (g == P3_POLL_A1) ld32c<0>(pf, pa, ws, 4096);
      if constexpr (g == 13) {
        SF(4, k) pin(hs[k]); SE
        const float s1 = xsum32((asf(hs[0].x) + asf(hs[1].x)) + (asf(hs[2].x) + asf(hs[3].x)));
        const float s2 = xsum32((asf(hs[0].y) + asf(hs[1].y)) + (asf(hs[2].y) + asf(hs[3].y)));
        const float mean = s1 * (1.f / 640.f);
        kk2 = fmaxf(s2 * (1.f / 640.f) - mean * mean, 0.f) + 1e-5f;
        c1 = -1.44269504f * __builtin_amdgcn_rsqf(kk2);
        unsigned m1, m2, m3;
        split3(lane < 32 ? -mean : 0.f, m1, m2, m3);
        mf = v4u{m2 | (m1 >> 16), m2 | (m1 >> 16), m1 | (m3 >> 16), m2 | (m3 >> 16)};
        pin(kk2); pin(c1); pin(mf);
      }
      SF(5, ep) if constexpr (g >= p3_epi_start(ep) && g <= p3_epi_start(ep) + P3_EPI_ST) epi(IC<ep>{}, IC<g - p3_epi_start(ep)>{}); SE
      if constexpr (g >= 246 && g < 260) { epi(IC<5>{}, IC<2 * (g - 246)>{}); epi(IC<5>{}, IC<2 * (g - 246) + 1>{}); }
      if constexpr (s == 39) mf32_vv(acc[p % 4], cf[p % 3], mf);
    SE
    STAMP(16);
    asm volatile("s_nop 11");
    {
      f32x16& a = acc[2];
      pin(a);
      float o[8];
      SF(8, e) o[e] = a[e] * a[8 + e] * __builtin_amdgcn_rcpf(fmaf(__builtin_amdgcn_exp2f(a[e] * c1), kk2, kk2)); SE
      SF(4, k) ov[k] = cvtpk(o[2 * k], o[2 * k + 1]); SE
      epi(IC<6>{}, IC<P3_EPI_ST>{});
    }
    poll_chk<1>(pf, ws, pa, 4096, tag, budget);
    STAMP(17);
    STAMP(18);
  }
  {
    f32x4 acc[4][5];
    v4u bf[2][5];
    auto rdw = [&](auto u_, auto t_) __attribute__((always_inline)) {
      constexpr int u = decltype(u_)::value, t = decltype(t_)::value, sl = (18 + u) % 7;
      dsr128<(sl % 3) * SLOT + t * 1024>(bf[u & 1][t], sl < 3 ? pb0 : sl < 6 ? pb1 : pb2);
    };
    SF(5, t)
      const unsigned x0 = t < 4 ? hp[(t >> 1) & 1][(t & 1) * 2] : hp4[0], x1 = t < 4 ? hp[(t >> 1) & 1][(t & 1) * 2 + 1] : hp4[1];
      acc[3][t] = f32x4{bflo(x0), bfhi(x0), bflo(x1), bfhi(x1)};
      pin(acc[3][t]);
    SE
    SF(11, u)
      if constexpr (u == 2) { WAITV(12); BARRIER(); st32c(vtag, fl_c, ws, flag_off(F_A3, r)); }
      if constexpr (u == 4) poll_chk<6>(pf, ws, pa, 8192, tag, budget);
      if constexpr (u == 7) WAITV(13);
      if constexpr (u == 8) WAITV(4);
      if constexpr (u == 9) poll_chk<0>(pf, ws, pa, 12288, tag, budget);
      if constexpr (u >= 4 && u < 10) WL(0);
      SF(4, mm) SF(5, t)
        if constexpr (u == 0 && mm < 3) mf16_lva0<VRES + 4 * t, areg(u, mm)>(acc[mm][t]);
        else if constexpr (u < 4) mf16_lva<VRES + 20 * u + 4 * t, areg(u, mm)>(acc[mm][t]);
        else if constexpr (u < 10) mf16_va<false, areg(u, mm)>(acc[mm][t], bf[u & 1][t]);
        else mf16_aa<false, 160 + 20 * (u - 10) + 4 * t, areg(u, mm)>(acc[mm][t]);
        constexpr int i = mm * 5 + t;
        if constexpr (u >= 3 && u < 9 && i < 10 && (i & 1)) rdw(IC<u + 1>{}, IC<i / 2>{});
        if constexpr (i == 0 && u == 3) ld32c<0>(pf, pa, ws, 8192);
        if constexpr (i == 0 && u == 8) ld32c<0>(pf, pa, ws, 12288);
        if constexpr (u < 8 && i < 12 && i % 3 == 2) lda(IC<u + 4>{}, IC<i / 3>{});
        if constexpr (u == 9 && i < 16 && !(i & 1)) lda(IC<12 + i / 8>{}, IC<(i / 2) % 4>{});
        if constexpr (u < 7 && (i == 13 || i == 16 || (i == 19 && u == 5))) {
          constexpr int k = 2 * u + (u > 5) + (i - 13) / 3;
          unit_dma<25 + k / 5, k % 5, slot_of(26 + k / 5)>(ws, lane16, lane16b, w4src + (7 + k / 5) * SLOT, ldsw);
        }
      SE SE
    SE
    f32x4 pr[3][5];
    unsigned rb[3];
    SF(3, mm) rb[mm] = mm * SLOT + ((w + 3 - mm) & 3) * 5120 + lane16; pin(rb[mm]); SE
    SF(4, mm)
      SF(5, t)
        mf16_aa<false, 180 + 4 * t, areg(11, mm)>(acc[mm][t]);
        if constexpr (mm > 0 && t >= 3) dsw128f<(mm - 1) * SLOT + (t - 3) * 1024>(acc[mm - 1][t - 3], pb0);
      SE
      if constexpr (mm == 0) WAITV(4);
      SF(5, t)
        mf16_aa<false, 200 + 4 * t, areg(12, mm)>(acc[mm][t]);
        if constexpr (mm > 0 && t < 3) dsw128f<(mm - 1) * SLOT + (t + 2) * 1024>(acc[mm - 1][t + 2], pb0);
      SE
      if constexpr (mm == 0) WAITV(0);
      if constexpr (mm == 3) BARRIER_L();
      SF(5, t)
        mf16_aa<false, 220 + 4 * t, areg(13, mm)>(acc[mm][t]);
        if constexpr (mm == 3) { SF(3, k) dsr128f<t * 1024>(pr[k][t], rb[k]); SE }
      SE
    SE
    STAMP(19);
    f32x4 v[5];
    SF(5, t)
      WL(12 - 3 * t);
      pin(pr[0][t]); pin(pr[1][t]); pin(pr[2][t]); pin(acc[3][t]);
      v[t] = ((pr[0][t] + pr[1][t]) + pr[2][t]) + acc[3][t];
    SE
    const rsrc_t ro = mk(ptr2(ka[12], ka[13]), 2048 * 640 * 2);
    const unsigned oo = ((128 * r + 64 * hf + 16 * w + li) * 640 + 80 * j) * 2;
    SF(2, k) st128p(v4u{cvtpk(v[2 * k][0], v[2 * k][1]), cvtpk(v[2 * k][2], v[2 * k][3]), cvtpk(v[2 * k + 1][0], v[2 * k + 1][1]), cvtpk(v[2 * k + 1][2], v[2 * k + 1][3])}, oo + lg * 16, ro, k * 64); SE
    st64p(v2u{cvtpk(v[4][0], v[4][1]), cvtpk(v[4][2], v[4][3])}, oo + lg * 8, ro, 128);
  }
  STAMP(20);
  asm volatile("fbk_end:");
}

extern "C" void kernel_launch(void* const* d_in, const int* in_sizes, int n_in, void* d_out, int out_size, void* d_ws, size_t ws_size, hipStream_t stream) {
  (void)in_sizes; (void)n_in; (void)out_size; (void)ws_size;
  (void)hipFuncSetAttribute((const void*)fused_block, hipFuncAttributeMaxDynamicSharedMemorySize, (int)LDS_BYTES);
  fused_block<<<256, 256, LDS_BYTES, stream>>>(d_in[0], d_in[1], d_in[2], d_in[4], d_in[5], d_in[6], d_out, (char*)d_ws);
}
```

```cpp
#include <hip/hip_runtime.h>
#include <utility>
#include <type_traits>

#define PROF 0

#define DI __device__ __forceinline__
#define LAS __attribute__((address_space(3)))
typedef unsigned v4u __attribute__((ext_vector_type(4)));
typedef unsigned v2u __attribute__((ext_vector_type(2)));
typedef float f32x2 __attribute__((ext_vector_type(2)));
typedef float f32x4 __attribute__((ext_vector_type(4)));
typedef float f32x16 __attribute__((ext_vector_type(16)));
typedef __bf16 bf16x2 __attribute__((ext_vector_type(2)));
typedef __amdgpu_buffer_rsrc_t rsrc_t;

template <int N> using IC = std::integral_constant<int, N>;
template <class Fn, int... Is> DI void sfor_impl(Fn&& f, std::integer_sequence<int, Is...>) { (f(IC<Is>{}), ...); }
template <int N, class Fn> DI void sfor(Fn&& f) { sfor_impl(f, std::make_integer_sequence<int, N>{}); }
#define SF(N, i) sfor<N>([&](auto i##_) __attribute__((always_inline)) { constexpr int i = decltype(i##_)::value;
#define SE });

constexpr unsigned WS_FLAG = 4096;
constexpr unsigned WS_PROF = 65536;
constexpr unsigned WS_W1T = 262144;
constexpr unsigned WS_CS1 = WS_W1T + 16 * 163840;
constexpr unsigned WS_W2T = WS_CS1 + 16 * 16 * 512;
constexpr unsigned WS_W3T = WS_W2T + 8 * 102400;
constexpr unsigned WS_CS3 = WS_W3T + 16 * 286720;
constexpr unsigned WS_W4T = WS_CS3 + 16 * 16 * 1024;
constexpr unsigned WS_KVP = WS_W4T + 8 * 286720;
constexpr unsigned WS_O = WS_KVP + 16 * 16 * 4096;
constexpr unsigned WS_H = WS_O + 128 * 20480;
constexpr unsigned WS_HS = WS_H + 64 * 40960;
constexpr unsigned WS_ACT = WS_HS + 8 * 2048 * 8;
constexpr unsigned WS_END = WS_ACT + 128 * 57344;
constexpr unsigned OOB = 0x40000000u;
enum { F_W1 = 0, F_W4 = 1, F_KV = 2, F_O = 3, F_H = 4, F_A0 = 5, F_A1 = 6, F_A2 = 7, F_A3 = 8 };
DI unsigned flag_off(int kind, int grp) { return WS_FLAG + (kind * 16 + grp) * 256; }

constexpr unsigned SLOT = 20480;
constexpr unsigned L_XST = 3 * SLOT;
constexpr unsigned L_MISC = 143360;
constexpr unsigned L_RED = L_MISC;
constexpr unsigned L_KVT = L_MISC + 13440;
constexpr unsigned L_MEAN = L_MISC + 17792, L_RSTD = L_MEAN + 512, L_CS1 = L_RSTD + 512, L_CS3 = L_CS1 + 512;
constexpr unsigned LDS_BYTES = 163840;

#define WAITV(n) asm volatile("s_waitcnt vmcnt(%0)" ::"i"(n) : "memory")
#define WAITL(n) asm volatile("s_waitcnt lgkmcnt(%0)" ::"i"(n) : "memory")
#define BARRIER() asm volatile("s_barrier" ::: "memory")
#define BARRIER_L() asm volatile("s_waitcnt lgkmcnt(0)\n\ts_barrier" ::: "memory")
#define FENCE() __builtin_amdgcn_sched_barrier(0)

template <int C> DI void set_m0(unsigned base) { asm volatile("s_add_u32 m0, %0, %c1\n\ts_nop 0" ::"s"(base), "i"(C) : "scc", "memory"); }
template <int IMM, bool SC1, int NEXT> DI void dma(unsigned voff, rsrc_t rs, unsigned soff, unsigned nb) {
  if constexpr (SC1) {
    if constexpr (NEXT >= 0) asm volatile("buffer_load_dwordx4 %0, %1, %2 offen offset:%c3 sc1 lds\n\ts_add_u32 m0, %4, %c5" ::"v"(voff), "s"(rs), "s"(soff), "i"(IMM), "s"(nb), "i"(NEXT) : "scc", "memory");
    else asm volatile("buffer_load_dwordx4 %0, %1, %2 offen offset:%c3 sc1 lds" ::"v"(voff), "s"(rs), "s"(soff), "i"(IMM) : "memory");
  } else {
    if constexpr (NEXT >= 0) asm volatile("buffer_load_dwordx4 %0, %1, %2 offen offset:%c3 lds\n\ts_add_u32 m0, %4, %c5" ::"v"(voff), "s"(rs), "s"(soff), "i"(IMM), "s"(nb), "i"(NEXT) : "scc", "memory");
    else asm volatile("buffer_load_dwordx4 %0, %1, %2 offen offset:%c3 lds" ::"v"(voff), "s"(rs), "s"(soff), "i"(IMM) : "memory");
  }
}
#define NOP1() asm volatile("s_nop 0")
template <int R, int IMM> DI void ld_a(unsigned voff, rsrc_t rs, unsigned soff) {
  asm volatile("buffer_load_dwordx4 a[%c0:%c1], %2, %3, %4 offen offset:%c5 sc1" ::"i"(R), "i"(R + 3), "v"(voff), "s"(rs), "s"(soff), "i"(IMM) : "memory");
}
template <int IMM> DI void ld128c(v4u& d, unsigned voff, rsrc_t rs, unsigned soff) {
  asm volatile("buffer_load_dwordx4 %0, %1, %2, %3 offen offset:%c4 sc1" : "=v"(d) : "v"(voff), "s"(rs), "s"(soff), "i"(IMM) : "memory");
}
template <int IMM> DI void ld64c(v2u& d, unsigned voff, rsrc_t rs, unsigned soff) {
  asm volatile("buffer_load_dwordx2 %0, %1, %2, %3 offen offset:%c4 sc1" : "=v"(d) : "v"(voff), "s"(rs), "s"(soff), "i"(IMM) : "memory");
}
template <int IMM> DI void ld32c(unsigned& d, unsigned voff, rsrc_t rs, unsigned soff) {
  asm volatile("buffer_load_dword %0, %1, %2, %3 offen offset:%c4 sc1" : "=v"(d) : "v"(voff), "s"(rs), "s"(soff), "i"(IMM) : "memory");
}
DI void st128c(v4u d, unsigned voff, rsrc_t rs, unsigned soff) {
  asm volatile("buffer_store_dwordx4 %0, %1, %2, %3 offen sc1\n\ts_nop 1" ::"v"(d), "v"(voff), "s"(rs), "s"(soff) : "memory");
}
DI void st64c(v2u d, unsigned voff, rsrc_t rs, unsigned soff) {
  asm volatile("buffer_store_dwordx2 %0, %1, %2, %3 offen sc1" ::"v"(d), "v"(voff), "s"(rs), "s"(soff) : "memory");
}
DI void st32c(unsigned d, unsigned voff, rsrc_t rs, unsigned soff) {
  asm volatile("buffer_store_dword %0, %1, %2, %3 offen sc1" ::"v"(d), "v"(voff), "s"(rs), "s"(soff) : "memory");
}
DI void st128p(v4u d, unsigned voff, rsrc_t rs, unsigned soff) {
  asm volatile("buffer_store_dwordx4 %0, %1, %2, %3 offen\n\ts_nop 1" ::"v"(d), "v"(voff), "s"(rs), "s"(soff) : "memory");
}
DI void st64p(v2u d, unsigned voff, rsrc_t rs, unsigned soff) {
  asm volatile("buffer_store_dwordx2 %0, %1, %2, %3 offen" ::"v"(d), "v"(voff), "s"(rs), "s"(soff) : "memory");
}
template <int OFF> DI void dsr128(v4u& d, unsigned addr) { asm volatile("ds_read_b128 %0, %1 offset:%c2" : "=v"(d) : "v"(addr), "i"(OFF) : "memory"); }
template <int OFF> DI void dsr128f(f32x4& d, unsigned addr) { asm volatile("ds_read_b128 %0, %1 offset:%c2" : "=v"(d) : "v"(addr), "i"(OFF) : "memory"); }
template <int R, int OFF> DI void dsr128a(unsigned addr) { asm volatile("ds_read_b128 a[%c0:%c1], %2 offset:%c3" ::"i"(R), "i"(R + 3), "v"(addr), "i"(OFF) : "memory"); }
template <int OFF> DI v4u tr_piece(unsigned addr) {
  v2u lo, hi;
  asm volatile("ds_read_b64_tr_b16 %0, %2 offset:%c3\n\tds_read_b64_tr_b16 %1, %2 offset:%c4\n\ts_waitcnt lgkmcnt(0)" : "=&v"(lo), "=&v"(hi) : "v"(addr), "i"(OFF), "i"(OFF + 512) : "memory");
  return v4u{lo.x, lo.y, hi.x, hi.y};
}
template <bool Z, int RA> DI void mf16_av(f32x4& acc, const v4u& b) {
  if constexpr (Z) asm volatile("v_mfma_f32_16x16x32_bf16 %0, a[%c1:%c2], %3, 0" : "=&v"(acc) : "i"(RA), "i"(RA + 3), "v"(b));
  else asm volatile("v_mfma_f32_16x16x32_bf16 %0, a[%c1:%c2], %3, %0" : "+v"(acc) : "i"(RA), "i"(RA + 3), "v"(b));
}
template <bool Z, int RB> DI void mf16_va(f32x4& acc, const v4u& a) {
  if constexpr (Z) asm volatile("v_mfma_f32_16x16x32_bf16 %0, %3, a[%c1:%c2], 0" : "=&v"(acc) : "i"(RB), "i"(RB + 3), "v"(a));
  else asm volatile("v_mfma_f32_16x16x32_bf16 %0, %3, a[%c1:%c2], %0" : "+v"(acc) : "i"(RB), "i"(RB + 3), "v"(a));
}
template <bool Z, int RA, int RB> DI void mf16_aa(f32x4& acc) {
  if constexpr (Z) asm volatile("v_mfma_f32_16x16x32_bf16 %0, a[%c1:%c2], a[%c3:%c4], 0" : "=&v"(acc) : "i"(RA), "i"(RA + 3), "i"(RB), "i"(RB + 3));
  else asm volatile("v_mfma_f32_16x16x32_bf16 %0, a[%c1:%c2], a[%c3:%c4], %0" : "+v"(acc) : "i"(RA), "i"(RA + 3), "i"(RB), "i"(RB + 3));
}
template <bool Z> DI void mf16_vv(f32x4& acc, const v4u& a, const v4u& b) {
  if constexpr (Z) asm volatile("s_nop 1\n\tv_mfma_f32_16x16x32_bf16 %0, %1, %2, 0" : "=&v"(acc) : "v"(a), "v"(b));
  else asm volatile("s_nop 1\n\tv_mfma_f32_16x16x32_bf16 %0, %1, %2, %0" : "+v"(acc) : "v"(a), "v"(b));
}
template <bool Z, int RB> DI void mf32_va(f32x16& acc, const v4u& a) {
  if constexpr (Z) asm volatile("v_mfma_f32_32x32x16_bf16 %0, %3, a[%c1:%c2], 0" : "=&v"(acc) : "i"(RB), "i"(RB + 3), "v"(a));
  else asm volatile("v_mfma_f32_32x32x16_bf16 %0, %3, a[%c1:%c2], %0" : "+v"(acc) : "i"(RB), "i"(RB + 3), "v"(a));
}
DI void mf32_vv(f32x16& acc, const v4u& a, const v4u& b) { asm volatile("v_mfma_f32_32x32x16_bf16 %0, %1, %2, %0" : "+v"(acc) : "v"(a), "v"(b)); }
template <int RA, int RB> DI void mf16_lva(f32x4& acc) {
  asm volatile("v_mfma_f32_16x16x32_bf16 %0, v[%c1:%c2], a[%c3:%c4], %0" : "+v"(acc) : "i"(RA), "i"(RA + 3), "i"(RB), "i"(RB + 3));
}
template <int RA, int RB> DI void mf16_lva0(f32x4& acc) {
  asm volatile("v_mfma_f32_16x16x32_bf16 %0, v[%c1:%c2], a[%c3:%c4], 0" : "=&v"(acc) : "i"(RA), "i"(RA + 3), "i"(RB), "i"(RB + 3));
}
template <int R, int IMM> DI void ld_v(unsigned voff, rsrc_t rs, unsigned soff) {
  asm volatile("buffer_load_dwordx4 v[%c0:%c1], %2, %3, %4 offen offset:%c5 sc1" ::"i"(R), "i"(R + 3), "v"(voff), "s"(rs), "s"(soff), "i"(IMM) : "memory");
}
#define MFMA_DRAIN() do { asm volatile("s_nop 15" ::: "memory"); FENCE(); } while (0)
template <class T> DI void pin(T& x) { asm volatile("" : "+v"(x)); }

DI unsigned cvtpk(float lo, float hi) { f32x2 v = {lo, hi}; bf16x2 b = __builtin_convertvector(v, bf16x2); return __builtin_bit_cast(unsigned, b); }
DI float asf(unsigned u) { return __builtin_bit_cast(float, u); }
DI float bflo(unsigned u) { return __builtin_bit_cast(float, u << 16); }
DI float bfhi(unsigned u) { return __builtin_bit_cast(float, u & 0xffff0000u); }
DI float sum8(v4u v, float acc) {
  acc += (bflo(v.x) + bfhi(v.x)) + (bflo(v.y) + bfhi(v.y));
  acc += (bflo(v.z) + bfhi(v.z)) + (bflo(v.w) + bfhi(v.w));
  return acc;
}
DI float phi1(float t1) {
  const float e = __builtin_amdgcn_fmed3f(__builtin_amdgcn_exp2f(fmaf(t1, 1.44269504f, -1.44269504e-6f)), 0.f, 1.f);
  return fmaxf(t1, 1e-6f) + e;
}
template <int R, int OFF, int P4 = 512> DI void tr_a(unsigned addr) {
  asm volatile("ds_read_b64_tr_b16 a[%c0:%c1], %4 offset:%c5\n\tds_read_b64_tr_b16 a[%c2:%c3], %4 offset:%c6" ::"i"(R), "i"(R + 1), "i"(R + 2), "i"(R + 3), "v"(addr), "i"(OFF), "i"(OFF + P4) : "memory");
}
template <int R> DI void st_a(unsigned voff, rsrc_t rs, unsigned soff) {
  asm volatile("buffer_store_dwordx4 a[%c0:%c1], %2, %3, %4 offen sc1" ::"i"(R), "i"(R + 3), "v"(voff), "s"(rs), "s"(soff) : "memory");
}
template <int OFF> DI void dsw128f(const f32x4& d, unsigned addr) { asm volatile("ds_write_b128 %0, %1 offset:%c2" ::"v"(addr), "v"(d), "i"(OFF) : "memory"); }
DI float v_fma(float a, float b, float c) { float r; asm volatile("v_fma_f32 %0, %1, %2, %3" : "=v"(r) : "v"(a), "v"(b), "v"(c)); return r; }
DI float v_mul(float a, float b) { float r; asm volatile("v_mul_f32 %0, %1, %2" : "=v"(r) : "v"(a), "v"(b)); return r; }
DI float v_exp(float a) { float r; asm volatile("v_exp_f32 %0, %1" : "=v"(r) : "v"(a)); return r; }
DI float v_rcp(float a) { float r; asm volatile("v_rcp_f32 %0, %1" : "=v"(r) : "v"(a)); return r; }
DI unsigned v_cvtpk(float a, float b) { unsigned r; asm volatile("v_cvt_pk_bf16_f32 %0, %1, %2" : "=v"(r) : "v"(a), "v"(b)); return r; }
DI rsrc_t mk(const void* p, unsigned bytes) { return __builtin_amdgcn_make_buffer_rsrc((void*)p, 0, (int)bytes, 0x00020000); }

#define ACLOB "a0","a1","a2","a3","a4","a5","a6","a7","a8","a9","a10","a11","a12","a13","a14","a15","a16","a17","a18","a19","a20","a21","a22","a23","a24","a25","a26","a27","a28","a29","a30","a31","a32","a33","a34","a35","a36","a37","a38","a39","a40","a41","a42","a43","a44","a45","a46","a47","a48","a49","a50","a51","a52","a53","a54","a55","a56","a57","a58","a59","a60","a61","a62","a63","a64","a65","a66","a67","a68","a69","a70","a71","a72","a73","a74","a75","a76","a77","a78","a79","a80","a81","a82","a83","a84","a85","a86","a87","a88","a89","a90","a91","a92","a93","a94","a95","a96","a97","a98","a99","a100","a101","a102","a103","a104","a105","a106","a107","a108","a109","a110","a111","a112","a113","a114","a115","a116","a117","a118","a119","a120","a121","a122","a123","a124","a125","a126","a127","a128","a129","a130","a131","a132","a133","a134","a135","a136","a137","a138","a139","a140","a141","a142","a143","a144","a145","a146","a147","a148","a149","a150","a151","a152","a153","a154","a155","a156","a157","a158","a159","a160","a161","a162","a163","a164","a165","a166","a167","a168","a169","a170","a171","a172","a173","a174","a175","a176","a177","a178","a179","a180","a181","a182","a183","a184","a185","a186","a187","a188","a189","a190","a191","a192","a193","a194","a195","a196","a197","a198","a199","a200","a201","a202","a203","a204","a205","a206","a207","a208","a209","a210","a211","a212","a213","a214","a215","a216","a217","a218","a219","a220","a221","a222","a223","a224","a225","a226","a227","a228","a229","a230","a231","a232","a233","a234","a235","a236","a237","a238","a239","a240","a241","a242","a243","a244","a245","a246","a247","a248","a249","a250","a251","a252","a253","a254","a255"

DI void poll_w0(rsrc_t ws, unsigned voff, unsigned tag, int& budget, int w) {
  unsigned f;
  asm volatile("s_cmp_lg_u32 %5, 0\n\ts_cbranch_scc1 2f\n"
               "1:\n\tbuffer_load_dword %0, %2, %3, 0 offen sc1\n\ts_waitcnt vmcnt(0)\n\tv_cmp_ne_u32 vcc, %4, %0\n\ts_nop 1\n\ts_cbranch_vccz 2f\n\t"
               "s_sub_u32 %1, %1, 1\n\ts_cmp_gt_i32 %1, 0\n\ts_cbranch_scc1 1b\n2:"
               : "=&v"(f), "+s"(budget) : "v"(voff), "s"(ws), "s"(tag), "s"(w) : "vcc", "scc", "memory");
}
template <int N> DI void poll_chk(unsigned f, rsrc_t ws, unsigned voff, unsigned soff, unsigned tag, int& budget) {
  asm volatile("s_waitcnt vmcnt(%c6)\n"
               "1:\n\tv_cmp_ne_u32 vcc, %4, %0\n\ts_nop 1\n\ts_cbranch_vccz 2f\n\t"
               "s_sub_u32 %1, %1, 1\n\ts_cmp_gt_i32 %1, 0\n\ts_cbranch_scc0 2f\n\t"
               "buffer_load_dword %0, %2, %3, %5 offen sc1\n\ts_waitcnt vmcnt(0)\n\ts_branch 1b\n2:"
               : "+v"(f), "+s"(budget) : "v"(voff), "s"(ws), "s"(tag), "s"(soff), "i"(N) : "vcc", "scc", "memory");
}

#define STAMP(i) asm volatile("; MARK %0" ::"i"(i))

constexpr int slot_of(int u) { return (u % 7) * (int)SLOT; }
template <int U, int FR, int NEXT> DI void unit_dma(rsrc_t ws, unsigned lane16, unsigned lane16b, unsigned soff, unsigned ldsw) {
  if constexpr (FR < 3) dma<FR * 1024, true, -1>(lane16, ws, soff, ldsw);
  else if constexpr (FR == 3) dma<3072, true, slot_of(U) + 1024>(lane16, ws, soff, ldsw);
  else dma<3072, true, NEXT>(lane16b, ws, soff, ldsw);
}

#define WL(n) __builtin_amdgcn_s_waitcnt(0xC07F | ((n) << 8))
template <int R, int OFF> DI void dsr64a(unsigned addr) { asm volatile("ds_read_b64 a[%c0:%c1], %2 offset:%c3" ::"i"(R), "i"(R + 1), "v"(addr), "i"(OFF) : "memory"); }
template <int OFF> DI void dsr32f(float& d, unsigned addr) { asm volatile("ds_read_b32 %0, %1 offset:%c2" : "=v"(d) : "v"(addr), "i"(OFF) : "memory"); }
constexpr unsigned long long kv_mask(int a, int bb) {
  unsigned long long m = 0;
  for (int l = 0; l < 64; ++l) {
    const int li = l & 15, dq = 4 * a + (l >> 4), hd = dq >= 5, eg = bb < 2 ? 16 * bb + li : 24 + li;
    const bool ok = dq < 10 && (bb == 3 ? li == 0 : (bb < 2 || li >= 8) && eg / 20 == hd);
    if (ok) m |= 1ull << l;
  }
  return m;
}
template <int A, int BB, int OFF> DI void dsw_kv(const f32x4& d, unsigned addr) {
  asm volatile("s_mov_b32 exec_lo, %c2\n\ts_mov_b32 exec_hi, %c3\n\tds_write_b128 %0, %1 offset:%c4" ::"v"(addr), "v"(d), "i"((int)(unsigned)kv_mask(A, BB)), "i"((int)(unsigned)(kv_mask(A, BB) >> 32)), "i"(OFF) : "memory");
}
DI float sel4(const f32x4& v, int i) { const float a = (i & 1) ? v[1] : v[0], b = (i & 1) ? v[3] : v[2]; return (i & 2) ? b : a; }
DI float xsum32(float s) {
  const unsigned u = __builtin_bit_cast(unsigned, s);
  const auto r = __builtin_amdgcn_permlane32_swap(u, u, false, false);
  return asf(r[0]) + asf(r[1]);
}
constexpr int VRES = 176;
constexpr int P3_HUP = 6;
constexpr int P3_LASTU = 24;
constexpr int p3_dma_start(int nu) { int d = nu - 15; int a = 85 + 15 * d, b = 36 + 20 * d; return a > b ? a : b; }
constexpr int p3_dma_at(int g) {
  for (int nu = 14; nu <= P3_LASTU; ++nu) { int s = p3_dma_start(nu); if (g >= s && g < s + 15 && (g - s) % 3 == 0) return nu * 8 + (g - s) / 3; }
  return -1;
}
constexpr int p3_w4a_at(int g) { return (g >= 72 && (g - 72) % 6 == 0 && (g - 72) / 6 < 30) ? (g - 72) / 6 : -1; }
constexpr int p3_epi_start(int p) { return p < 5 ? 123 + 24 * p : 246; }
constexpr int P3_EPI_ST = 26;
constexpr int p3_st_step(int p) { return p < 5 ? p3_epi_start(p) + P3_EPI_ST : p3_epi_start(5) + P3_EPI_ST / 2; }
constexpr int P3_FL0 = 196, P3_FL1 = 236, P3_FL2 = 276;
constexpr int P3_POLL_A0 = 232, P3_CHK_A0 = 246, P3_POLL_A1 = 278, P3_AL0 = 247;
constexpr bool p3_hload(int g) { return (g & 1) && (g >> 1) + P3_HUP < 40; }
constexpr bool p3_aload(int g) { return g >= P3_AL0 && g < P3_AL0 + 32 && (g - P3_AL0) % 2 == 0; }
constexpr bool p3_store(int g) { for (int p = 0; p < 6; ++p) if (g == p3_st_step(p)) return true; return false; }
constexpr int p3_nvm(int g) { return p3_hload(g) + (p3_dma_at(g) >= 0) + (p3_w4a_at(g) >= 0) + p3_aload(g) + p3_store(g) + (g == P3_POLL_A0) + (g == P3_POLL_A1) + (g == P3_FL0) + (g == P3_FL1) + (g == P3_FL2); }
constexpr int p3_cnt(int gx, int g) { int n = 0; for (int k = gx + 1; k < g; ++k) n += p3_nvm(k); return n > 63 ? 63 : n; }
DI void split3(float x, unsigned& a, unsigned& b, unsigned& c) {
  a = __builtin_bit_cast(unsigned, x) & 0xffff0000u;
  const float r1 = x - asf(a);
  b = __builtin_bit_cast(unsigned, r1) & 0xffff0000u;
  c = __builtin_bit_cast(unsigned, r1 - asf(b)) & 0xffff0000u;
}
constexpr int areg(int u, int mm) { return (u == 0 ? 240 : u < 11 ? 16 * (u - 1) : 16 * (u - 11)) + 4 * mm; }
typedef unsigned v16u __attribute__((ext_vector_type(16)));
DI const void* ptr2(unsigned lo, unsigned hi) { return (const void*)(((unsigned long long)hi << 32) | lo); }

__global__ void __launch_bounds__(256, 1) __attribute__((amdgpu_num_vgpr(VRES)))
fused_block(const void* __restrict__ xp_, const void* __restrict__ wqkvp_, const void* __restrict__ wop_, const void* __restrict__ wgp_,
            const void* __restrict__ wup_, const void* __restrict__ wdp_, void* __restrict__ outp_, char* __restrict__ wsp_) {
  extern __shared__ __attribute__((aligned(16))) char smem_[];
  LAS char* L = (LAS char*)smem_;
  unsigned long long pc0; unsigned codelen; v16u ka;
  asm volatile("s_load_dwordx16 %2, %3, 0\nfbk_begin:\n\ts_getpc_b64 %0\n\ts_mov_b32 %1, fbk_end-fbk_begin"
               : "=&s"(pc0), "=&s"(codelen), "=&s"(ka) : "s"(__builtin_amdgcn_kernarg_segment_ptr()) : ACLOB, "v255");
  FENCE();
  const int tid = threadIdx.x, lane = tid & 63;
  const int w = __builtin_amdgcn_readfirstlane(tid >> 6);
  const int b = blockIdx.x, xcd = b & 7, bi = b >> 3;
  const int r = (xcd >> 1) * 4 + (bi & 3), c = (xcd & 1) * 8 + (bi >> 2);
  const int j = (c >> 3) * 4 + (c & 3), hf = (c >> 2) & 1;
  const int li = lane & 15, lg = lane >> 4;
  const unsigned lane16 = lane * 16, lane16b = lane16 + 1024;
  const unsigned ldsw = w * 5120;
  int budget = 200000;
  const int drow = lane >> 3, dch = lane & 7, tq = li >> 2, tp = li & 3;
  const int pp = 2 * r + hf;
  const unsigned xst = L_XST + w * 20480;
  unsigned vo1, vo1b, vo2, vct;
  {
    int col = 640 + 8 * li;
    col = li >= 5 ? 1232 + 8 * li : col; col = li == 5 ? 1312 : col; col = li >= 10 ? 8 * li - 80 : col; col = li == 15 ? 32 : col;
    vo1 = ((40 * r + 12 * w + lg) * 1920 + 40 * c + col) * 2;
    vo1b = w == 3 ? OOB : vo1;
    pin(vo1); pin(vo1b);
  }
  set_m0<SLOT>(3072 * w);
  asm volatile("s_waitcnt lgkmcnt(0)" : "+s"(ka));
  char* const wsp = (char*)ptr2(ka[14], ka[15]);
  const rsrc_t ws = mk(wsp, WS_END);
  const rsrc_t rx = mk(ptr2(ka[0], ka[1]), 2048 * 640 * 2);
  STAMP(0);
  unsigned epoch, cpf;
  {
    const rsrc_t rw = mk(ptr2(ka[2], ka[3]), 640 * 1920 * 2);
    dma<0, false, -1>(vo1, rw, 0, 0);
    dma<1024, false, -1>(vo1b, rw, 4 * 3840 - 1024, 0);
    dma<2048, false, 2 * SLOT>(vo1b, rw, 8 * 3840 - 2048, ldsw);
    const int oc = 32 * (w < 3 ? w : dch >> 1) + pp, n = oc >> 2, g = oc & 3;
    const int ra = 40 * n + 8 * g, rb = 160 * (n - 16) + 40 * g + 32;
    vo2 = ((n < 16 ? ra : rb) + drow) * 1280 + 160 * j + (w < 3 ? dch * 16 : 128 + (dch & 1) * 16);
    vo2 = oc < 80 ? vo2 : OOB;
    vct = tid < 16 ? (unsigned)(bi * 16 + tid) * 128 : OOB;
    const rsrc_t rw2 = mk((const char*)ptr2(ka[4], ka[5]) - 3072, 640 * 640 * 2 + 3072);
    dma<3072, false, 0>(vo2, rw2, 0, xst);
    unsigned z = 0; ld32c<0>(epoch, z, ws, 0);
    const rsrc_t rc = mk((const void*)pc0, codelen);
    asm volatile("buffer_load_dword %0, %1, %2, 0 offen" : "=v"(cpf) : "v"(vct), "s"(rc) : "memory");
  }
  unsigned xv[4];
  SF(4, pc) xv[pc] = drow * 1280 + ((dch ^ (4 * (pc & 1) + lg)) * 16) + pc * (10240 - 1024); SE
  const unsigned xrow = (128 * r + 32 * w) * 1280;
  auto x_piece = [&](auto ch_, auto pc_) __attribute__((always_inline)) {
    constexpr int ch = decltype(ch_)::value, pc = decltype(pc_)::value;
    dma<pc * 1024, false, (pc == 3 ? ((ch + 1) % 5) * 4096 : -1)>(xv[pc], rx, xrow + ch * 128, xst);
  };
  auto x_chunk = [&](auto ch_) __attribute__((always_inline)) { SF(4, pc) x_piece(ch_, IC<pc>{}); SE NOP1(); };
  STAMP(21);
  x_chunk(IC<0>{}); x_chunk(IC<1>{});
  const unsigned trY = tq * 128 + lg * 32 + tp * 8, trX = tq * 128 + (lg >> 1) * 64 + tp * 16 + (lg & 1) * 8;
  unsigned dst1[3], d2, ta, tb, dc; v4u sel;
  {
    const unsigned s1 = (li == (lg & 1)) ? 0x3f803f80u : 0u;
    sel = v4u{s1, s1, s1, s1};
    const int cg = 2 * w + (lg & 1);
    SF(3, k)
      const int ol = 2 * k + (lg >> 1), oc = 5 * r + ol, ks = oc >> 2;
      const int fr = cg < 5 ? ks * 5 + cg : 95 + ks * 3 + cg;
      dst1[k] = ol < 5 ? (unsigned)(fr * 1024 + (oc & 3) * 256 + li * 16) : OOB;
      pin(dst1[k]);
    SE
    const int oc = 32 * (w < 3 ? w : lg) + pp;
    d2 = oc < 80 ? (unsigned)((oc >> 2) * 5120 + (oc & 3) * 256 + (w < 3 ? lg : 4) * 1024 + li * 16) : OOB;
    ta = SLOT + ((lg >> 1) * 8 + tq) * 256 + (2 * w + (lg & 1)) * 32 + tp * 8; tb = 2 * SLOT + ldsw + (w < 3 ? trX : trY); dc = lane < 16 ? (unsigned)(16 * w + li) * 8 : OOB;
    pin(sel); pin(d2); pin(ta); pin(tb); pin(dc);
  }
  x_chunk(IC<2>{});
  STAMP(22);
  unsigned fl_r = tid == 0 ? (unsigned)(4 * r) : OOB, fl_c = tid == 0 ? (unsigned)(4 * c) : OOB;
  pin(fl_r); pin(fl_c);
  unsigned v3, s4[2], d4[2], v4y, d3, d3s, d4y;
  {
    const int ps = 4 * (w >> 1) + (dch >> 1);
    const unsigned fa = (6 * c + ps) * 32, fb = c < 12 ? (96 + c) * 32 : OOB;
    v3 = (ps < 6 ? fa : ps == 6 ? fb : OOB) + (40 * r + drow) * 3456 + 16 * (dch & 1);
    const int pd = 4 * (w >> 1) + lg;
    d3s = pd < 3 ? 2048u : 0u;
    d3 = (pd < 7 ? (unsigned)(pd * (pd < 3 ? 1024 : 40960)) : OOB) + (w & 1) * 256 + li * 16;
    pin(v3); pin(d3); pin(d3s);
  }
  x_chunk(IC<3>{});
  {
    SF(2, k)
      const int oc = 32 * (2 * w + k) + pp, nn = oc >> 2, g = oc & 3;
      const int ft = (w < 3 ? 6 * (nn & 15) + 2 * w : 2 * nn) + (g >> 1);
      const unsigned sv = (16 * ft + 8 * (g & 1)) * 1280 + 160 * j, dv = WS_W4T + j * 286720 + nn * 5120 + g * 256;
      s4[k] = oc < 216 ? sv : OOB; d4[k] = oc < 216 ? dv : OOB;
    SE
    v4y = (dch < 2 ? s4[0] : dch < 4 ? s4[1] : OOB) + drow * 1280 + 128 + (dch & 1) * 16;
    d4y = (lg == 0 ? d4[0] : lg == 1 ? d4[1] : OOB) + 4096 + li * 16;
    pin(v4y); pin(d4y);
  }
  unsigned tag, vtag;
  {
    STAMP(23);
    asm volatile("s_waitcnt vmcnt(16)" : "+v"(epoch), "+v"(cpf)::"memory");
    BARRIER();
    STAMP(24);
    SF(3, k) tr_a<160 + 4 * k, k * 4096, 1024>(ta); SE
    tr_a<172, 3072>(tb);
    tag = __builtin_amdgcn_readfirstlane(epoch) * 1664525u + 1013904223u;
    vtag = tag; pin(vtag);
    f32x4 cs;
    WAITL(2);
    SF(3, k) mf16_va<k == 0, 160 + 4 * k>(cs, sel); SE
    SF(3, k) st_a<160 + 4 * k>(dst1[k], ws, WS_W1T + c * 163840); SE
    WAITL(0);
    st_a<172>(d2, ws, WS_W2T + j * 102400);
    asm volatile("s_nop 3" : "+v"(cs));
    const float c0 = cs[0], c1 = cs[1];
    st64c(v2u{__builtin_bit_cast(unsigned, c0), __builtin_bit_cast(unsigned, c1)}, dc, ws, WS_CS1 + (c * 16 + r) * 512);
  }
  STAMP(25);
  x_chunk(IC<4>{});
  unsigned xa0 = xst + li * 128 + ((lg ^ (li >> 1)) * 16), xa1 = xst + li * 128 + (((4 + lg) ^ (li >> 1)) * 16);
  auto x_read = [&](auto ch_) __attribute__((always_inline)) {
    constexpr int ch = decltype(ch_)::value;
    dsr128a<(4 * ch + 0) * 4, (ch % 5) * 4096>(xa0);
    dsr128a<(4 * ch + 1) * 4, (ch % 5) * 4096 + 2048>(xa0);
    dsr128a<(4 * ch + 2) * 4, (ch % 5) * 4096>(xa1);
    dsr128a<(4 * ch + 3) * 4, (ch % 5) * 4096 + 2048>(xa1);
  };
  v4u ones = {0x3f803f80u, 0x3f803f80u, 0x3f803f80u, 0x3f803f80u};
  unsigned pv2 = flag_off(0, (lane & 16) ? (c ^ 4) : c) + li * 4;
  pin(ones); pin(pv2);
  f32x4 gr[2], sm[2];
  auto x_stat1 = [&](auto ch_, auto q_) __attribute__((always_inline)) {
    constexpr int ch = decltype(ch_)::value, q = decltype(q_)::value;
    mf16_aa<(ch == 0 && q < 2), (4 * ch + q) * 4, (4 * ch + q) * 4>(gr[q & 1]);
    mf16_va<(ch == 0 && q < 2), (4 * ch + q) * 4>(sm[q & 1], ones);
  };
  const unsigned w1src = WS_W1T + c * 163840 + ldsw, w3src = WS_W3T + c * 286720 + ldsw;
  unsigned pf;
  auto x_step = [&](auto k_) __attribute__((always_inline)) {
    constexpr int k = decltype(k_)::value;
    WAITL(4);
    SF(4, q) x_stat1(IC<k>{}, IC<q>{}); if constexpr (k < 5) x_piece(IC<5 + k>{}, IC<q>{}); SE
  };
  WAITV(17); x_read(IC<0>{}); x_read(IC<1>{});
  x_step(IC<0>{});
  STAMP(26);
  WAITV(8);
  BARRIER();
  st32c(vtag, fl_r, ws, flag_off(F_W1, c));
  STAMP(1);
  x_read(IC<2>{}); x_step(IC<1>{});
  x_read(IC<3>{}); x_step(IC<2>{});
  WAITV(13); x_read(IC<4>{}); x_step(IC<3>{});
  WAITV(13); x_read(IC<5>{}); x_step(IC<4>{});
  ld32c<0>(pf, pv2, ws, F_W1 * 4096);
  {
    const v4u z4 = {0u, 0u, 0u, 0u};
    *(LAS v4u*)(L + L_KVT + tid * 16) = z4;
    if (tid < 16) *(LAS v4u*)(L + L_KVT + 4096 + tid * 16) = z4;
  }
  STAMP(2);
  WAITV(13); x_read(IC<6>{}); x_step(IC<5>{});
  WAITV(9); x_read(IC<7>{});
  set_m0<0>(ldsw);
  x_step(IC<6>{});
  WAITV(5); x_read(IC<8>{}); x_step(IC<7>{});
  poll_chk<0>(pf, ws, pv2, F_W1 * 4096, tag, budget);
  STAMP(3);
  x_read(IC<9>{});
  SF(5, f) unit_dma<0, f, slot_of(1)>(ws, lane16, lane16b, w1src, ldsw); if constexpr (f >= 3) NOP1(); SE
  WAITL(0);
  SF(2, k) SF(4, q)
    x_stat1(IC<8 + k>{}, IC<q>{});
    constexpr int i = 4 * k + q;
    if constexpr (i < 5) unit_dma<1, i, slot_of(2)>(ws, lane16, lane16b, w1src + SLOT, ldsw);
    else unit_dma<2, i - 5, -1>(ws, lane16, lane16b, w1src + 2 * SLOT, ldsw);
  SE SE
  unit_dma<2, 3, -1>(ws, lane16, lane16b, w1src + 2 * SLOT, ldsw); NOP1();
  unit_dma<2, 4, slot_of(3)>(ws, lane16, lane16b, w1src + 2 * SLOT, ldsw);
  const unsigned lb0 = lane16, lb1 = lane16 + 3 * SLOT, lb2 = lane16 + 6 * SLOT;
  auto rd = [&](auto fr_, v4u& d) __attribute__((always_inline)) {
    constexpr int fr = decltype(fr_)::value, sl = (fr / 20) % 7, off = (sl % 3) * SLOT + (fr % 20) * 1024;
    dsr128<off>(d, sl < 3 ? lb0 : sl < 6 ? lb1 : lb2);
  };
  f32x4 akv[5][2], aq[3][2], enb[2], era[2];
  float ecs[5];
  const unsigned v4x = drow * 1280 + dch * 16, d4x = lg * 1024 + li * 16;
  const rsrc_t rw3 = mk((w & 1) ? ptr2(ka[8], ka[9]) : ptr2(ka[6], ka[7]), 640 * 1728 * 2);
  const rsrc_t rw4 = mk((const char*)ptr2(ka[10], ka[11]) - 2048, 1728 * 640 * 2 + 2048);
  {
    v4u bf[8];
    unsigned cs1p[8];
    unsigned vc1 = (lane >> 5) * 4096 + (32 * w + (lane & 31)) * 4;
    unsigned ea = L_MEAN + (32 * w + 4 * lg) * 4, ec = L_CS1 + li * 4;
    pin(vc1); pin(ea); pin(ec);
    WAITV(10);
    BARRIER();
    STAMP(4);
    SF(6, f) rd(IC<f>{}, bf[f]); SE
    SF(100, f)
      constexpr int ks = f / 5, t = f % 5;
      if constexpr (f % 20 == 14 && f < 80) { WAITV(f == 14 ? 14 : f == 34 ? 19 : f == 54 ? 16 : 15); BARRIER(); }
      if constexpr (!(f & 1)) WL(f + 6 <= 100 ? 4 : f == 96 ? 6 : 9);
      mf16_av<ks == 0, (2 * ks) * 4>(akv[t][0], bf[f & 7]);
      if constexpr (f + 6 < 100) rd(IC<f + 6>{}, bf[(f + 6) & 7]);
      mf16_av<ks == 0, (2 * ks + 1) * 4>(akv[t][1], bf[f & 7]);
      if constexpr (f % 3 == 1 && f < 75) { constexpr int i = f / 3; unit_dma<3 + i / 5, i % 5, slot_of(4 + i / 5)>(ws, lane16, lane16b, w1src + (3 + i / 5) * SLOT, ldsw); }
      if constexpr (f >= 76 && f % 3 == 1) {
        constexpr int k = (f - 76) / 3;
        if constexpr (k < 5) dma<(k < 4 ? k : 3) * 1024, false, (k == 3 ? (int)SLOT + 1024 : k == 4 ? 2 * (int)SLOT : -1)>(v3, rw3, k * 8 * 3456 - (k < 4 ? k : 3) * 1024, ldsw);
        else if constexpr (k == 5) dma<0, false, -1>(v4x, rw4, s4[0] + 2048, 0);
        else if constexpr (k == 6) dma<1024, false, -1>(v4x, rw4, s4[1] + 1024, 0);
        else dma<2048, false, slot_of(8)>(v4y, rw4, 0, ldsw);
      }
      if constexpr (f % 3 == 2 && f < 24) ld32c<(f / 3) * 512>(cs1p[f / 3], vc1, ws, WS_CS1 + c * 8192);
      if constexpr (f == 27) {
    float sta[2], stb[2];
    SF(2, m)
      pin(gr[m]); pin(sm[m]);
      const float sq = sel4(gr[m], tp);
      const float mean = sm[m][0] * (1.f / 640.f);
      const float rs = __builtin_amdgcn_rsqf(fmaxf(sq * (1.f / 640.f) - mean * mean, 0.f) + 1e-5f);
      stb[m] = -mean * rs; sta[m] = rs;
    SE
    if (lg == tq) {
      SF(2, m) *(LAS float*)(L + L_MEAN + (32 * w + 16 * m + li) * 4) = stb[m]; *(LAS float*)(L + L_RSTD + (32 * w + 16 * m + li) * 4) = sta[m]; SE
    }
      }
      if constexpr (f == 94) { dsr128f<0>(enb[0], ea); dsr128f<512>(era[0], ea); }
      if constexpr (f == 95) { dsr128f<64>(enb[1], ea); dsr128f<576>(era[1], ea); }
      if constexpr (f == 96) { dsr32f<0>(ecs[0], ec); dsr32f<64>(ecs[1], ec); dsr32f<128>(ecs[2], ec); }
      if constexpr (f == 97) { dsr32f<192>(ecs[3], ec); dsr32f<256>(ecs[4], ec); }
      if constexpr (f == 56) {
        WAITV(11);
        SF(8, k) pin(cs1p[k]); SE
        const float s = xsum32(((asf(cs1p[0]) + asf(cs1p[1])) + (asf(cs1p[2]) + asf(cs1p[3]))) + ((asf(cs1p[4]) + asf(cs1p[5])) + (asf(cs1p[6]) + asf(cs1p[7]))));
        *(LAS float*)(L + L_CS1 + (32 * w + (lane & 1) * 16 + ((lane & 31) >> 1)) * 4) = s;
      }
    SE
  }
  STAMP(5);
  const unsigned w2src = WS_W2T + j * 102400 + w * 25600;
  auto ldw2 = [&](auto i_) __attribute__((always_inline)) {
    constexpr int i = decltype(i_)::value;
    ld_a<(i < 24 ? 160 + 4 * i : 0), (i & 3) * 1024>(lane16, ws, w2src + (i >> 2) * 4096);
  };
  {
    v4u fr[5];
    unsigned ib = L_RED + w * 3360 + (5 * li + lg) * 16, iz = L_RED + w * 3360 + (lg ? 204 + lg : 104) * 16;
    pin(ib); pin(iz);
    asm volatile("s_waitcnt lgkmcnt(0)\n\ts_nop 5" : "+v"(enb[0]), "+v"(enb[1]), "+v"(era[0]), "+v"(era[1]), "+v"(ecs[0]), "+v"(ecs[1]), "+v"(ecs[2]), "+v"(ecs[3]), "+v"(ecs[4])::"memory");
    FENCE();
    SF(5, t)
      float v[2][4];
      const float eps = t < 2 ? 1e-6f : (t == 2 && li < 8) ? 1e-6f : 0.f;
      SF(2, m)
        if constexpr (t < 3) ldw2(IC<2 * t + m>{});
        pin(akv[t][m]);
        SF(4, e)
          float x = fmaf(akv[t][m][e], era[m][e], fmaf(enb[m][e], ecs[t], eps));
          if constexpr (t < 2) x = phi1(x);
          if constexpr (t == 2) { const float y = phi1(x); x = li < 8 ? y : x; }
          v[m][e] = x;
        SE
        fr[t][2 * m] = cvtpk(v[m][0], v[m][1]); fr[t][2 * m + 1] = cvtpk(v[m][2], v[m][3]);
      SE
      if constexpr (t < 3) pin(fr[t]);
    SE
    f32x4 dd[10];
    asm volatile("s_nop 1\n\t"
                 "v_mfma_f32_16x16x32_bf16 %7, %10, %15, 0\n\tv_mfma_f32_16x16x32_bf16 %8, %11, %15, 0\n\tv_mfma_f32_16x16x32_bf16 %9, %12, %15, 0\n\t"
                 "v_mfma_f32_16x16x32_bf16 %4, %11, %12, 0\n\tv_mfma_f32_16x16x32_bf16 %6, %12, %12, 0\n\t"
                 "v_mfma_f32_16x16x32_bf16 %0, %10, %13, 0\n\tv_mfma_f32_16x16x32_bf16 %2, %11, %13, 0\n\t"
                 "v_mfma_f32_16x16x32_bf16 %1, %10, %14, 0\n\tv_mfma_f32_16x16x32_bf16 %3, %11, %14, 0\n\tv_mfma_f32_16x16x32_bf16 %5, %12, %14, 0"
                 : "=&v"(dd[0]), "=&v"(dd[1]), "=&v"(dd[2]), "=&v"(dd[3]), "=&v"(dd[4]), "=&v"(dd[5]), "=&v"(dd[6]), "=&v"(dd[7]), "=&v"(dd[8]), "=&v"(dd[9])
                 : "v"(fr[0]), "v"(fr[1]), "v"(fr[2]), "v"(fr[3]), "v"(fr[4]), "v"(ones));
    FENCE();
    dsw_kv<0, 3, 100 * 16>(dd[7], ib); dsw_kv<1, 3, 0>(dd[8], iz); dsw_kv<2, 3, 208 * 16>(dd[9], ib);
    dsw_kv<1, 2, 124 * 16>(dd[4], ib); dsw_kv<2, 2, 128 * 16>(dd[6], ib);
    dsw_kv<0, 0, 0>(dd[0], ib); dsw_kv<1, 0, 4 * 16>(dd[2], ib);
    dsw_kv<0, 1, 80 * 16>(dd[1], ib); dsw_kv<1, 1, 84 * 16>(dd[3], ib); dsw_kv<2, 1, 88 * 16>(dd[5], ib);
    asm volatile("s_mov_b64 exec, -1" ::: "memory");
    FENCE();
    ldw2(IC<6>{}); ldw2(IC<7>{});
    unsigned ra = L_RED + tid * 16, gv0 = tid < 210 ? (unsigned)tid * 16 : OOB;
    pin(ra); pin(gv0);
    WAITV(16);
    BARRIER_L();
    f32x4 p[4];
    SF(4, k) dsr128f<k * 3360>(p[k], ra); SE
    asm volatile("s_waitcnt lgkmcnt(0)" : "+v"(p[0]), "+v"(p[1]), "+v"(p[2]), "+v"(p[3])::"memory");
    st128c(__builtin_bit_cast(v4u, (p[0] + p[1]) + (p[2] + p[3])), gv0, ws, WS_KVP + (c * 16 + r) * 4096);
  }
  STAMP(6);
  v4u pq[2][2];
  v4u xr[2]; v2u xr4;
  unsigned pvk = flag_off(F_KV, c) + li * 4;
  {
    v4u bf[8];
    SF(6, f) rd(IC<100 + f>{}, bf[f]); SE
    unsigned xo = ((128 * r + 64 * hf + 16 * w + li) * 640 + 80 * j) * 2;
    unsigned xo16 = xo + lg * 16, xo8 = xo + lg * 8;
    pin(xo16); pin(xo8); pin(pvk);
    f32x4 cs3;
    unsigned t3 = SLOT + ldsw + tq * 128 + lg * 32 + (2 * (tp & 1) + (tp >> 1)) * 8, t4x = 2 * SLOT + ldsw + trX, t4y = 2 * SLOT + ldsw + trY;
    unsigned dc3 = lane < 16 ? (unsigned)(((w & 1) * 16 + li) * 32 + (w >> 1) * 16) : OOB;
    const unsigned s13 = li == lg ? 0x3f803f80u : 0u;
    v4u sel3 = {s13, s13, s13, s13};
    pin(t3); pin(t4x); pin(t4y); pin(dc3); pin(sel3);
    WAITV(9);
    SF(60, f)
      constexpr int ks = f / 3, t = f % 3;
      if constexpr (f == 16) { STAMP(30); WAITV(5); BARRIER(); st32c(vtag, fl_r, ws, flag_off(F_KV, c)); STAMP(31); }
      if constexpr (!(f & 1)) WL(f + 6 <= 60 ? 4 : 58 - f);
      mf16_va<ks == 0, (2 * ks) * 4>(aq[t][0], bf[f & 7]);
      if constexpr (f < 16 && !(f & 1)) {
        constexpr int k = f / 2;
        if constexpr (k < 5) tr_a<224 + 4 * k, k * 1024>(t3);
        else tr_a<224 + 4 * k, (k - 5) * 1024>(k < 7 ? t4x : t4y);
      }
      if constexpr (f + 6 < 60) rd(IC<100 + f + 6>{}, bf[(f + 6) & 7]);
      mf16_va<ks == 0, (2 * ks + 1) * 4>(aq[t][1], bf[f & 7]);
      if constexpr (f >= 7 && f < 23 && (f & 1)) {
        constexpr int k = (f - 7) / 2;
        if constexpr (k < 5) { st_a<224 + 4 * k>(d3 + d3s * ((5 * r + k) >> 1), ws, WS_W3T + c * 286720 + (5 * r + k) * 512); mf16_va<k == 0, 224 + 4 * k>(cs3, sel3); }
        else if constexpr (k < 7) st_a<224 + 4 * k>(d4x, ws, d4[(k - 5) & 1]);
        else st_a<224 + 4 * k>(d4y, ws, 0);
      }
      if constexpr (f == 24) {
        pin(cs3);
        st128c(__builtin_bit_cast(v4u, cs3), dc3, ws, WS_CS3 + (c * 16 + r) * 1024);
      }
      if constexpr (f >= 26 && f % 4 == 2 && f < 58) ldw2(IC<8 + (f - 26) / 4>{});
      if constexpr (f == 32) ld128c<0>(xr[0], xo16, rx, 0);
      if constexpr (f == 41) ld128c<64>(xr[1], xo16, rx, 0);
      if constexpr (f == 50) ld64c<128>(xr4, xo8, rx, 0);
      if constexpr (f == 58) ld32c<0>(pf, pvk, ws, 0);
    SE
  }
  STAMP(7);
  const unsigned w4src = WS_W4T + j * 286720 + ldsw;
  auto pfx = [&](auto i_) __attribute__((always_inline)) {
    constexpr int i = decltype(i_)::value;
    if constexpr (i < 30) { unit_dma<8 + i / 5, i % 5, slot_of(9 + i / 5)>(ws, lane16, lane16b, w3src + (i / 5) * SLOT, ldsw); if constexpr (i % 5 >= 3) NOP1(); }
    else { constexpr int k = i - 30; ld_v<VRES + 4 * k, (k % 5 < 4 ? k % 5 : 3) * 1024>(k % 5 < 4 ? lane16 : lane16b, ws, w4src + (k / 5) * SLOT); }
  };
  f32x4 acc2[4][5];
  {
    float nb[2], ra[2]; f32x4 cs[3];
    SF(2, m) nb[m] = *(LAS float*)(L + L_MEAN + (32 * w + 16 * m + li) * 4); ra[m] = *(LAS float*)(L + L_RSTD + (32 * w + 16 * m + li) * 4); SE
    SF(3, t) cs[t] = *(LAS f32x4*)(L + L_CS1 + (80 + 16 * t + 4 * lg) * 4); SE
    asm volatile("s_nop 7" ::: "memory");
    FENCE();
    unsigned pk[2][3][2];
    auto qepi = [&](auto m_, auto t_) __attribute__((always_inline)) {
      constexpr int m = decltype(m_)::value, t = decltype(t_)::value;
      pin(aq[t][m]);
      float v[4];
      SF(4, e) v[e] = phi1(fmaf(aq[t][m][e], ra[m], fmaf(nb[m], cs[t][e], 1e-6f))); SE
      pk[m][t][0] = cvtpk(v[0], v[1]); pk[m][t][1] = cvtpk(v[2], v[3]);
      pin(pk[m][t][0]); pin(pk[m][t][1]);
    };
    SF(3, t) qepi(IC<0>{}, IC<t>{}); SE
    unsigned gv = tid < 210 ? (unsigned)tid * 16 : OOB;
    unsigned kw, k0 = L_KVT + (8 * tq + tp) * 96 + lg * 8, k2 = L_KVT + (li >= 8 ? 24 + li : 42) * 96 + lg * 8, k3 = L_KVT + (tp < 2 ? 40 + tp : 42) * 96 + lg * 8;
    {
      const int hd = tid >= 105, u = tid - 105 * hd, e = (u * 205) >> 10, dq = u - 5 * e + 5 * hd;
      kw = L_KVT + (tid >= 210 ? 43 * 96 : (e == 20 ? 40 + hd : e + 20 * hd) * 96 + dq * 8);
    }
    pin(gv); pin(kw); pin(k0); pin(k2); pin(k3);
    poll_chk<0>(pf, ws, pvk, 0, tag, budget);
    pin(xr[0]); pin(xr[1]); pin(xr4);
    STAMP(8);
    v4u g[16];
    SF(16, k)
      ld128c<0>(g[k], gv, ws, WS_KVP + (c * 16 + k) * 4096);
      if constexpr (k == 3 || k == 7 || k == 11) qepi(IC<1>{}, IC<k / 4>{});
    SE
    SF(2, m)
      pq[m][0] = v4u{pk[m][0][0], pk[m][0][1], pk[m][1][0], pk[m][1][1]};
      pq[m][1] = v4u{lg < 2 ? pk[m][2][0] : 0u, lg < 2 ? pk[m][2][1] : 0u, 0u, 0u};
      pin(pq[m][0]); pin(pq[m][1]);
    SE
    asm volatile("v_accvgpr_write_b32 a106, 0\n\tv_accvgpr_write_b32 a107, 0\n\tv_accvgpr_write_b32 a114, 0\n\tv_accvgpr_write_b32 a115, 0\n\t"
                 "v_accvgpr_write_b32 a122, 0\n\tv_accvgpr_write_b32 a123, 0\n\tv_accvgpr_write_b32 a130, 0\n\tv_accvgpr_write_b32 a131, 0");
    STAMP(9);
    f32x4 s;
    SF(4, q)
      ldw2(IC<16 + q>{});
      WAITV(13 - 3 * q);
      SF(4, k) pin(g[4 * q + k]); SE
      const f32x4 t4 = (__builtin_bit_cast(f32x4, g[4 * q]) + __builtin_bit_cast(f32x4, g[4 * q + 1])) + (__builtin_bit_cast(f32x4, g[4 * q + 2]) + __builtin_bit_cast(f32x4, g[4 * q + 3]));
      if constexpr (q == 0) s = t4; else s += t4;
      pin(s);
    SE
    *(LAS v2u*)(L + kw) = v2u{cvtpk(s[0], s[1]), cvtpk(s[2], s[3])};
    BARRIER_L();
    st32c(vtag, fl_r, ws, flag_off(F_W4, c));
    STAMP(10);
    SF(4, t1) constexpr int t = (t1 + 3) & 3; SF(3, p) dsr64a<100 + 8 * t + 2 * p, (t == 1 ? 4 * 96 : 0) + p * 32>(t < 2 ? k0 : t == 2 ? k2 : k3); SE SE
    f32x4 od[2][4];
    SF(4, t1)
      constexpr int t = (t1 + 3) & 3;
      WL(9 - 3 * t1);
      SF(2, m) mf16_av<true, 100 + 8 * t>(od[m][t], pq[m][0]); SE
      SF(2, m) mf16_av<false, 104 + 8 * t>(od[m][t], pq[m][1]); SE
      ldw2(IC<20 + t1>{});
    SE
    asm volatile("s_nop 7");
    FENCE();
    SF(2, m)
      SF(4, t) pin(od[m][t]); SE
      const float r0 = __builtin_amdgcn_rcpf(od[m][3][0] + 1e-6f), r1 = __builtin_amdgcn_rcpf(od[m][3][1] + 1e-6f);
      const float ra = lg < 3 ? r0 : r1, rb = lg < 2 ? r0 : r1;
      const v4u o = {cvtpk(od[m][0][0] * ra, od[m][0][1] * ra), cvtpk(od[m][0][2] * ra, od[m][0][3] * ra),
                     cvtpk(od[m][1][0] * rb, od[m][1][1] * rb), cvtpk(od[m][1][2] * rb, od[m][1][3] * rb)};
      const v2u o2 = {cvtpk(od[m][2][0] * r1, od[m][2][1] * r1), cvtpk(od[m][2][2] * r1, od[m][2][3] * r1)};
      const unsigned ob = WS_O + (8 * r + 2 * w + m) * 20480;
      st128c(o, lane16, ws, ob + c * 1024);
      st64c(o2, lg >= 2 ? (unsigned)(li * 16 + (lg - 2) * 8) : OOB, ws, ob + (16 + (c >> 2)) * 1024 + (c & 3) * 256);
    SE
    ld32c<0>(pf, pv2, ws, F_W4 * 4096);
    ldw2(IC<24>{});
    SF(5, t)
      const unsigned x0 = t < 4 ? xr[(t >> 1) & 1][(t & 1) * 2] : xr4[0], x1 = t < 4 ? xr[(t >> 1) & 1][(t & 1) * 2 + 1] : xr4[1];
      acc2[3][t] = f32x4{bflo(x0), bfhi(x0), bflo(x1), bfhi(x1)};
      pin(acc2[3][t]);
    SE
    WAITV(2);
    BARRIER();
    st32c(vtag, fl_c, ws, flag_off(F_O, r));
    STAMP(11);
    poll_chk<2>(pf, ws, pv2, F_W4 * 4096, tag, budget);
    SF(14, k) pfx(IC<k>{}); SE
    poll_w0(ws, flag_off(F_O, r) + li * 4, tag, budget, w);
    st32c(vtag, (b == 0 && tid == 0) ? 0u : OOB, ws, 0);
    BARRIER();
  }
  STAMP(12);
  const int mt0 = 8 * r + 4 * hf;
  const bool dead = w >= 2;
  auto ldw4a = [&](auto i_) __attribute__((always_inline)) {
    constexpr int i = decltype(i_)::value;
    ld_a<160 + 4 * i, (i % 5 < 4 ? i % 5 : 3) * 1024>(i % 5 < 4 ? lane16 : lane16b, ws, (i >= 15 && dead) ? OOB : w4src + (10 + i / 5) * SLOT);
  };
  v4u hp[2]; v2u hp4;
  const unsigned pb0 = lb0 + ldsw, pb1 = lb1 + ldsw, pb2 = lb2 + ldsw;
  {
    unsigned cs3p[16];
    auto ldo = [&](auto i_) __attribute__((always_inline)) {
      constexpr int i = decltype(i_)::value, mm = i / 5, kk = i % 5;
      ld_a<4 + i * 4, (kk < 4 ? kk : 3) * 1024>(kk < 4 ? lane16 : lane16b, ws, WS_O + (mt0 + ((w + mm + 1) & 3)) * 20480 + ldsw);
    };
    constexpr int UP = 10;
    SF(UP, i) ldo(IC<i>{}); SE
    f32x4 pr[3][5];
    unsigned rb[3];
    SF(3, mm) rb[mm] = slot_of(12 + mm) + ((w + 3 - mm) & 3) * 5120 + lane16; pin(rb[mm]); SE
    SF(20, i)
      constexpr int mm = i / 5, kk = i % 5;
      WAITV(i + UP <= 20 ? UP - 1 : 19 - i + (i - 10));
      if constexpr (i == 17) BARRIER_L();
      SF(5, t)
        constexpr int fb = kk * 5 + t;
        mf16_aa<(kk == 0 && mm < 3), (fb < 24 ? 160 + 4 * fb : 0), 4 + i * 4>(acc2[mm][t]);
        if constexpr (t == 0 && i + UP < 20) ldo(IC<i + UP>{});
        if constexpr (i >= 10 && t == 0) pfx(IC<(i < 16 ? 14 + i - 10 : 30 + i - 16)>{});
        if constexpr (mm > 0 && kk == 1) {
          constexpr int sl = (12 + mm - 1) % 7;
          dsw128f<(sl % 3) * SLOT + t * 1024>(acc2[mm - 1][t], sl < 3 ? pb0 : sl < 6 ? pb1 : pb2);
        }
        if constexpr (i == 17) { dsr128f<t * 1024>(pr[0][t], rb[0]); dsr128f<t * 1024>(pr[1][t], rb[1]); }
        if constexpr (i == 18) dsr128f<t * 1024>(pr[2][t], rb[2]);
        if constexpr (i == 19) { WL(4 - t); pin(pr[0][t]); pin(pr[1][t]); pin(pr[2][t]); pr[0][t] = (pr[0][t] + pr[1][t]) + pr[2][t]; pin(pr[0][t]); }
      SE
    SE
    STAMP(13);
    asm volatile("s_nop 3");
    f32x4 v[5];
    SF(5, t) pin(acc2[3][t]); v[t] = pr[0][t] + acc2[3][t]; SE
    SF(2, k) hp[k] = v4u{cvtpk(v[2 * k][0], v[2 * k][1]), cvtpk(v[2 * k][2], v[2 * k][3]), cvtpk(v[2 * k + 1][0], v[2 * k + 1][1]), cvtpk(v[2 * k + 1][2], v[2 * k + 1][3])}; SE
    hp4 = v2u{cvtpk(v[4][0], v[4][1]), cvtpk(v[4][2], v[4][3])};
    const v4u h4 = {hp4.x, hp4.y, 0u, 0u};
    f32x4 g2, s2;
    mf16_vv<true>(g2, hp[0], hp[0]); mf16_vv<true>(s2, ones, hp[0]);
    mf16_vv<false>(g2, hp[1], hp[1]); mf16_vv<false>(s2, ones, hp[1]);
    mf16_vv<false>(g2, h4, h4); mf16_vv<false>(s2, ones, h4);
    const int mt = mt0 + w;
    const unsigned hb = WS_H + (mt >> 1) * 40960 + 5 * j * 1024 + (mt & 1) * 256;
    const unsigned hv = (lg >> 1) * 1024 + (lg & 1) * 512 + li * 16;
    st128c(hp[0], hv, ws, hb); st128c(hp[1], hv, ws, hb + 2048);
    st64c(hp4, (lg >> 1) * 512 + li * 16 + (lg & 1) * 8, ws, hb + 4096);
    asm volatile("s_nop 1" : "+v"(g2), "+v"(s2));
    const float sq = sel4(g2, tp), sv = s2[0];
    st64c(v2u{__builtin_bit_cast(unsigned, sv), __builtin_bit_cast(unsigned, sq)}, lg == tq ? (unsigned)li * 8 : OOB, ws, WS_HS + (j * 2048 + 16 * mt) * 8);
    SF(16, k) ld32c<(k & 3) * 1024>(cs3p[k], (unsigned)tid * 4, ws, WS_CS3 + c * 16384 + (k >> 2) * 4096); SE
    SF(4, k) pfx(IC<34 + k>{}); SE
    WAITV(20);
    BARRIER();
    st32c(vtag, fl_c, ws, flag_off(F_H, r));
    STAMP(14);
    SF(10, k) pfx(IC<20 + k>{}); SE
    SF(2, k) pfx(IC<38 + k>{}); SE
    {
      WAITV(17);
      SF(16, k) pin(cs3p[k]); SE
      float q4[4];
      SF(4, k) q4[k] = (asf(cs3p[4 * k]) + asf(cs3p[4 * k + 1])) + (asf(cs3p[4 * k + 2]) + asf(cs3p[4 * k + 3])); SE
      unsigned c1, c2, c3;
      split3((q4[0] + q4[1]) + (q4[2] + q4[3]), c1, c2, c3);
      *(LAS v4u*)(L + L_RED + ((tid & 7) * 32 + (tid >> 3)) * 16) = v4u{c1 | (c1 >> 16), c2 | (c2 >> 16), c3 | (c1 >> 16), c3 | (c2 >> 16)};
    }
    poll_w0(ws, flag_off(F_H, r) + li * 4, tag, budget, w);
    BARRIER();
  }
  STAMP(15);
  unsigned va[4];
  auto lda = [&](auto u_, auto mm_) __attribute__((always_inline)) {
    constexpr int u = decltype(u_)::value, mm = decltype(mm_)::value;
    ld_a<areg(u, mm), 0>(va[mm], ws, (u == 13 && dead) ? OOB : WS_ACT + u * 4096);
  };
  unsigned pa = flag_off(F_A0, r) + li * 4;
  {
    v2u hs[4];
    const unsigned hsrc = WS_H + (4 * r + w) * 40960;
    SF(4, s) ld_a<4 * s, (s & 3) * 1024>(lane16, ws, hsrc); SE
    v4u bf[6];
    SF(4, k) rd(IC<160 + k>{}, bf[k]); SE
    SF(4, k) ld64c<0>(hs[k], (unsigned)(((lane >> 5) * 4 * 2048 + (lane & 31)) * 8), ws, WS_HS + (k * 2048 + 128 * r + 32 * w) * 8); SE
    SF(P3_HUP - 4, s) ld_a<4 * (s + 4), (s & 3) * 1024>(lane16, ws, hsrc + ((s + 4) >> 2) * 4096); SE
    f32x16 acc[4];
    v4u cf[3], mf;
    float c1, kk2, eP[2], eT[2], eO[8];
    unsigned ov[4];
    unsigned csa = L_RED + (lane & 31) * 16;
    unsigned av = ((lane >> 4) & 1) * 57344 + (lane >> 5) * 256 + li * 16;
    SF(4, mm) va[mm] = lane16 + (mt0 + ((w + mm + 1) & 3)) * 57344 + w * 1024; pin(va[mm]); SE
    pin(csa); pin(av); pin(pa);
    const unsigned ab = WS_ACT + (8 * r + 2 * w) * 57344;
    auto epi = [&](auto p_, auto q_) __attribute__((always_inline)) {
      constexpr int p = decltype(p_)::value, q = decltype(q_)::value;
      f32x16& a = acc[p % 4];
      if constexpr (q < 24) {
        constexpr int e = q / 3, ph = q % 3, x = e & 1, y = x ^ 1;
        const float ag = a[e], au = a[8 + e];
        if constexpr (ph == 0) {
          if constexpr (e == 0) asm volatile("v_mul_f32 %0, %1, %2" : "=v"(eT[x]) : "v"(ag), "v"(c1));
          else asm volatile("v_rcp_f32 %1, %1\n\tv_mul_f32 %0, %2, %3" : "=v"(eT[x]), "+v"(eT[y]) : "v"(ag), "v"(c1));
        } else if constexpr (ph == 1) {
          if constexpr (e == 0) asm volatile("v_exp_f32 %0, %0" : "+v"(eT[x]));
          else asm volatile("v_exp_f32 %0, %0\n\tv_mul_f32 %1, %2, %3" : "+v"(eT[x]), "=v"(eO[e - 1]) : "v"(eP[y]), "v"(eT[y]));
        } else {
          if constexpr (e >= 2 && !(e & 1)) asm volatile("v_mul_f32 %0, %3, %4\n\tv_fma_f32 %1, %1, %5, %5\n\tv_cvt_pk_bf16_f32 %2, %6, %7" : "=&v"(eP[x]), "+v"(eT[x]), "=v"(ov[(e - 2) >> 1]) : "v"(ag), "v"(au), "v"(kk2), "v"(eO[e - 2]), "v"(eO[e - 1]));
          else asm volatile("v_mul_f32 %0, %2, %3\n\tv_fma_f32 %1, %1, %4, %4" : "=&v"(eP[x]), "+v"(eT[x]) : "v"(ag), "v"(au), "v"(kk2));
        }
      } else if constexpr (q == 24) asm volatile("v_rcp_f32 %0, %0\n\ts_nop 0" : "+v"(eT[1]));
      else if constexpr (q == 25) asm volatile("v_mul_f32 %1, %1, %2\n\tv_cvt_pk_bf16_f32 %0, %3, %1" : "=v"(ov[3]), "+v"(eP[1]) : "v"(eT[1]), "v"(eO[6]));
      else if constexpr (q == P3_EPI_ST) {
        unsigned so;
        if constexpr (p == 6) so = c < 12 ? ab + (48 + (c >> 1)) * 1024 + (c & 1) * 512 : OOB;
        else so = ab + (16 * (p >> 1) + c) * 1024 + (p & 1) * 512;
        st128c(v4u{ov[0], ov[1], ov[2], ov[3]}, av, ws, so);
      }
    };
    SF(280, g)
      constexpr int p = g < 120 ? g % 3 : g / 40, s = g < 120 ? g / 3 : g % 40;
      if constexpr (g == 120 || g == 200) STAMP(g == 120 ? 28 : 29);
      if constexpr (g == P3_CHK_A0) poll_chk<p3_cnt(P3_POLL_A0, g)>(pf, ws, pa, 0, tag, budget);
      if constexpr (g % 20 == 16) {
        constexpr int nu = 8 + g / 20 + 1;
        if constexpr (g == P3_FL0) WAITV(p3_cnt(p3_st_step(1), g));
        else if constexpr (g == P3_FL1) WAITV(p3_cnt(p3_st_step(3), g));
        else if constexpr (g == P3_FL2) WAITV(p3_cnt(p3_st_step(5), g));
        else if constexpr (nu >= 14 && nu < 22) WAITV(p3_cnt(p3_dma_start(nu) + 12, g));
        BARRIER();
      }
      if constexpr (g < 120 && g % 3 == 0) {
        if constexpr (s < 4) WAITV(P3_HUP + 3 - s + p3_cnt(-1, g));
        else if constexpr (s < P3_HUP) WAITV(P3_HUP - 1 - s + p3_cnt(-1, g));
        else WAITV(p3_cnt(2 * (s - P3_HUP) + 1, g));
      }
      WL(g + 4 < 280 ? 3 : 279 - g);
      mf32_va<s == 0, 4 * s>(acc[p % 4], bf[g % 6]);
      if constexpr (s == 34) dsr128<p * 512>(cf[p % 3], csa);
      if constexpr (g + 4 < 280) rd(IC<160 + g + 4>{}, bf[(g + 4) % 6]);
      if constexpr (g == P3_FL0) st32c(vtag, fl_c, ws, flag_off(F_A0, r));
      if constexpr (g == P3_FL1) st32c(vtag, fl_c, ws, flag_off(F_A1, r));
      if constexpr (g == P3_FL2) st32c(vtag, fl_c, ws, flag_off(F_A2, r));
      if constexpr (p3_hload(g)) { constexpr int hsx = (g >> 1) + P3_HUP; ld_a<4 * hsx, (hsx & 3) * 1024>(lane16, ws, hsrc + (hsx >> 2) * 4096); }
      if constexpr (p3_dma_at(g) >= 0) {
        constexpr int nu = p3_dma_at(g) / 8, f = p3_dma_at(g) % 8;
        unit_dma<nu, f, slot_of(nu + 1)>(ws, lane16, lane16b, nu < 22 ? w3src + (nu - 8) * SLOT : w4src + (nu - 18) * SLOT, ldsw);
      }
      if constexpr (p3_w4a_at(g) >= 10) ldw4a(IC<p3_w4a_at(g) - 10>{});
      else if constexpr (p3_w4a_at(g) >= 0) pfx(IC<40 + p3_w4a_at(g)>{});
      if constexpr (p3_aload(g)) lda(IC<(g - P3_AL0) / 8>{}, IC<((g - P3_AL0) / 2) % 4>{});
      if constexpr (g == P3_POLL_A0) ld32c<0>(pf, pa, ws, 0);
      if constexpr (g == P3_POLL_A1) ld32c<0>(pf, pa, ws, 4096);
      if constexpr (g == 13) {
        SF(4, k) pin(hs[k]); SE
        const float s1 = xsum32((asf(hs[0].x) + asf(hs[1].x)) + (asf(hs[2].x) + asf(hs[3].x)));
        const float s2 = xsum32((asf(hs[0].y) + asf(hs[1].y)) + (asf(hs[2].y) + asf(hs[3].y)));
        const float mean = s1 * (1.f / 640.f);
        kk2 = fmaxf(s2 * (1.f / 640.f) - mean * mean, 0.f) + 1e-5f;
        c1 = -1.44269504f * __builtin_amdgcn_rsqf(kk2);
        unsigned m1, m2, m3;
        split3(lane < 32 ? -mean : 0.f, m1, m2, m3);
        mf = v4u{m2 | (m1 >> 16), m2 | (m1 >> 16), m1 | (m3 >> 16), m2 | (m3 >> 16)};
        pin(kk2); pin(c1); pin(mf);
      }
      SF(5, ep) if constexpr (g >= p3_epi_start(ep) && g <= p3_epi_start(ep) + P3_EPI_ST) epi(IC<ep>{}, IC<g - p3_epi_start(ep)>{}); SE
      if constexpr (g >= 246 && g < 260) { epi(IC<5>{}, IC<2 * (g - 246)>{}); epi(IC<5>{}, IC<2 * (g - 246) + 1>{}); }
      if constexpr (s == 39) mf32_vv(acc[p % 4], cf[p % 3], mf);
    SE
    STAMP(16);
    asm volatile("s_nop 11");
    {
      f32x16& a = acc[2];
      pin(a);
      float o[8];
      SF(8, e) o[e] = a[e] * a[8 + e] * __builtin_amdgcn_rcpf(fmaf(__builtin_amdgcn_exp2f(a[e] * c1), kk2, kk2)); SE
      SF(4, k) ov[k] = cvtpk(o[2 * k], o[2 * k + 1]); SE
      epi(IC<6>{}, IC<P3_EPI_ST>{});
    }
    poll_chk<1>(pf, ws, pa, 4096, tag, budget);
    STAMP(17);
    STAMP(18);
  }
  {
    f32x4 acc[4][5];
    v4u bf[2][5];
    auto rdw = [&](auto u_, auto t_) __attribute__((always_inline)) {
      constexpr int u = decltype(u_)::value, t = decltype(t_)::value, sl = (18 + u) % 7;
      dsr128<(sl % 3) * SLOT + t * 1024>(bf[u & 1][t], sl < 3 ? pb0 : sl < 6 ? pb1 : pb2);
    };
    SF(5, t)
      const unsigned x0 = t < 4 ? hp[(t >> 1) & 1][(t & 1) * 2] : hp4[0], x1 = t < 4 ? hp[(t >> 1) & 1][(t & 1) * 2 + 1] : hp4[1];
      acc[3][t] = f32x4{bflo(x0), bfhi(x0), bflo(x1), bfhi(x1)};
      pin(acc[3][t]);
    SE
    SF(11, u)
      if constexpr (u == 2) { WAITV(12); BARRIER(); st32c(vtag, fl_c, ws, flag_off(F_A3, r)); }
      if constexpr (u == 4) poll_chk<6>(pf, ws, pa, 8192, tag, budget);
      if constexpr (u == 7) WAITV(13);
      if constexpr (u == 8) WAITV(4);
      if constexpr (u == 9) poll_chk<0>(pf, ws, pa, 12288, tag, budget);
      if constexpr (u >= 4 && u < 10) WL(0);
      SF(4, mm) SF(5, t)
        if constexpr (u == 0 && mm < 3) mf16_lva0<VRES + 4 * t, areg(u, mm)>(acc[mm][t]);
        else if constexpr (u < 4) mf16_lva<VRES + 20 * u + 4 * t, areg(u, mm)>(acc[mm][t]);
        else if constexpr (u < 10) mf16_va<false, areg(u, mm)>(acc[mm][t], bf[u & 1][t]);
        else mf16_aa<false, 160 + 20 * (u - 10) + 4 * t, areg(u, mm)>(acc[mm][t]);
        constexpr int i = mm * 5 + t;
        if constexpr (u >= 3 && u < 9 && i < 10 && (i & 1)) rdw(IC<u + 1>{}, IC<i / 2>{});
        if constexpr (i == 0 && u == 3) ld32c<0>(pf, pa, ws, 8192);
        if constexpr (i == 0 && u == 8) ld32c<0>(pf, pa, ws, 12288);
        if constexpr (u < 8 && i < 12 && i % 3 == 2) lda(IC<u + 4>{}, IC<i / 3>{});
        if constexpr (u == 9 && i < 16 && !(i & 1)) lda(IC<12 + i / 8>{}, IC<(i / 2) % 4>{});
        if constexpr (u < 7 && (i == 13 || i == 16 || (i == 19 && u == 5))) {
          constexpr int k = 2 * u + (u > 5) + (i - 13) / 3;
          unit_dma<25 + k / 5, k % 5, slot_of(26 + k / 5)>(ws, lane16, lane16b, w4src + (7 + k / 5) * SLOT, ldsw);
        }
      SE SE
    SE
    f32x4 pr[3][5];
    unsigned rb[3];
    SF(3, mm) rb[mm] = mm * SLOT + ((w + 3 - mm) & 3) * 5120 + lane16; pin(rb[mm]); SE
    SF(4, mm)
      SF(5, t)
        mf16_aa<false, 180 + 4 * t, areg(11, mm)>(acc[mm][t]);
        if constexpr (mm > 0 && t >= 3) dsw128f<(mm - 1) * SLOT + (t - 3) * 1024>(acc[mm - 1][t - 3], pb0);
      SE
      if constexpr (mm == 0) WAITV(4);
      SF(5, t)
        mf16_aa<false, 200 + 4 * t, areg(12, mm)>(acc[mm][t]);
        if constexpr (mm > 0 && t < 3) dsw128f<(mm - 1) * SLOT + (t + 2) * 1024>(acc[mm - 1][t + 2], pb0);
      SE
      if constexpr (mm == 0) WAITV(0);
      if constexpr (mm == 3) BARRIER_L();
      SF(5, t)
        mf16_aa<false, 220 + 4 * t, areg(13, mm)>(acc[mm][t]);
        if constexpr (mm == 3) { SF(3, k) dsr128f<t * 1024>(pr[k][t], rb[k]); SE }
      SE
    SE
    STAMP(19);
    f32x4 v[5];
    SF(5, t)
      WL(12 - 3 * t);
      pin(pr[0][t]); pin(pr[1][t]); pin(pr[2][t]); pin(acc[3][t]);
      v[t] = ((pr[0][t] + pr[1][t]) + pr[2][t]) + acc[3][t];
    SE
    const rsrc_t ro = mk(ptr2(ka[12], ka[13]), 2048 * 640 * 2);
    const unsigned oo = ((128 * r + 64 * hf + 16 * w + li) * 640 + 80 * j) * 2;
    SF(2, k) st128p(v4u{cvtpk(v[2 * k][0], v[2 * k][1]), cvtpk(v[2 * k][2], v[2 * k][3]), cvtpk(v[2 * k + 1][0], v[2 * k + 1][1]), cvtpk(v[2 * k + 1][2], v[2 * k + 1][3])}, oo + lg * 16, ro, k * 64); SE
    st64p(v2u{cvtpk(v[4][0], v[4][1]), cvtpk(v[4][2], v[4][3])}, oo + lg * 8, ro, 128);
  }
  STAMP(20);
  asm volatile("fbk_end:");
}

extern "C" void kernel_launch(void* const* d_in, const int* in_sizes, int n_in, void* d_out, int out_size, void* d_ws, size_t ws_size, hipStream_t stream) {
  (void)in_sizes; (void)n_in; (void)out_size; (void)ws_size;
  (void)hipFuncSetAttribute((const void*)fused_block, hipFuncAttributeMaxDynamicSharedMemorySize, (int)LDS_BYTES);
  fused_block<<<256, 256, LDS_BYTES, stream>>>(d_in[0], d_in[1], d_in[2], d_in[4], d_in[5], d_in[6], d_out, (char*)d_ws);
}
```

```cpp
#include <hip/hip_runtime.h>
#include <utility>
#include <type_traits>

#define PROF 0

#define DI __device__ __forceinline__
#define LAS __attribute__((address_space(3)))
typedef unsigned v4u __attribute__((ext_vector_type(4)));
typedef unsigned v2u __attribute__((ext_vector_type(2)));
typedef float f32x2 __attribute__((ext_vector_type(2)));
typedef float f32x4 __attribute__((ext_vector_type(4)));
typedef float f32x16 __attribute__((ext_vector_type(16)));
typedef __bf16 bf16x2 __attribute__((ext_vector_type(2)));
typedef __amdgpu_buffer_rsrc_t rsrc_t;

template <int N> using IC = std::integral_constant<int, N>;
template <class Fn, int... Is> DI void sfor_impl(Fn&& f, std::integer_sequence<int, Is...>) { (f(IC<Is>{}), ...); }
template <int N, class Fn> DI void sfor(Fn&& f) { sfor_impl(f, std::make_integer_sequence<int, N>{}); }
#define SF(N, i) sfor<N>([&](auto i##_) __attribute__((always_inline)) { constexpr int i = decltype(i##_)::value;
#define SE });

constexpr unsigned WS_FLAG = 4096;
constexpr unsigned WS_PROF = 65536;
constexpr unsigned WS_W1T = 262144;
constexpr unsigned WS_CS1 = WS_W1T + 16 * 163840;
constexpr unsigned WS_W2T = WS_CS1 + 16 * 16 * 512;
constexpr unsigned WS_W3T = WS_W2T + 8 * 102400;
constexpr unsigned WS_CS3 = WS_W3T + 16 * 286720;
constexpr unsigned WS_W4T = WS_CS3 + 16 * 16 * 1024;
constexpr unsigned WS_KVP = WS_W4T + 8 * 286720;
constexpr unsigned WS_O = WS_KVP + 16 * 16 * 4096;
constexpr unsigned WS_H = WS_O + 128 * 20480;
constexpr unsigned WS_HS = WS_H + 64 * 40960;
constexpr unsigned WS_ACT = WS_HS + 8 * 2048 * 8;
constexpr unsigned WS_END = WS_ACT + 128 * 57344;
constexpr unsigned OOB = 0x40000000u;
enum { F_W1 = 0, F_W4 = 1, F_KV = 2, F_O = 3, F_H = 4, F_A0 = 5, F_A1 = 6, F_A2 = 7, F_A3 = 8 };
DI unsigned flag_off(int kind, int grp) { return WS_FLAG + (kind * 16 + grp) * 256; }

constexpr unsigned SLOT = 20480;
constexpr unsigned L_XST = 3 * SLOT;
constexpr unsigned L_MISC = 143360;
constexpr unsigned L_RED = L_MISC;
constexpr unsigned L_KVT = L_MISC + 13440;
constexpr unsigned L_MEAN = L_MISC + 17792, L_RSTD = L_MEAN + 512, L_CS1 = L_RSTD + 512, L_CS3 = L_CS1 + 512;
constexpr unsigned LDS_BYTES = 163840;

#define WAITV(n) asm volatile("s_waitcnt vmcnt(%0)" ::"i"(n) : "memory")
#define WAITL(n) asm volatile("s_waitcnt lgkmcnt(%0)" ::"i"(n) : "memory")
#define BARRIER() asm volatile("s_barrier" ::: "memory")
#define BARRIER_L() asm volatile("s_waitcnt lgkmcnt(0)\n\ts_barrier" ::: "memory")
#define FENCE() __builtin_amdgcn_sched_barrier(0)

template <int C> DI void set_m0(unsigned base) { asm volatile("s_add_u32 m0, %0, %c1\n\ts_nop 0" ::"s"(base), "i"(C) : "scc", "memory"); }
template <int IMM, bool SC1, int NEXT> DI void dma(unsigned voff, rsrc_t rs, unsigned soff, unsigned nb) {
  if constexpr (SC1) {
    if constexpr (NEXT >= 0) asm volatile("buffer_load_dwordx4 %0, %1, %2 offen offset:%c3 sc1 lds\n\ts_add_u32 m0, %4, %c5" ::"v"(voff), "s"(rs), "s"(soff), "i"(IMM), "s"(nb), "i"(NEXT) : "scc", "memory");
    else asm volatile("buffer_load_dwordx4 %0, %1, %2 offen offset:%c3 sc1 lds" ::"v"(voff), "s"(rs), "s"(soff), "i"(IMM) : "memory");
  } else {
    if constexpr (NEXT >= 0) asm volatile("buffer_load_dwordx4 %0, %1, %2 offen offset:%c3 lds\n\ts_add_u32 m0, %4, %c5" ::"v"(voff), "s"(rs), "s"(soff), "i"(IMM), "s"(nb), "i"(NEXT) : "scc", "memory");
    else asm volatile("buffer_load_dwordx4 %0, %1, %2 offen offset:%c3 lds" ::"v"(voff), "s"(rs), "s"(soff), "i"(IMM) : "memory");
  }
}
#define NOP1() asm volatile("s_nop 0")
template <int R, int IMM> DI void ld_a(unsigned voff, rsrc_t rs, unsigned soff) {
  asm volatile("buffer_load_dwordx4 a[%c0:%c1], %2, %3, %4 offen offset:%c5 sc1" ::"i"(R), "i"(R + 3), "v"(voff), "s"(rs), "s"(soff), "i"(IMM) : "memory");
}
template <int IMM> DI void ld128c(v4u& d, unsigned voff, rsrc_t rs, unsigned soff) {
  asm volatile("buffer_load_dwordx4 %0, %1, %2, %3 offen offset:%c4 sc1" : "=v"(d) : "v"(voff), "s"(rs), "s"(soff), "i"(IMM) : "memory");
}
template <int IMM> DI void ld64c(v2u& d, unsigned voff, rsrc_t rs, unsigned soff) {
  asm volatile("buffer_load_dwordx2 %0, %1, %2, %3 offen offset:%c4 sc1" : "=v"(d) : "v"(voff), "s"(rs), "s"(soff), "i"(IMM) : "memory");
}
template <int IMM> DI void ld32c(unsigned& d, unsigned voff, rsrc_t rs, unsigned soff) {
  asm volatile("buffer_load_dword %0, %1, %2, %3 offen offset:%c4 sc1" : "=v"(d) : "v"(voff), "s"(rs), "s"(soff), "i"(IMM) : "memory");
}
DI void st128c(v4u d, unsigned voff, rsrc_t rs, unsigned soff) {
  asm volatile("buffer_store_dwordx4 %0, %1, %2, %3 offen sc1\n\ts_nop 1" ::"v"(d), "v"(voff), "s"(rs), "s"(soff) : "memory");
}
DI void st64c(v2u d, unsigned voff, rsrc_t rs, unsigned soff) {
  asm volatile("buffer_store_dwordx2 %0, %1, %2, %3 offen sc1" ::"v"(d), "v"(voff), "s"(rs), "s"(soff) : "memory");
}
DI void st32c(unsigned d, unsigned voff, rsrc_t rs, unsigned soff) {
  asm volatile("buffer_store_dword %0, %1, %2, %3 offen sc1" ::"v"(d), "v"(voff), "s"(rs), "s"(soff) : "memory");
}
DI void publish(unsigned tag, unsigned voff, rsrc_t rs, unsigned soff) {
  asm volatile("buffer_atomic_swap %0, %1, %2, %3 offen sc1" ::"v"(tag), "v"(voff), "s"(rs), "s"(soff) : "memory");
}
DI void st128p(v4u d, unsigned voff, rsrc_t rs, unsigned soff) {
  asm volatile("buffer_store_dwordx4 %0, %1, %2, %3 offen\n\ts_nop 1" ::"v"(d), "v"(voff), "s"(rs), "s"(soff) : "memory");
}
DI void st64p(v2u d, unsigned voff, rsrc_t rs, unsigned soff) {
  asm volatile("buffer_store_dwordx2 %0, %1, %2, %3 offen" ::"v"(d), "v"(voff), "s"(rs), "s"(soff) : "memory");
}
template <int OFF> DI void dsr128(v4u& d, unsigned addr) { asm volatile("ds_read_b128 %0, %1 offset:%c2" : "=v"(d) : "v"(addr), "i"(OFF) : "memory"); }
template <int OFF> DI void dsr128f(f32x4& d, unsigned addr) { asm volatile("ds_read_b128 %0, %1 offset:%c2" : "=v"(d) : "v"(addr), "i"(OFF) : "memory"); }
template <int R, int OFF> DI void dsr128a(unsigned addr) { asm volatile("ds_read_b128 a[%c0:%c1], %2 offset:%c3" ::"i"(R), "i"(R + 3), "v"(addr), "i"(OFF) : "memory"); }
template <int OFF> DI v4u tr_piece(unsigned addr) {
  v2u lo, hi;
  asm volatile("ds_read_b64_tr_b16 %0, %2 offset:%c3\n\tds_read_b64_tr_b16 %1, %2 offset:%c4\n\ts_waitcnt lgkmcnt(0)" : "=&v"(lo), "=&v"(hi) : "v"(addr), "i"(OFF), "i"(OFF + 512) : "memory");
  return v4u{lo.x, lo.y, hi.x, hi.y};
}
template <bool Z, int RA> DI void mf16_av(f32x4& acc, const v4u& b) {
  if constexpr (Z) asm volatile("v_mfma_f32_16x16x32_bf16 %0, a[%c1:%c2], %3, 0" : "=&v"(acc) : "i"(RA), "i"(RA + 3), "v"(b));
  else asm volatile("v_mfma_f32_16x16x32_bf16 %0, a[%c1:%c2], %3, %0" : "+v"(acc) : "i"(RA), "i"(RA + 3), "v"(b));
}
template <bool Z, int RB> DI void mf16_va(f32x4& acc, const v4u& a) {
  if constexpr (Z) asm volatile("v_mfma_f32_16x16x32_bf16 %0, %3, a[%c1:%c2], 0" : "=&v"(acc) : "i"(RB), "i"(RB + 3), "v"(a));
  else asm volatile("v_mfma_f32_16x16x32_bf16 %0, %3, a[%c1:%c2], %0" : "+v"(acc) : "i"(RB), "i"(RB + 3), "v"(a));
}
template <bool Z, int RA, int RB> DI void mf16_aa(f32x4& acc) {
  if constexpr (Z) asm volatile("v_mfma_f32_16x16x32_bf16 %0, a[%c1:%c2], a[%c3:%c4], 0" : "=&v"(acc) : "i"(RA), "i"(RA + 3), "i"(RB), "i"(RB + 3));
  else asm volatile("v_mfma_f32_16x16x32_bf16 %0, a[%c1:%c2], a[%c3:%c4], %0" : "+v"(acc) : "i"(RA), "i"(RA + 3), "i"(RB), "i"(RB + 3));
}
template <bool Z> DI void mf16_vv(f32x4& acc, const v4u& a, const v4u& b) {
  if constexpr (Z) asm volatile("s_nop 1\n\tv_mfma_f32_16x16x32_bf16 %0, %1, %2, 0" : "=&v"(acc) : "v"(a), "v"(b));
  else asm volatile("s_nop 1\n\tv_mfma_f32_16x16x32_bf16 %0, %1, %2, %0" : "+v"(acc) : "v"(a), "v"(b));
}
template <bool Z, int RB> DI void mf32_va(f32x16& acc, const v4u& a) {
  if constexpr (Z) asm volatile("v_mfma_f32_32x32x16_bf16 %0, %3, a[%c1:%c2], 0" : "=&v"(acc) : "i"(RB), "i"(RB + 3), "v"(a));
  else asm volatile("v_mfma_f32_32x32x16_bf16 %0, %3, a[%c1:%c2], %0" : "+v"(acc) : "i"(RB), "i"(RB + 3), "v"(a));
}
DI void mf32_vv(f32x16& acc, const v4u& a, const v4u& b) { asm volatile("v_mfma_f32_32x32x16_bf16 %0, %1, %2, %0" : "+v"(acc) : "v"(a), "v"(b)); }
template <int RA, int RB> DI void mf16_lva(f32x4& acc) {
  asm volatile("v_mfma_f32_16x16x32_bf16 %0, v[%c1:%c2], a[%c3:%c4], %0" : "+v"(acc) : "i"(RA), "i"(RA + 3), "i"(RB), "i"(RB + 3));
}
template <int RA, int RB> DI void mf16_lva0(f32x4& acc) {
  asm volatile("v_mfma_f32_16x16x32_bf16 %0, v[%c1:%c2], a[%c3:%c4], 0" : "=&v"(acc) : "i"(RA), "i"(RA + 3), "i"(RB), "i"(RB + 3));
}
template <int R, int IMM> DI void ld_v(unsigned voff, rsrc_t rs, unsigned soff) {
  asm volatile("buffer_load_dwordx4 v[%c0:%c1], %2, %3, %4 offen offset:%c5 sc1" ::"i"(R), "i"(R + 3), "v"(voff), "s"(rs), "s"(soff), "i"(IMM) : "memory");
}
#define MFMA_DRAIN() do { asm volatile("s_nop 15" ::: "memory"); FENCE(); } while (0)
template <class T> DI void pin(T& x) { asm volatile("" : "+v"(x)); }

DI unsigned cvtpk(float lo, float hi) { f32x2 v = {lo, hi}; bf16x2 b = __builtin_convertvector(v, bf16x2); return __builtin_bit_cast(unsigned, b); }
DI float asf(unsigned u) { return __builtin_bit_cast(float, u); }
DI float bflo(unsigned u) { return __builtin_bit_cast(float, u << 16); }
DI float bfhi(unsigned u) { return __builtin_bit_cast(float, u & 0xffff0000u); }
DI float sum8(v4u v, float acc) {
  acc += (bflo(v.x) + bfhi(v.x)) + (bflo(v.y) + bfhi(v.y));
  acc += (bflo(v.z) + bfhi(v.z)) + (bflo(v.w) + bfhi(v.w));
  return acc;
}
DI float phi1(float t1) {
  const float e = __builtin_amdgcn_fmed3f(__builtin_amdgcn_exp2f(fmaf(t1, 1.44269504f, -1.44269504e-6f)), 0.f, 1.f);
  return fmaxf(t1, 1e-6f) + e;
}
template <int R, int OFF, int P4 = 512> DI void tr_a(unsigned addr) {
  asm volatile("ds_read_b64_tr_b16 a[%c0:%c1], %4 offset:%c5\n\tds_read_b64_tr_b16 a[%c2:%c3], %4 offset:%c6" ::"i"(R), "i"(R + 1), "i"(R + 2), "i"(R + 3), "v"(addr), "i"(OFF), "i"(OFF + P4) : "memory");
}
template <int R> DI void st_a(unsigned voff, rsrc_t rs, unsigned soff) {
  asm volatile("buffer_store_dwordx4 a[%c0:%c1], %2, %3, %4 offen sc1" ::"i"(R), "i"(R + 3), "v"(voff), "s"(rs), "s"(soff) : "memory");
}
template <int OFF> DI void dsw128f(const f32x4& d, unsigned addr) { asm volatile("ds_write_b128 %0, %1 offset:%c2" ::"v"(addr), "v"(d), "i"(OFF) : "memory"); }
DI float v_fma(float a, float b, float c) { float r; asm volatile("v_fma_f32 %0, %1, %2, %3" : "=v"(r) : "v"(a), "v"(b), "v"(c)); return r; }
DI float v_mul(float a, float b) { float r; asm volatile("v_mul_f32 %0, %1, %2" : "=v"(r) : "v"(a), "v"(b)); return r; }
DI float v_exp(float a) { float r; asm volatile("v_exp_f32 %0, %1" : "=v"(r) : "v"(a)); return r; }
DI float v_rcp(float a) { float r; asm volatile("v_rcp_f32 %0, %1" : "=v"(r) : "v"(a)); return r; }
DI unsigned v_cvtpk(float a, float b) { unsigned r; asm volatile("v_cvt_pk_bf16_f32 %0, %1, %2" : "=v"(r) : "v"(a), "v"(b)); return r; }
DI rsrc_t mk(const void* p, unsigned bytes) { return __builtin_amdgcn_make_buffer_rsrc((void*)p, 0, (int)bytes, 0x00020000); }

#define ACLOB "a0","a1","a2","a3","a4","a5","a6","a7","a8","a9","a10","a11","a12","a13","a14","a15","a16","a17","a18","a19","a20","a21","a22","a23","a24","a25","a26","a27","a28","a29","a30","a31","a32","a33","a34","a35","a36","a37","a38","a39","a40","a41","a42","a43","a44","a45","a46","a47","a48","a49","a50","a51","a52","a53","a54","a55","a56","a57","a58","a59","a60","a61","a62","a63","a64","a65","a66","a67","a68","a69","a70","a71","a72","a73","a74","a75","a76","a77","a78","a79","a80","a81","a82","a83","a84","a85","a86","a87","a88","a89","a90","a91","a92","a93","a94","a95","a96","a97","a98","a99","a100","a101","a102","a103","a104","a105","a106","a107","a108","a109","a110","a111","a112","a113","a114","a115","a116","a117","a118","a119","a120","a121","a122","a123","a124","a125","a126","a127","a128","a129","a130","a131","a132","a133","a134","a135","a136","a137","a138","a139","a140","a141","a142","a143","a144","a145","a146","a147","a148","a149","a150","a151","a152","a153","a154","a155","a156","a157","a158","a159","a160","a161","a162","a163","a164","a165","a166","a167","a168","a169","a170","a171","a172","a173","a174","a175","a176","a177","a178","a179","a180","a181","a182","a183","a184","a185","a186","a187","a188","a189","a190","a191","a192","a193","a194","a195","a196","a197","a198","a199","a200","a201","a202","a203","a204","a205","a206","a207","a208","a209","a210","a211","a212","a213","a214","a215","a216","a217","a218","a219","a220","a221","a222","a223","a224","a225","a226","a227","a228","a229","a230","a231","a232","a233","a234","a235","a236","a237","a238","a239","a240","a241","a242","a243","a244","a245","a246","a247","a248","a249","a250","a251","a252","a253","a254","a255"

DI void poll_w0(rsrc_t ws, unsigned voff, unsigned tag, int& budget, int w) {
  unsigned f;
  asm volatile("s_cmp_lg_u32 %5, 0\n\ts_cbranch_scc1 2f\n"
               "1:\n\tbuffer_load_dword %0, %2, %3, 0 offen sc1\n\ts_waitcnt vmcnt(0)\n\tv_cmp_ne_u32 vcc, %4, %0\n\ts_nop 1\n\ts_cbranch_vccz 2f\n\t"
               "s_sleep 2\n\ts_sub_u32 %1, %1, 1\n\ts_cmp_gt_i32 %1, 0\n\ts_cbranch_scc1 1b\n2:"
               : "=&v"(f), "+s"(budget) : "v"(voff), "s"(ws), "s"(tag), "s"(w) : "vcc", "scc", "memory");
}
template <int N> DI void poll_chk(unsigned f, rsrc_t ws, unsigned voff, unsigned soff, unsigned tag, int& budget) {
  asm volatile("s_waitcnt vmcnt(%c6)\n"
               "1:\n\tv_cmp_ne_u32 vcc, %4, %0\n\ts_nop 1\n\ts_cbranch_vccz 2f\n\t"
               "s_sub_u32 %1, %1, 1\n\ts_cmp_gt_i32 %1, 0\n\ts_cbranch_scc0 2f\n\ts_sleep 4\n\t"
               "buffer_load_dword %0, %2, %3, %5 offen sc1\n\ts_waitcnt vmcnt(0)\n\ts_branch 1b\n2:"
               : "+v"(f), "+s"(budget) : "v"(voff), "s"(ws), "s"(tag), "s"(soff), "i"(N) : "vcc", "scc", "memory");
}

#define STAMP(i) asm volatile("; MARK %0" ::"i"(i))

constexpr int slot_of(int u) { return (u % 7) * (int)SLOT; }
template <int U, int FR, int NEXT> DI void unit_dma(rsrc_t ws, unsigned lane16, unsigned lane16b, unsigned soff, unsigned ldsw) {
  if constexpr (FR < 3) dma<FR * 1024, true, -1>(lane16, ws, soff, ldsw);
  else if constexpr (FR == 3) dma<3072, true, slot_of(U) + 1024>(lane16, ws, soff, ldsw);
  else dma<3072, true, NEXT>(lane16b, ws, soff, ldsw);
}

#define WL(n) __builtin_amdgcn_s_waitcnt(0xC07F | ((n) << 8))
template <int R, int OFF> DI void dsr64a(unsigned addr) { asm volatile("ds_read_b64 a[%c0:%c1], %2 offset:%c3" ::"i"(R), "i"(R + 1), "v"(addr), "i"(OFF) : "memory"); }
template <int OFF> DI void dsr32f(float& d, unsigned addr) { asm volatile("ds_read_b32 %0, %1 offset:%c2" : "=v"(d) : "v"(addr), "i"(OFF) : "memory"); }
constexpr unsigned long long kv_mask(int a, int bb) {
  unsigned long long m = 0;
  for (int l = 0; l < 64; ++l) {
    const int li = l & 15, dq = 4 * a + (l >> 4), hd = dq >= 5, eg = bb < 2 ? 16 * bb + li : 24 + li;
    const bool ok = dq < 10 && (bb == 3 ? li == 0 : (bb < 2 || li >= 8) && eg / 20 == hd);
    if (ok) m |= 1ull << l;
  }
  return m;
}
template <int A, int BB, int OFF> DI void dsw_kv(const f32x4& d, unsigned addr) {
  asm volatile("s_mov_b32 exec_lo, %c2\n\ts_mov_b32 exec_hi, %c3\n\tds_write_b128 %0, %1 offset:%c4" ::"v"(addr), "v"(d), "i"((int)(unsigned)kv_mask(A, BB)), "i"((int)(unsigned)(kv_mask(A, BB) >> 32)), "i"(OFF) : "memory");
}
DI float sel4(const f32x4& v, int i) { const float a = (i & 1) ? v[1] : v[0], b = (i & 1) ? v[3] : v[2]; return (i & 2) ? b : a; }
DI float xsum32(float s) {
  const unsigned u = __builtin_bit_cast(unsigned, s);
  const auto r = __builtin_amdgcn_permlane32_swap(u, u, false, false);
  return asf(r[0]) + asf(r[1]);
}
constexpr int VRES = 176;
constexpr int P3_HUP = 6;
constexpr int P3_LASTU = 24;
constexpr int p3_dma_start(int nu) { int d = nu - 15; int a = 85 + 15 * d, b = 36 + 20 * d; return a > b ? a : b; }
constexpr int p3_dma_at(int g) {
  for (int nu = 14; nu <= P3_LASTU; ++nu) { int s = p3_dma_start(nu); if (g >= s && g < s + 15 && (g - s) % 3 == 0) return nu * 8 + (g - s) / 3; }
  return -1;
}
constexpr int p3_w4a_at(int g) { return (g >= 72 && (g - 72) % 6 == 0 && (g - 72) / 6 < 30) ? (g - 72) / 6 : -1; }
constexpr int p3_epi_start(int p) { return p < 5 ? 123 + 24 * p : 246; }
constexpr int P3_EPI_ST = 26;
constexpr int p3_st_step(int p) { return p < 5 ? p3_epi_start(p) + P3_EPI_ST : p3_epi_start(5) + P3_EPI_ST / 2; }
constexpr int P3_FL0 = 196, P3_FL1 = 236, P3_FL2 = 276;
constexpr int P3_POLL_A0 = 232, P3_CHK_A0 = 246, P3_POLL_A1 = 278, P3_AL0 = 247;
constexpr bool p3_hload(int g) { return (g & 1) && (g >> 1) + P3_HUP < 40; }
constexpr bool p3_aload(int g) { return g >= P3_AL0 && g < P3_AL0 + 32 && (g - P3_AL0) % 2 == 0; }
constexpr bool p3_store(int g) { for (int p = 0; p < 6; ++p) if (g == p3_st_step(p)) return true; return false; }
constexpr int p3_nvm(int g) { return p3_hload(g) + (p3_dma_at(g) >= 0) + (p3_w4a_at(g) >= 0) + p3_aload(g) + p3_store(g) + (g == P3_POLL_A0) + (g == P3_POLL_A1) + (g == P3_FL0) + (g == P3_FL1) + (g == P3_FL2); }
constexpr int p3_cnt(int gx, int g) { int n = 0; for (int k = gx + 1; k < g; ++k) n += p3_nvm(k); return n > 63 ? 63 : n; }
DI void split3(float x, unsigned& a, unsigned& b, unsigned& c) {
  a = __builtin_bit_cast(unsigned, x) & 0xffff0000u;
  const float r1 = x - asf(a);
  b = __builtin_bit_cast(unsigned, r1) & 0xffff0000u;
  c = __builtin_bit_cast(unsigned, r1 - asf(b)) & 0xffff0000u;
}
constexpr int areg(int u, int mm) { return (u == 0 ? 240 : u < 11 ? 16 * (u - 1) : 16 * (u - 11)) + 4 * mm; }
typedef unsigned v16u __attribute__((ext_vector_type(16)));
DI const void* ptr2(unsigned lo, unsigned hi) { return (const void*)(((unsigned long long)hi << 32) | lo); }

__global__ void __launch_bounds__(256, 1) __attribute__((amdgpu_num_vgpr(VRES)))
fused_block(const void* __restrict__ xp_, const void* __restrict__ wqkvp_, const void* __restrict__ wop_, const void* __restrict__ wgp_,
            const void* __restrict__ wup_, const void* __restrict__ wdp_, void* __restrict__ outp_, char* __restrict__ wsp_) {
  extern __shared__ __attribute__((aligned(16))) char smem_[];
  LAS char* L = (LAS char*)smem_;
  unsigned long long pc0; unsigned codelen; v16u ka;
  asm volatile("s_load_dwordx16 %2, %3, 0\nfbk_begin:\n\ts_getpc_b64 %0\n\ts_mov_b32 %1, fbk_end-fbk_begin"
               : "=&s"(pc0), "=&s"(codelen), "=&s"(ka) : "s"(__builtin_amdgcn_kernarg_segment_ptr()) : ACLOB, "v255");
  FENCE();
  const int tid = threadIdx.x, lane = tid & 63;
  const int w = __builtin_amdgcn_readfirstlane(tid >> 6);
  const int b = blockIdx.x, xcd = b & 7, bi = b >> 3;
  const int r = (xcd >> 1) * 4 + (bi & 3), c = (xcd & 1) * 8 + (bi >> 2);
  const int j = (c >> 3) * 4 + (c & 3), hf = (c >> 2) & 1;
  const int li = lane & 15, lg = lane >> 4;
  const unsigned lane16 = lane * 16, lane16b = lane16 + 1024;
  const unsigned ldsw = w * 5120;
  int budget = 200000;
  const int drow = lane >> 3, dch = lane & 7, tq = li >> 2, tp = li & 3;
  const int pp = 2 * r + hf;
  const unsigned xst = L_XST + w * 20480;
  unsigned vo1, vo1b, vo2, vct;
  {
    int col = 640 + 8 * li;
    col = li >= 5 ? 1232 + 8 * li : col; col = li == 5 ? 1312 : col; col = li >= 10 ? 8 * li - 80 : col; col = li == 15 ? 32 : col;
    vo1 = ((40 * r + 12 * w + lg) * 1920 + 40 * c + col) * 2;
    vo1b = w == 3 ? OOB : vo1;
    pin(vo1); pin(vo1b);
  }
  set_m0<SLOT>(3072 * w);
  asm volatile("s_waitcnt lgkmcnt(0)" : "+s"(ka));
  char* const wsp = (char*)ptr2(ka[14], ka[15]);
  const rsrc_t ws = mk(wsp, WS_END);
  const rsrc_t rx = mk(ptr2(ka[0], ka[1]), 2048 * 640 * 2);
  STAMP(0);
  unsigned epoch, cpf;
  {
    const rsrc_t rw = mk(ptr2(ka[2], ka[3]), 640 * 1920 * 2);
    dma<0, false, -1>(vo1, rw, 0, 0);
    dma<1024, false, -1>(vo1b, rw, 4 * 3840 - 1024, 0);
    dma<2048, false, 2 * SLOT>(vo1b, rw, 8 * 3840 - 2048, ldsw);
    const int oc = 32 * (w < 3 ? w : dch >> 1) + pp, n = oc >> 2, g = oc & 3;
    const int ra = 40 * n + 8 * g, rb = 160 * (n - 16) + 40 * g + 32;
    vo2 = ((n < 16 ? ra : rb) + drow) * 1280 + 160 * j + (w < 3 ? dch * 16 : 128 + (dch & 1) * 16);
    vo2 = oc < 80 ? vo2 : OOB;
    vct = tid < 16 ? (unsigned)(bi * 16 + tid) * 128 : OOB;
    const rsrc_t rw2 = mk((const char*)ptr2(ka[4], ka[5]) - 3072, 640 * 640 * 2 + 3072);
    dma<3072, false, 0>(vo2, rw2, 0, xst);
    unsigned z = 0; ld32c<0>(epoch, z, ws, 0);
    const rsrc_t rc = mk((const void*)pc0, codelen);
    asm volatile("buffer_load_dword %0, %1, %2, 0 offen" : "=v"(cpf) : "v"(vct), "s"(rc) : "memory");
  }
  unsigned xv[4];
  SF(4, pc) xv[pc] = drow * 1280 + ((dch ^ (4 * (pc & 1) + lg)) * 16) + pc * (10240 - 1024); SE
  const unsigned xrow = (128 * r + 32 * w) * 1280;
  auto x_piece = [&](auto ch_, auto pc_) __attribute__((always_inline)) {
    constexpr int ch = decltype(ch_)::value, pc = decltype(pc_)::value;
    dma<pc * 1024, false, (pc == 3 ? ((ch + 1) % 5) * 4096 : -1)>(xv[pc], rx, xrow + ch * 128, xst);
  };
  auto x_chunk = [&](auto ch_) __attribute__((always_inline)) { SF(4, pc) x_piece(ch_, IC<pc>{}); SE NOP1(); };
  STAMP(21);
  x_chunk(IC<0>{}); x_chunk(IC<1>{});
  const unsigned trY = tq * 128 + lg * 32 + tp * 8, trX = tq * 128 + (lg >> 1) * 64 + tp * 16 + (lg & 1) * 8;
  unsigned dst1[3], d2, ta, tb, dc; v4u sel;
  {
    const unsigned s1 = (li == (lg & 1)) ? 0x3f803f80u : 0u;
    sel = v4u{s1, s1, s1, s1};
    const int cg = 2 * w + (lg & 1);
    SF(3, k)
      const int ol = 2 * k + (lg >> 1), oc = 5 * r + ol, ks = oc >> 2;
      const int fr = cg < 5 ? ks * 5 + cg : 95 + ks * 3 + cg;
      dst1[k] = ol < 5 ? (unsigned)(fr * 1024 + (oc & 3) * 256 + li * 16) : OOB;
      pin(dst1[k]);
    SE
    const int oc = 32 * (w < 3 ? w : lg) + pp;
    d2 = oc < 80 ? (unsigned)((oc >> 2) * 5120 + (oc & 3) * 256 + (w < 3 ? lg : 4) * 1024 + li * 16) : OOB;
    ta = SLOT + ((lg >> 1) * 8 + tq) * 256 + (2 * w + (lg & 1)) * 32 + tp * 8; tb = 2 * SLOT + ldsw + (w < 3 ? trX : trY); dc = lane < 16 ? (unsigned)(16 * w + li) * 8 : OOB;
    pin(sel); pin(d2); pin(ta); pin(tb); pin(dc);
  }
  x_chunk(IC<2>{});
  STAMP(22);
  unsigned fl_r = tid == 0 ? (unsigned)(4 * r) : OOB, fl_c = tid == 0 ? (unsigned)(4 * c) : OOB, fl_p = tid == 0 ? (unsigned)(4 * pp) : OOB;
  pin(fl_r); pin(fl_c); pin(fl_p);
  unsigned v3, s4[2], d4[2], v4y, d3, d3s, d4y;
  {
    const int ps = 4 * (w >> 1) + (dch >> 1);
    const unsigned fa = (6 * c + ps) * 32, fb = c < 12 ? (96 + c) * 32 : OOB;
    v3 = (ps < 6 ? fa : ps == 6 ? fb : OOB) + (40 * r + drow) * 3456 + 16 * (dch & 1);
    const int pd = 4 * (w >> 1) + lg;
    d3s = pd < 3 ? 2048u : 0u;
    d3 = (pd < 7 ? (unsigned)(pd * (pd < 3 ? 1024 : 40960)) : OOB) + (w & 1) * 256 + li * 16;
    pin(v3); pin(d3); pin(d3s);
  }
  x_chunk(IC<3>{});
  {
    SF(2, k)
      const int oc = 32 * (2 * w + k) + pp, nn = oc >> 2, g = oc & 3;
      const int ft = (w < 3 ? 6 * (nn & 15) + 2 * w : 2 * nn) + (g >> 1);
      const unsigned sv = (16 * ft + 8 * (g & 1)) * 1280 + 160 * j, dv = WS_W4T + j * 286720 + nn * 5120 + g * 256;
      s4[k] = oc < 216 ? sv : OOB; d4[k] = oc < 216 ? dv : OOB;
    SE
    v4y = (dch < 2 ? s4[0] : dch < 4 ? s4[1] : OOB) + drow * 1280 + 128 + (dch & 1) * 16;
    d4y = (lg == 0 ? d4[0] : lg == 1 ? d4[1] : OOB) + 4096 + li * 16;
    pin(v4y); pin(d4y);
  }
  unsigned tag, vtag;
  {
    STAMP(23);
    asm volatile("s_waitcnt vmcnt(16)" : "+v"(epoch), "+v"(cpf)::"memory");
    BARRIER();
    STAMP(24);
    SF(3, k) tr_a<160 + 4 * k, k * 4096, 1024>(ta); SE
    tr_a<172, 3072>(tb);
    tag = __builtin_amdgcn_readfirstlane(epoch) * 1664525u + 1013904223u;
    vtag = tag; pin(vtag);
    f32x4 cs;
    WAITL(2);
    SF(3, k) mf16_va<k == 0, 160 + 4 * k>(cs, sel); SE
    SF(3, k) st_a<160 + 4 * k>(dst1[k], ws, WS_W1T + c * 163840); SE
    WAITL(0);
    st_a<172>(d2, ws, WS_W2T + j * 102400);
    asm volatile("s_nop 3" : "+v"(cs));
    const float c0 = cs[0], c1 = cs[1];
    st64c(v2u{__builtin_bit_cast(unsigned, c0), __builtin_bit_cast(unsigned, c1)}, dc, ws, WS_CS1 + (c * 16 + r) * 512);
  }
  STAMP(25);
  x_chunk(IC<4>{});
  unsigned xa0 = xst + li * 128 + ((lg ^ (li >> 1)) * 16), xa1 = xst + li * 128 + (((4 + lg) ^ (li >> 1)) * 16);
  auto x_read = [&](auto ch_) __attribute__((always_inline)) {
    constexpr int ch = decltype(ch_)::value;
    dsr128a<(4 * ch + 0) * 4, (ch % 5) * 4096>(xa0);
    dsr128a<(4 * ch + 1) * 4, (ch % 5) * 4096 + 2048>(xa0);
    dsr128a<(4 * ch + 2) * 4, (ch % 5) * 4096>(xa1);
    dsr128a<(4 * ch + 3) * 4, (ch % 5) * 4096 + 2048>(xa1);
  };
  v4u ones = {0x3f803f80u, 0x3f803f80u, 0x3f803f80u, 0x3f803f80u};
  unsigned pv2 = flag_off(0, j) + (lane & 31) * 4;
  pin(ones); pin(pv2);
  f32x4 gr[2], sm[2];
  auto x_stat1 = [&](auto ch_, auto q_) __attribute__((always_inline)) {
    constexpr int ch = decltype(ch_)::value, q = decltype(q_)::value;
    mf16_aa<(ch == 0 && q < 2), (4 * ch + q) * 4, (4 * ch + q) * 4>(gr[q & 1]);
    mf16_va<(ch == 0 && q < 2), (4 * ch + q) * 4>(sm[q & 1], ones);
  };
  const unsigned w1src = WS_W1T + c * 163840 + ldsw, w3src = WS_W3T + c * 286720 + ldsw;
  unsigned pf;
  auto x_step = [&](auto k_) __attribute__((always_inline)) {
    constexpr int k = decltype(k_)::value;
    WAITL(4);
    SF(4, q) x_stat1(IC<k>{}, IC<q>{}); if constexpr (k < 5) x_piece(IC<5 + k>{}, IC<q>{}); SE
  };
  WAITV(17); x_read(IC<0>{}); x_read(IC<1>{});
  x_step(IC<0>{});
  STAMP(26);
  WAITV(8);
  BARRIER();
  publish(vtag, fl_p, ws, flag_off(F_W1, j));
  STAMP(1);
  x_read(IC<2>{}); x_step(IC<1>{});
  x_read(IC<3>{}); x_step(IC<2>{});
  WAITV(13); x_read(IC<4>{}); x_step(IC<3>{});
  WAITV(13); x_read(IC<5>{}); x_step(IC<4>{});
  ld32c<0>(pf, pv2, ws, F_W1 * 4096);
  {
    const v4u z4 = {0u, 0u, 0u, 0u};
    *(LAS v4u*)(L + L_KVT + tid * 16) = z4;
    if (tid < 16) *(LAS v4u*)(L + L_KVT + 4096 + tid * 16) = z4;
  }
  STAMP(2);
  WAITV(13); x_read(IC<6>{}); x_step(IC<5>{});
  WAITV(9); x_read(IC<7>{});
  set_m0<0>(ldsw);
  x_step(IC<6>{});
  WAITV(5); x_read(IC<8>{}); x_step(IC<7>{});
  poll_chk<0>(pf, ws, pv2, F_W1 * 4096, tag, budget);
  STAMP(3);
  x_read(IC<9>{});
  SF(5, f) unit_dma<0, f, slot_of(1)>(ws, lane16, lane16b, w1src, ldsw); if constexpr (f >= 3) NOP1(); SE
  WAITL(0);
  SF(2, k) SF(4, q)
    x_stat1(IC<8 + k>{}, IC<q>{});
    constexpr int i = 4 * k + q;
    if constexpr (i < 5) unit_dma<1, i, slot_of(2)>(ws, lane16, lane16b, w1src + SLOT, ldsw);
    else unit_dma<2, i - 5, -1>(ws, lane16, lane16b, w1src + 2 * SLOT, ldsw);
  SE SE
  unit_dma<2, 3, -1>(ws, lane16, lane16b, w1src + 2 * SLOT, ldsw); NOP1();
  unit_dma<2, 4, slot_of(3)>(ws, lane16, lane16b, w1src + 2 * SLOT, ldsw);
  const unsigned lb0 = lane16, lb1 = lane16 + 3 * SLOT, lb2 = lane16 + 6 * SLOT;
  auto rd = [&](auto fr_, v4u& d) __attribute__((always_inline)) {
    constexpr int fr = decltype(fr_)::value, sl = (fr / 20) % 7, off = (sl % 3) * SLOT + (fr % 20) * 1024;
    dsr128<off>(d, sl < 3 ? lb0 : sl < 6 ? lb1 : lb2);
  };
  f32x4 akv[5][2], aq[3][2], enb[2], era[2];
  float ecs[5];
  const unsigned v4x = drow * 1280 + dch * 16, d4x = lg * 1024 + li * 16;
  const rsrc_t rw3 = mk((w & 1) ? ptr2(ka[8], ka[9]) : ptr2(ka[6], ka[7]), 640 * 1728 * 2);
  const rsrc_t rw4 = mk((const char*)ptr2(ka[10], ka[11]) - 2048, 1728 * 640 * 2 + 2048);
  {
    v4u bf[8];
    unsigned cs1p[8];
    unsigned vc1 = (lane >> 5) * 4096 + (32 * w + (lane & 31)) * 4;
    unsigned ea = L_MEAN + (32 * w + 4 * lg) * 4, ec = L_CS1 + li * 4;
    pin(vc1); pin(ea); pin(ec);
    WAITV(10);
    BARRIER();
    STAMP(4);
    SF(6, f) rd(IC<f>{}, bf[f]); SE
    SF(100, f)
      constexpr int ks = f / 5, t = f % 5;
      if constexpr (f % 20 == 14 && f < 80) { WAITV(f == 14 ? 14 : f == 34 ? 19 : f == 54 ? 16 : 15); BARRIER(); }
      if constexpr (!(f & 1)) WL(f + 6 <= 100 ? 4 : f == 96 ? 6 : 9);
      mf16_av<ks == 0, (2 * ks) * 4>(akv[t][0], bf[f & 7]);
      if constexpr (f + 6 < 100) rd(IC<f + 6>{}, bf[(f + 6) & 7]);
      mf16_av<ks == 0, (2 * ks + 1) * 4>(akv[t][1], bf[f & 7]);
      if constexpr (f % 3 == 1 && f < 75) { constexpr int i = f / 3; unit_dma<3 + i / 5, i % 5, slot_of(4 + i / 5)>(ws, lane16, lane16b, w1src + (3 + i / 5) * SLOT, ldsw); }
      if constexpr (f >= 76 && f % 3 == 1) {
        constexpr int k = (f - 76) / 3;
        if constexpr (k < 5) dma<(k < 4 ? k : 3) * 1024, false, (k == 3 ? (int)SLOT + 1024 : k == 4 ? 2 * (int)SLOT : -1)>(v3, rw3, k * 8 * 3456 - (k < 4 ? k : 3) * 1024, ldsw);
        else if constexpr (k == 5) dma<0, false, -1>(v4x, rw4, s4[0] + 2048, 0);
        else if constexpr (k == 6) dma<1024, false, -1>(v4x, rw4, s4[1] + 1024, 0);
        else dma<2048, false, slot_of(8)>(v4y, rw4, 0, ldsw);
      }
      if constexpr (f % 3 == 2 && f < 24) ld32c<(f / 3) * 512>(cs1p[f / 3], vc1, ws, WS_CS1 + c * 8192);
      if constexpr (f == 27) {
    float sta[2], stb[2];
    SF(2, m)
      pin(gr[m]); pin(sm[m]);
      const float sq = sel4(gr[m], tp);
      const float mean = sm[m][0] * (1.f / 640.f);
      const float rs = __builtin_amdgcn_rsqf(fmaxf(sq * (1.f / 640.f) - mean * mean, 0.f) + 1e-5f);
      stb[m] = -mean * rs; sta[m] = rs;
    SE
    if (lg == tq) {
      SF(2, m) *(LAS float*)(L + L_MEAN + (32 * w + 16 * m + li) * 4) = stb[m]; *(LAS float*)(L + L_RSTD + (32 * w + 16 * m + li) * 4) = sta[m]; SE
    }
      }
      if constexpr (f == 94) { dsr128f<0>(enb[0], ea); dsr128f<512>(era[0], ea); }
      if constexpr (f == 95) { dsr128f<64>(enb[1], ea); dsr128f<576>(era[1], ea); }
      if constexpr (f == 96) { dsr32f<0>(ecs[0], ec); dsr32f<64>(ecs[1], ec); dsr32f<128>(ecs[2], ec); }
      if constexpr (f == 97) { dsr32f<192>(ecs[3], ec); dsr32f<256>(ecs[4], ec); }
      if constexpr (f == 56) {
        WAITV(11);
        SF(8, k) pin(cs1p[k]); SE
        const float s = xsum32(((asf(cs1p[0]) + asf(cs1p[1])) + (asf(cs1p[2]) + asf(cs1p[3]))) + ((asf(cs1p[4]) + asf(cs1p[5])) + (asf(cs1p[6]) + asf(cs1p[7]))));
        *(LAS float*)(L + L_CS1 + (32 * w + (lane & 1) * 16 + ((lane & 31) >> 1)) * 4) = s;
      }
    SE
  }
  STAMP(5);
  const unsigned w2src = WS_W2T + j * 102400 + w * 25600;
  auto ldw2 = [&](auto i_) __attribute__((always_inline)) {
    constexpr int i = decltype(i_)::value;
    ld_a<(i < 24 ? 160 + 4 * i : 0), (i & 3) * 1024>(lane16, ws, w2src + (i >> 2) * 4096);
  };
  {
    v4u fr[5];
    unsigned ib = L_RED + w * 3360 + (5 * li + lg) * 16, iz = L_RED + w * 3360 + (lg ? 204 + lg : 104) * 16;
    pin(ib); pin(iz);
    asm volatile("s_waitcnt lgkmcnt(0)\n\ts_nop 5" : "+v"(enb[0]), "+v"(enb[1]), "+v"(era[0]), "+v"(era[1]), "+v"(ecs[0]), "+v"(ecs[1]), "+v"(ecs[2]), "+v"(ecs[3]), "+v"(ecs[4])::"memory");
    FENCE();
    SF(5, t)
      float v[2][4];
      const float eps = t < 2 ? 1e-6f : (t == 2 && li < 8) ? 1e-6f : 0.f;
      SF(2, m)
        if constexpr (t < 3) ldw2(IC<2 * t + m>{});
        pin(akv[t][m]);
        SF(4, e)
          float x = fmaf(akv[t][m][e], era[m][e], fmaf(enb[m][e], ecs[t], eps));
          if constexpr (t < 2) x = phi1(x);
          if constexpr (t == 2) { const float y = phi1(x); x = li < 8 ? y : x; }
          v[m][e] = x;
        SE
        fr[t][2 * m] = cvtpk(v[m][0], v[m][1]); fr[t][2 * m + 1] = cvtpk(v[m][2], v[m][3]);
      SE
      if constexpr (t < 3) pin(fr[t]);
    SE
    f32x4 dd[10];
    asm volatile("s_nop 1\n\t"
                 "v_mfma_f32_16x16x32_bf16 %7, %10, %15, 0\n\tv_mfma_f32_16x16x32_bf16 %8, %11, %15, 0\n\tv_mfma_f32_16x16x32_bf16 %9, %12, %15, 0\n\t"
                 "v_mfma_f32_16x16x32_bf16 %4, %11, %12, 0\n\tv_mfma_f32_16x16x32_bf16 %6, %12, %12, 0\n\t"
                 "v_mfma_f32_16x16x32_bf16 %0, %10, %13, 0\n\tv_mfma_f32_16x16x32_bf16 %2, %11, %13, 0\n\t"
                 "v_mfma_f32_16x16x32_bf16 %1, %10, %14, 0\n\tv_mfma_f32_16x16x32_bf16 %3, %11, %14, 0\n\tv_mfma_f32_16x16x32_bf16 %5, %12, %14, 0"
                 : "=&v"(dd[0]), "=&v"(dd[1]), "=&v"(dd[2]), "=&v"(dd[3]), "=&v"(dd[4]), "=&v"(dd[5]), "=&v"(dd[6]), "=&v"(dd[7]), "=&v"(dd[8]), "=&v"(dd[9])
                 : "v"(fr[0]), "v"(fr[1]), "v"(fr[2]), "v"(fr[3]), "v"(fr[4]), "v"(ones));
    FENCE();
    dsw_kv<0, 3, 100 * 16>(dd[7], ib); dsw_kv<1, 3, 0>(dd[8], iz); dsw_kv<2, 3, 208 * 16>(dd[9], ib);
    dsw_kv<1, 2, 124 * 16>(dd[4], ib); dsw_kv<2, 2, 128 * 16>(dd[6], ib);
    dsw_kv<0, 0, 0>(dd[0], ib); dsw_kv<1, 0, 4 * 16>(dd[2], ib);
    dsw_kv<0, 1, 80 * 16>(dd[1], ib); dsw_kv<1, 1, 84 * 16>(dd[3], ib); dsw_kv<2, 1, 88 * 16>(dd[5], ib);
    asm volatile("s_mov_b64 exec, -1" ::: "memory");
    FENCE();
    ldw2(IC<6>{}); ldw2(IC<7>{});
    unsigned ra = L_RED + tid * 16, gv0 = tid < 210 ? (unsigned)tid * 16 : OOB;
    pin(ra); pin(gv0);
    WAITV(16);
    BARRIER_L();
    f32x4 p[4];
    SF(4, k) dsr128f<k * 3360>(p[k], ra); SE
    asm volatile("s_waitcnt lgkmcnt(0)" : "+v"(p[0]), "+v"(p[1]), "+v"(p[2]), "+v"(p[3])::"memory");
    st128c(__builtin_bit_cast(v4u, (p[0] + p[1]) + (p[2] + p[3])), gv0, ws, WS_KVP + (c * 16 + r) * 4096);
  }
  STAMP(6);
  v4u pq[2][2];
  v4u xr[2]; v2u xr4;
  unsigned pvk = flag_off(F_KV, c) + li * 4;
  {
    v4u bf[8];
    SF(6, f) rd(IC<100 + f>{}, bf[f]); SE
    unsigned xo = ((128 * r + 64 * hf + 16 * w + li) * 640 + 80 * j) * 2;
    unsigned xo16 = xo + lg * 16, xo8 = xo + lg * 8;
    pin(xo16); pin(xo8); pin(pvk);
    f32x4 cs3;
    unsigned t3 = SLOT + ldsw + tq * 128 + lg * 32 + (2 * (tp & 1) + (tp >> 1)) * 8, t4x = 2 * SLOT + ldsw + trX, t4y = 2 * SLOT + ldsw + trY;
    unsigned dc3 = lane < 16 ? (unsigned)(((w & 1) * 16 + li) * 32 + (w >> 1) * 16) : OOB;
    const unsigned s13 = li == lg ? 0x3f803f80u : 0u;
    v4u sel3 = {s13, s13, s13, s13};
    pin(t3); pin(t4x); pin(t4y); pin(dc3); pin(sel3);
    WAITV(9);
    SF(60, f)
      constexpr int ks = f / 3, t = f % 3;
      if constexpr (f == 16) { STAMP(30); WAITV(5); BARRIER(); publish(vtag, fl_r, ws, flag_off(F_KV, c)); STAMP(31); }
      if constexpr (!(f & 1)) WL(f + 6 <= 60 ? 4 : 58 - f);
      mf16_va<ks == 0, (2 * ks) * 4>(aq[t][0], bf[f & 7]);
      if constexpr (f < 16 && !(f & 1)) {
        constexpr int k = f / 2;
        if constexpr (k < 5) tr_a<224 + 4 * k, k * 1024>(t3);
        else tr_a<224 + 4 * k, (k - 5) * 1024>(k < 7 ? t4x : t4y);
      }
      if constexpr (f + 6 < 60) rd(IC<100 + f + 6>{}, bf[(f + 6) & 7]);
      mf16_va<ks == 0, (2 * ks + 1) * 4>(aq[t][1], bf[f & 7]);
      if constexpr (f >= 7 && f < 23 && (f & 1)) {
        constexpr int k = (f - 7) / 2;
        if constexpr (k < 5) { st_a<224 + 4 * k>(d3 + d3s * ((5 * r + k) >> 1), ws, WS_W3T + c * 286720 + (5 * r + k) * 512); mf16_va<k == 0, 224 + 4 * k>(cs3, sel3); }
        else if constexpr (k < 7) st_a<224 + 4 * k>(d4x, ws, d4[(k - 5) & 1]);
        else st_a<224 + 4 * k>(d4y, ws, 0);
      }
      if constexpr (f == 24) {
        pin(cs3);
        st128c(__builtin_bit_cast(v4u, cs3), dc3, ws, WS_CS3 + (c * 16 + r) * 1024);
      }
      if constexpr (f >= 26 && f % 4 == 2 && f < 58) ldw2(IC<8 + (f - 26) / 4>{});
      if constexpr (f == 32) ld128c<0>(xr[0], xo16, rx, 0);
      if constexpr (f == 41) ld128c<64>(xr[1], xo16, rx, 0);
      if constexpr (f == 50) ld64c<128>(xr4, xo8, rx, 0);
      if constexpr (f == 58) ld32c<0>(pf, pvk, ws, 0);
    SE
  }
  STAMP(7);
  const unsigned w4src = WS_W4T + j * 286720 + ldsw;
  auto pfx = [&](auto i_) __attribute__((always_inline)) {
    constexpr int i = decltype(i_)::value;
    if constexpr (i < 30) { unit_dma<8 + i / 5, i % 5, slot_of(9 + i / 5)>(ws, lane16, lane16b, w3src + (i / 5) * SLOT, ldsw); if constexpr (i % 5 >= 3) NOP1(); }
    else { constexpr int k = i - 30; ld_v<VRES + 4 * k, (k % 5 < 4 ? k % 5 : 3) * 1024>(k % 5 < 4 ? lane16 : lane16b, ws, w4src + (k / 5) * SLOT); }
  };
  f32x4 acc2[4][5];
  {
    float nb[2], ra[2]; f32x4 cs[3];
    SF(2, m) nb[m] = *(LAS float*)(L + L_MEAN + (32 * w + 16 * m + li) * 4); ra[m] = *(LAS float*)(L + L_RSTD + (32 * w + 16 * m + li) * 4); SE
    SF(3, t) cs[t] = *(LAS f32x4*)(L + L_CS1 + (80 + 16 * t + 4 * lg) * 4); SE
    asm volatile("s_nop 7" ::: "memory");
    FENCE();
    unsigned pk[2][3][2];
    auto qepi = [&](auto m_, auto t_) __attribute__((always_inline)) {
      constexpr int m = decltype(m_)::value, t = decltype(t_)::value;
      pin(aq[t][m]);
      float v[4];
      SF(4, e) v[e] = phi1(fmaf(aq[t][m][e], ra[m], fmaf(nb[m], cs[t][e], 1e-6f))); SE
      pk[m][t][0] = cvtpk(v[0], v[1]); pk[m][t][1] = cvtpk(v[2], v[3]);
      pin(pk[m][t][0]); pin(pk[m][t][1]);
    };
    SF(3, t) qepi(IC<0>{}, IC<t>{}); SE
    unsigned gv = tid < 210 ? (unsigned)tid * 16 : OOB;
    unsigned kw, k0 = L_KVT + (8 * tq + tp) * 96 + lg * 8, k2 = L_KVT + (li >= 8 ? 24 + li : 42) * 96 + lg * 8, k3 = L_KVT + (tp < 2 ? 40 + tp : 42) * 96 + lg * 8;
    {
      const int hd = tid >= 105, u = tid - 105 * hd, e = (u * 205) >> 10, dq = u - 5 * e + 5 * hd;
      kw = L_KVT + (tid >= 210 ? 43 * 96 : (e == 20 ? 40 + hd : e + 20 * hd) * 96 + dq * 8);
    }
    pin(gv); pin(kw); pin(k0); pin(k2); pin(k3);
    poll_chk<0>(pf, ws, pvk, 0, tag, budget);
    pin(xr[0]); pin(xr[1]); pin(xr4);
    STAMP(8);
    v4u g[16];
    SF(16, k)
      ld128c<0>(g[k], gv, ws, WS_KVP + (c * 16 + k) * 4096);
      if constexpr (k == 3 || k == 7 || k == 11) qepi(IC<1>{}, IC<k / 4>{});
    SE
    SF(2, m)
      pq[m][0] = v4u{pk[m][0][0], pk[m][0][1], pk[m][1][0], pk[m][1][1]};
      pq[m][1] = v4u{lg < 2 ? pk[m][2][0] : 0u, lg < 2 ? pk[m][2][1] : 0u, 0u, 0u};
      pin(pq[m][0]); pin(pq[m][1]);
    SE
    asm volatile("v_accvgpr_write_b32 a106, 0\n\tv_accvgpr_write_b32 a107, 0\n\tv_accvgpr_write_b32 a114, 0\n\tv_accvgpr_write_b32 a115, 0\n\t"
                 "v_accvgpr_write_b32 a122, 0\n\tv_accvgpr_write_b32 a123, 0\n\tv_accvgpr_write_b32 a130, 0\n\tv_accvgpr_write_b32 a131, 0");
    STAMP(9);
    f32x4 s;
    SF(4, q)
      ldw2(IC<16 + q>{});
      WAITV(13 - 3 * q);
      SF(4, k) pin(g[4 * q + k]); SE
      const f32x4 t4 = (__builtin_bit_cast(f32x4, g[4 * q]) + __builtin_bit_cast(f32x4, g[4 * q + 1])) + (__builtin_bit_cast(f32x4, g[4 * q + 2]) + __builtin_bit_cast(f32x4, g[4 * q + 3]));
      if constexpr (q == 0) s = t4; else s += t4;
      pin(s);
    SE
    *(LAS v2u*)(L + kw) = v2u{cvtpk(s[0], s[1]), cvtpk(s[2], s[3])};
    BARRIER_L();
    publish(vtag, fl_p, ws, flag_off(F_W4, j));
    STAMP(10);
    SF(4, t1) constexpr int t = (t1 + 3) & 3; SF(3, p) dsr64a<100 + 8 * t + 2 * p, (t == 1 ? 4 * 96 : 0) + p * 32>(t < 2 ? k0 : t == 2 ? k2 : k3); SE SE
    f32x4 od[2][4];
    SF(4, t1)
      constexpr int t = (t1 + 3) & 3;
      WL(9 - 3 * t1);
      SF(2, m) mf16_av<true, 100 + 8 * t>(od[m][t], pq[m][0]); SE
      SF(2, m) mf16_av<false, 104 + 8 * t>(od[m][t], pq[m][1]); SE
      ldw2(IC<20 + t1>{});
    SE
    asm volatile("s_nop 7");
    FENCE();
    SF(2, m)
      SF(4, t) pin(od[m][t]); SE
      const float r0 = __builtin_amdgcn_rcpf(od[m][3][0] + 1e-6f), r1 = __builtin_amdgcn_rcpf(od[m][3][1] + 1e-6f);
      const float ra = lg < 3 ? r0 : r1, rb = lg < 2 ? r0 : r1;
      const v4u o = {cvtpk(od[m][0][0] * ra, od[m][0][1] * ra), cvtpk(od[m][0][2] * ra, od[m][0][3] * ra),
                     cvtpk(od[m][1][0] * rb, od[m][1][1] * rb), cvtpk(od[m][1][2] * rb, od[m][1][3] * rb)};
      const v2u o2 = {cvtpk(od[m][2][0] * r1, od[m][2][1] * r1), cvtpk(od[m][2][2] * r1, od[m][2][3] * r1)};
      const unsigned ob = WS_O + (8 * r + 2 * w + m) * 20480;
      st128c(o, lane16, ws, ob + c * 1024);
      st64c(o2, lg >= 2 ? (unsigned)(li * 16 + (lg - 2) * 8) : OOB, ws, ob + (16 + (c >> 2)) * 1024 + (c & 3) * 256);
    SE
    ld32c<0>(pf, pv2, ws, F_W4 * 4096);
    ldw2(IC<24>{});
    SF(5, t)
      const unsigned x0 = t < 4 ? xr[(t >> 1) & 1][(t & 1) * 2] : xr4[0], x1 = t < 4 ? xr[(t >> 1) & 1][(t & 1) * 2 + 1] : xr4[1];
      acc2[3][t] = f32x4{bflo(x0), bfhi(x0), bflo(x1), bfhi(x1)};
      pin(acc2[3][t]);
    SE
    WAITV(2);
    BARRIER();
    publish(vtag, fl_c, ws, flag_off(F_O, r));
    STAMP(11);
    poll_chk<2>(pf, ws, pv2, F_W4 * 4096, tag, budget);
    SF(14, k) pfx(IC<k>{}); SE
    poll_w0(ws, flag_off(F_O, r) + li * 4, tag, budget, w);
    st32c(vtag, (b == 0 && tid == 0) ? 0u : OOB, ws, 0);
    BARRIER();
  }
  STAMP(12);
  const int mt0 = 8 * r + 4 * hf;
  const bool dead = w >= 2;
  auto ldw4a = [&](auto i_) __attribute__((always_inline)) {
    constexpr int i = decltype(i_)::value;
    ld_a<160 + 4 * i, (i % 5 < 4 ? i % 5 : 3) * 1024>(i % 5 < 4 ? lane16 : lane16b, ws, (i >= 15 && dead) ? OOB : w4src + (10 + i / 5) * SLOT);
  };
  v4u hp[2]; v2u hp4;
  const unsigned pb0 = lb0 + ldsw, pb1 = lb1 + ldsw, pb2 = lb2 + ldsw;
  {
    unsigned cs3p[16];
    auto ldo = [&](auto i_) __attribute__((always_inline)) {
      constexpr int i = decltype(i_)::value, mm = i / 5, kk = i % 5;
      ld_a<4 + i * 4, (kk < 4 ? kk : 3) * 1024>(kk < 4 ? lane16 : lane16b, ws, WS_O + (mt0 + ((w + mm + 1) & 3)) * 20480 + ldsw);
    };
    constexpr int UP = 10;
    SF(UP, i) ldo(IC<i>{}); SE
    f32x4 pr[3][5];
    unsigned rb[3];
    SF(3, mm) rb[mm] = slot_of(12 + mm) + ((w + 3 - mm) & 3) * 5120 + lane16; pin(rb[mm]); SE
    SF(20, i)
      constexpr int mm = i / 5, kk = i % 5;
      WAITV(i + UP <= 20 ? UP - 1 : 19 - i + (i - 10));
      if constexpr (i == 17) BARRIER_L();
      SF(5, t)
        constexpr int fb = kk * 5 + t;
        mf16_aa<(kk == 0 && mm < 3), (fb < 24 ? 160 + 4 * fb : 0), 4 + i * 4>(acc2[mm][t]);
        if constexpr (t == 0 && i + UP < 20) ldo(IC<i + UP>{});
        if constexpr (i >= 10 && t == 0) pfx(IC<(i < 16 ? 14 + i - 10 : 30 + i - 16)>{});
        if constexpr (mm > 0 && kk == 1) {
          constexpr int sl = (12 + mm - 1) % 7;
          dsw128f<(sl % 3) * SLOT + t * 1024>(acc2[mm - 1][t], sl < 3 ? pb0 : sl < 6 ? pb1 : pb2);
        }
        if constexpr (i == 17) { dsr128f<t * 1024>(pr[0][t], rb[0]); dsr128f<t * 1024>(pr[1][t], rb[1]); }
        if constexpr (i == 18) dsr128f<t * 1024>(pr[2][t], rb[2]);
        if constexpr (i == 19) { WL(4 - t); pin(pr[0][t]); pin(pr[1][t]); pin(pr[2][t]); pr[0][t] = (pr[0][t] + pr[1][t]) + pr[2][t]; pin(pr[0][t]); }
      SE
    SE
    STAMP(13);
    asm volatile("s_nop 3");
    f32x4 v[5];
    SF(5, t) pin(acc2[3][t]); v[t] = pr[0][t] + acc2[3][t]; SE
    SF(2, k) hp[k] = v4u{cvtpk(v[2 * k][0], v[2 * k][1]), cvtpk(v[2 * k][2], v[2 * k][3]), cvtpk(v[2 * k + 1][0], v[2 * k + 1][1]), cvtpk(v[2 * k + 1][2], v[2 * k + 1][3])}; SE
    hp4 = v2u{cvtpk(v[4][0], v[4][1]), cvtpk(v[4][2], v[4][3])};
    const v4u h4 = {hp4.x, hp4.y, 0u, 0u};
    f32x4 g2, s2;
    mf16_vv<true>(g2, hp[0], hp[0]); mf16_vv<true>(s2, ones, hp[0]);
    mf16_vv<false>(g2, hp[1], hp[1]); mf16_vv<false>(s2, ones, hp[1]);
    mf16_vv<false>(g2, h4, h4); mf16_vv<false>(s2, ones, h4);
    const int mt = mt0 + w;
    const unsigned hb = WS_H + (mt >> 1) * 40960 + 5 * j * 1024 + (mt & 1) * 256;
    const unsigned hv = (lg >> 1) * 1024 + (lg & 1) * 512 + li * 16;
    st128c(hp[0], hv, ws, hb); st128c(hp[1], hv, ws, hb + 2048);
    st64c(hp4, (lg >> 1) * 512 + li * 16 + (lg & 1) * 8, ws, hb + 4096);
    asm volatile("s_nop 1" : "+v"(g2), "+v"(s2));
    const float sq = sel4(g2, tp), sv = s2[0];
    st64c(v2u{__builtin_bit_cast(unsigned, sv), __builtin_bit_cast(unsigned, sq)}, lg == tq ? (unsigned)li * 8 : OOB, ws, WS_HS + (j * 2048 + 16 * mt) * 8);
    SF(16, k) ld32c<(k & 3) * 1024>(cs3p[k], (unsigned)tid * 4, ws, WS_CS3 + c * 16384 + (k >> 2) * 4096); SE
    SF(4, k) pfx(IC<34 + k>{}); SE
    WAITV(20);
    BARRIER();
    publish(vtag, fl_c, ws, flag_off(F_H, r));
    STAMP(14);
    SF(10, k) pfx(IC<20 + k>{}); SE
    SF(2, k) pfx(IC<38 + k>{}); SE
    {
      WAITV(17);
      SF(16, k) pin(cs3p[k]); SE
      float q4[4];
      SF(4, k) q4[k] = (asf(cs3p[4 * k]) + asf(cs3p[4 * k + 1])) + (asf(cs3p[4 * k + 2]) + asf(cs3p[4 * k + 3])); SE
      unsigned c1, c2, c3;
      split3((q4[0] + q4[1]) + (q4[2] + q4[3]), c1, c2, c3);
      *(LAS v4u*)(L + L_RED + ((tid & 7) * 32 + (tid >> 3)) * 16) = v4u{c1 | (c1 >> 16), c2 | (c2 >> 16), c3 | (c1 >> 16), c3 | (c2 >> 16)};
    }
    poll_w0(ws, flag_off(F_H, r) + li * 4, tag, budget, w);
    BARRIER();
  }
  STAMP(15);
  unsigned va[4];
  auto lda = [&](auto u_, auto mm_) __attribute__((always_inline)) {
    constexpr int u = decltype(u_)::value, mm = decltype(mm_)::value;
    ld_a<areg(u, mm), 0>(va[mm], ws, (u == 13 && dead) ? OOB : WS_ACT + u * 4096);
  };
  unsigned pa = flag_off(F_A0, r) + li * 4;
  {
    v2u hs[4];
    const unsigned hsrc = WS_H + (4 * r + w) * 40960;
    SF(4, s) ld_a<4 * s, (s & 3) * 1024>(lane16, ws, hsrc); SE
    v4u bf[6];
    SF(4, k) rd(IC<160 + k>{}, bf[k]); SE
    SF(4, k) ld64c<0>(hs[k], (unsigned)(((lane >> 5) * 4 * 2048 + (lane & 31)) * 8), ws, WS_HS + (k * 2048 + 128 * r + 32 * w) * 8); SE
    SF(P3_HUP - 4, s) ld_a<4 * (s + 4), (s & 3) * 1024>(lane16, ws, hsrc + ((s + 4) >> 2) * 4096); SE
    f32x16 acc[4];
    v4u cf[3], mf;
    float c1, kk2, eP[2], eT[2], eO[8];
    unsigned ov[4];
    unsigned csa = L_RED + (lane & 31) * 16;
    unsigned av = ((lane >> 4) & 1) * 57344 + (lane >> 5) * 256 + li * 16;
    SF(4, mm) va[mm] = lane16 + (mt0 + ((w + mm + 1) & 3)) * 57344 + w * 1024; pin(va[mm]); SE
    pin(csa); pin(av); pin(pa);
    const unsigned ab = WS_ACT + (8 * r + 2 * w) * 57344;
    auto epi = [&](auto p_, auto q_) __attribute__((always_inline)) {
      constexpr int p = decltype(p_)::value, q = decltype(q_)::value;
      f32x16& a = acc[p % 4];
      if constexpr (q < 24) {
        constexpr int e = q / 3, ph = q % 3, x = e & 1, y = x ^ 1;
        const float ag = a[e], au = a[8 + e];
        if constexpr (ph == 0) {
          if constexpr (e == 0) asm volatile("v_mul_f32 %0, %1, %2" : "=v"(eT[x]) : "v"(ag), "v"(c1));
          else asm volatile("v_rcp_f32 %1, %1\n\tv_mul_f32 %0, %2, %3" : "=v"(eT[x]), "+v"(eT[y]) : "v"(ag), "v"(c1));
        } else if constexpr (ph == 1) {
          if constexpr (e == 0) asm volatile("v_exp_f32 %0, %0" : "+v"(eT[x]));
          else asm volatile("v_exp_f32 %0, %0\n\tv_mul_f32 %1, %2, %3" : "+v"(eT[x]), "=v"(eO[e - 1]) : "v"(eP[y]), "v"(eT[y]));
        } else {
          if constexpr (e >= 2 && !(e & 1)) asm volatile("v_mul_f32 %0, %3, %4\n\tv_fma_f32 %1, %1, %5, %5\n\tv_cvt_pk_bf16_f32 %2, %6, %7" : "=&v"(eP[x]), "+v"(eT[x]), "=v"(ov[(e - 2) >> 1]) : "v"(ag), "v"(au), "v"(kk2), "v"(eO[e - 2]), "v"(eO[e - 1]));
          else asm volatile("v_mul_f32 %0, %2, %3\n\tv_fma_f32 %1, %1, %4, %4" : "=&v"(eP[x]), "+v"(eT[x]) : "v"(ag), "v"(au), "v"(kk2));
        }
      } else if constexpr (q == 24) asm volatile("v_rcp_f32 %0, %0\n\ts_nop 0" : "+v"(eT[1]));
      else if constexpr (q == 25) asm volatile("v_mul_f32 %1, %1, %2\n\tv_cvt_pk_bf16_f32 %0, %3, %1" : "=v"(ov[3]), "+v"(eP[1]) : "v"(eT[1]), "v"(eO[6]));
      else if constexpr (q == P3_EPI_ST) {
        unsigned so;
        if constexpr (p == 6) so = c < 12 ? ab + (48 + (c >> 1)) * 1024 + (c & 1) * 512 : OOB;
        else so = ab + (16 * (p >> 1) + c) * 1024 + (p & 1) * 512;
        st128c(v4u{ov[0], ov[1], ov[2], ov[3]}, av, ws, so);
      }
    };
    SF(280, g)
      constexpr int p = g < 120 ? g % 3 : g / 40, s = g < 120 ? g / 3 : g % 40;
      if constexpr (g == 120 || g == 200) STAMP(g == 120 ? 28 : 29);
      if constexpr (g == P3_CHK_A0) poll_chk<p3_cnt(P3_POLL_A0, g)>(pf, ws, pa, 0, tag, budget);
      if constexpr (g % 20 == 16) {
        constexpr int nu = 8 + g / 20 + 1;
        if constexpr (g == P3_FL0) WAITV(p3_cnt(p3_st_step(1), g));
        else if constexpr (g == P3_FL1) WAITV(p3_cnt(p3_st_step(3), g));
        else if constexpr (g == P3_FL2) WAITV(p3_cnt(p3_st_step(5), g));
        else if constexpr (nu >= 14 && nu < 22) WAITV(p3_cnt(p3_dma_start(nu) + 12, g));
        BARRIER();
      }
      if constexpr (g < 120 && g % 3 == 0) {
        if constexpr (s < 4) WAITV(P3_HUP + 3 - s + p3_cnt(-1, g));
        else if constexpr (s < P3_HUP) WAITV(P3_HUP - 1 - s + p3_cnt(-1, g));
        else WAITV(p3_cnt(2 * (s - P3_HUP) + 1, g));
      }
      WL(g + 4 < 280 ? 3 : 279 - g);
      mf32_va<s == 0, 4 * s>(acc[p % 4], bf[g % 6]);
      if constexpr (s == 34) dsr128<p * 512>(cf[p % 3], csa);
      if constexpr (g + 4 < 280) rd(IC<160 + g + 4>{}, bf[(g + 4) % 6]);
      if constexpr (g == P3_FL0) publish(vtag, fl_c, ws, flag_off(F_A0, r));
      if constexpr (g == P3_FL1) publish(vtag, fl_c, ws, flag_off(F_A1, r));
      if constexpr (g == P3_FL2) publish(vtag, fl_c, ws, flag_off(F_A2, r));
      if constexpr (p3_hload(g)) { constexpr int hsx = (g >> 1) + P3_HUP; ld_a<4 * hsx, (hsx & 3) * 1024>(lane16, ws, hsrc + (hsx >> 2) * 4096); }
      if constexpr (p3_dma_at(g) >= 0) {
        constexpr int nu = p3_dma_at(g) / 8, f = p3_dma_at(g) % 8;
        unit_dma<nu, f, slot_of(nu + 1)>(ws, lane16, lane16b, nu < 22 ? w3src + (nu - 8) * SLOT : w4src + (nu - 18) * SLOT, ldsw);
      }
      if constexpr (p3_w4a_at(g) >= 10) ldw4a(IC<p3_w4a_at(g) - 10>{});
      else if constexpr (p3_w4a_at(g) >= 0) pfx(IC<40 + p3_w4a_at(g)>{});
      if constexpr (p3_aload(g)) lda(IC<(g - P3_AL0) / 8>{}, IC<((g - P3_AL0) / 2) % 4>{});
      if constexpr (g == P3_POLL_A0) ld32c<0>(pf, pa, ws, 0);
      if constexpr (g == P3_POLL_A1) ld32c<0>(pf, pa, ws, 4096);
      if constexpr (g == 13) {
        SF(4, k) pin(hs[k]); SE
        const float s1 = xsum32((asf(hs[0].x) + asf(hs[1].x)) + (asf(hs[2].x) + asf(hs[3].x)));
        const float s2 = xsum32((asf(hs[0].y) + asf(hs[1].y)) + (asf(hs[2].y) + asf(hs[3].y)));
        const float mean = s1 * (1.f / 640.f);
        kk2 = fmaxf(s2 * (1.f / 640.f) - mean * mean, 0.f) + 1e-5f;
        c1 = -1.44269504f * __builtin_amdgcn_rsqf(kk2);
        unsigned m1, m2, m3;
        split3(lane < 32 ? -mean : 0.f, m1, m2, m3);
        mf = v4u{m2 | (m1 >> 16), m2 | (m1 >> 16), m1 | (m3 >> 16), m2 | (m3 >> 16)};
        pin(kk2); pin(c1); pin(mf);
      }
      SF(5, ep) if constexpr (g >= p3_epi_start(ep) && g <= p3_epi_start(ep) + P3_EPI_ST) epi(IC<ep>{}, IC<g - p3_epi_start(ep)>{}); SE
      if constexpr (g >= 246 && g < 260) { epi(IC<5>{}, IC<2 * (g - 246)>{}); epi(IC<5>{}, IC<2 * (g - 246) + 1>{}); }
      if constexpr (s == 39) mf32_vv(acc[p % 4], cf[p % 3], mf);
    SE
    STAMP(16);
    asm volatile("s_nop 11");
    {
      f32x16& a = acc[2];
      pin(a);
      float o[8];
      SF(8, e) o[e] = a[e] * a[8 + e] * __builtin_amdgcn_rcpf(fmaf(__builtin_amdgcn_exp2f(a[e] * c1), kk2, kk2)); SE
      SF(4, k) ov[k] = cvtpk(o[2 * k], o[2 * k + 1]); SE
      epi(IC<6>{}, IC<P3_EPI_ST>{});
    }
    poll_chk<1>(pf, ws, pa, 4096, tag, budget);
    STAMP(17);
    STAMP(18);
  }
  {
    f32x4 acc[4][5];
    v4u bf[2][5];
    auto rdw = [&](auto u_, auto t_) __attribute__((always_inline)) {
      constexpr int u = decltype(u_)::value, t = decltype(t_)::value, sl = (18 + u) % 7;
      dsr128<(sl % 3) * SLOT + t * 1024>(bf[u & 1][t], sl < 3 ? pb0 : sl < 6 ? pb1 : pb2);
    };
    SF(5, t)
      const unsigned x0 = t < 4 ? hp[(t >> 1) & 1][(t & 1) * 2] : hp4[0], x1 = t < 4 ? hp[(t >> 1) & 1][(t & 1) * 2 + 1] : hp4[1];
      acc[3][t] = f32x4{bflo(x0), bfhi(x0), bflo(x1), bfhi(x1)};
      pin(acc[3][t]);
    SE
    SF(11, u)
      if constexpr (u == 2) { WAITV(12); BARRIER(); publish(vtag, fl_c, ws, flag_off(F_A3, r)); }
      if constexpr (u == 4) poll_chk<6>(pf, ws, pa, 8192, tag, budget);
      if constexpr (u == 7) WAITV(13);
      if constexpr (u == 8) WAITV(4);
      if constexpr (u == 9) poll_chk<0>(pf, ws, pa, 12288, tag, budget);
      if constexpr (u >= 4 && u < 10) WL(0);
      SF(4, mm) SF(5, t)
        if constexpr (u == 0 && mm < 3) mf16_lva0<VRES + 4 * t, areg(u, mm)>(acc[mm][t]);
        else if constexpr (u < 4) mf16_lva<VRES + 20 * u + 4 * t, areg(u, mm)>(acc[mm][t]);
        else if constexpr (u < 10) mf16_va<false, areg(u, mm)>(acc[mm][t], bf[u & 1][t]);
        else mf16_aa<false, 160 + 20 * (u - 10) + 4 * t, areg(u, mm)>(acc[mm][t]);
        constexpr int i = mm * 5 + t;
        if constexpr (u >= 3 && u < 9 && i < 10 && (i & 1)) rdw(IC<u + 1>{}, IC<i / 2>{});
        if constexpr (i == 0 && u == 3) ld32c<0>(pf, pa, ws, 8192);
        if constexpr (i == 0 && u == 8) ld32c<0>(pf, pa, ws, 12288);
        if constexpr (u < 8 && i < 12 && i % 3 == 2) lda(IC<u + 4>{}, IC<i / 3>{});
        if constexpr (u == 9 && i < 16 && !(i & 1)) lda(IC<12 + i / 8>{}, IC<(i / 2) % 4>{});
        if constexpr (u < 7 && (i == 13 || i == 16 || (i == 19 && u == 5))) {
          constexpr int k = 2 * u + (u > 5) + (i - 13) / 3;
          unit_dma<25 + k / 5, k % 5, slot_of(26 + k / 5)>(ws, lane16, lane16b, w4src + (7 + k / 5) * SLOT, ldsw);
        }
      SE SE
    SE
    f32x4 pr[3][5];
    unsigned rb[3];
    SF(3, mm) rb[mm] = mm * SLOT + ((w + 3 - mm) & 3) * 5120 + lane16; pin(rb[mm]); SE
    SF(4, mm)
      SF(5, t)
        mf16_aa<false, 180 + 4 * t, areg(11, mm)>(acc[mm][t]);
        if constexpr (mm > 0 && t >= 3) dsw128f<(mm - 1) * SLOT + (t - 3) * 1024>(acc[mm - 1][t - 3], pb0);
      SE
      if constexpr (mm == 0) WAITV(4);
      SF(5, t)
        mf16_aa<false, 200 + 4 * t, areg(12, mm)>(acc[mm][t]);
        if constexpr (mm > 0 && t < 3) dsw128f<(mm - 1) * SLOT + (t + 2) * 1024>(acc[mm - 1][t + 2], pb0);
      SE
      if constexpr (mm == 0) WAITV(0);
      if constexpr (mm == 3) BARRIER_L();
      SF(5, t)
        mf16_aa<false, 220 + 4 * t, areg(13, mm)>(acc[mm][t]);
        if constexpr (mm == 3) { SF(3, k) dsr128f<t * 1024>(pr[k][t], rb[k]); SE }
      SE
    SE
    STAMP(19);
    f32x4 v[5];
    SF(5, t)
      WL(12 - 3 * t);
      pin(pr[0][t]); pin(pr[1][t]); pin(pr[2][t]); pin(acc[3][t]);
      v[t] = ((pr[0][t] + pr[1][t]) + pr[2][t]) + acc[3][t];
    SE
    const rsrc_t ro = mk(ptr2(ka[12], ka[13]), 2048 * 640 * 2);
    const unsigned oo = ((128 * r + 64 * hf + 16 * w + li) * 640 + 80 * j) * 2;
    SF(2, k) st128p(v4u{cvtpk(v[2 * k][0], v[2 * k][1]), cvtpk(v[2 * k][2], v[2 * k][3]), cvtpk(v[2 * k + 1][0], v[2 * k + 1][1]), cvtpk(v[2 * k + 1][2], v[2 * k + 1][3])}, oo + lg * 16, ro, k * 64); SE
    st64p(v2u{cvtpk(v[4][0], v[4][1]), cvtpk(v[4][2], v[4][3])}, oo + lg * 8, ro, 128);
  }
  STAMP(20);
  asm volatile("fbk_end:");
}

extern "C" void kernel_launch(void* const* d_in, const int* in_sizes, int n_in, void* d_out, int out_size, void* d_ws, size_t ws_size, hipStream_t stream) {
  (void)in_sizes; (void)n_in; (void)out_size; (void)ws_size;
  (void)hipFuncSetAttribute((const void*)fused_block, hipFuncAttributeMaxDynamicSharedMemorySize, (int)LDS_BYTES);
  fused_block<<<256, 256, LDS_BYTES, stream>>>(d_in[0], d_in[1], d_in[2], d_in[4], d_in[5], d_in[6], d_out, (char*)d_ws);
}
```

```cpp
#include <hip/hip_runtime.h>
#include <utility>
#include <type_traits>

#define DI __device__ __forceinline__
#define LAS __attribute__((address_space(3)))
typedef unsigned v4u __attribute__((ext_vector_type(4)));
typedef unsigned v2u __attribute__((ext_vector_type(2)));
typedef float f32x2 __attribute__((ext_vector_type(2)));
typedef float f32x4 __attribute__((ext_vector_type(4)));
typedef float f32x16 __attribute__((ext_vector_type(16)));
typedef __bf16 bf16x2 __attribute__((ext_vector_type(2)));
typedef __amdgpu_buffer_rsrc_t rsrc_t;

template <int N> using IC = std::integral_constant<int, N>;
template <class Fn, int... Is> DI void sfor_impl(Fn&& f, std::integer_sequence<int, Is...>) { (f(IC<Is>{}), ...); }
template <int N, class Fn> DI void sfor(Fn&& f) { sfor_impl(f, std::make_integer_sequence<int, N>{}); }
#define SF(N, i) sfor<N>([&](auto i##_) __attribute__((always_inline)) { constexpr int i = decltype(i##_)::value;
#define SE });

constexpr unsigned WS_FLAG = 4096;
constexpr unsigned WS_W1T = 65536;
constexpr unsigned WS_CS1 = WS_W1T + 16 * 163840;
constexpr unsigned WS_W2T = WS_CS1 + 16 * 16 * 512;
constexpr unsigned WS_W3T = WS_W2T + 8 * 102400;
constexpr unsigned WS_CS3 = WS_W3T + 16 * 286720;
constexpr unsigned WS_W4T = WS_CS3 + 16 * 16 * 1024;
constexpr unsigned WS_KVP = WS_W4T + 8 * 286720;
constexpr unsigned WS_O = WS_KVP + 16 * 16 * 4096;
constexpr unsigned WS_H = WS_O + 128 * 20480;
constexpr unsigned WS_HS = WS_H + 64 * 40960;
constexpr unsigned WS_ACT = WS_HS + 8 * 2048 * 8;
constexpr unsigned WS_END = WS_ACT + 128 * 57344;
constexpr unsigned OOB = 0x40000000u;
enum { F_W1 = 0, F_W4 = 1, F_KV = 2, F_O = 3, F_H = 4, F_A0 = 5, F_A1 = 6, F_A2 = 7, F_A3 = 8 };
DI unsigned flag_off(int kind, int grp) { return WS_FLAG + (kind * 16 + grp) * 256; }

constexpr unsigned SLOT = 20480;
constexpr unsigned L_XST = 3 * SLOT;
constexpr unsigned L_MISC = 143360;
constexpr unsigned L_RED = L_MISC;
constexpr unsigned L_KVT = L_MISC + 13440;
constexpr unsigned L_MEAN = L_MISC + 17792, L_RSTD = L_MEAN + 512, L_CS1 = L_RSTD + 512;
constexpr unsigned LDS_BYTES = 163840;

#define WAITV(n) asm volatile("s_waitcnt vmcnt(%0)" ::"i"(n) : "memory")
#define WAITL(n) asm volatile("s_waitcnt lgkmcnt(%0)" ::"i"(n) : "memory")
#define BARRIER() asm volatile("s_barrier" ::: "memory")
#define BARRIER_L() asm volatile("s_waitcnt lgkmcnt(0)\n\ts_barrier" ::: "memory")
#define FENCE() __builtin_amdgcn_sched_barrier(0)

template <int C> DI void set_m0(unsigned base) { asm volatile("s_add_u32 m0, %0, %c1\n\ts_nop 0" ::"s"(base), "i"(C) : "scc", "memory"); }
template <int IMM, bool SC1, int NEXT> DI void dma(unsigned voff, rsrc_t rs, unsigned soff, unsigned nb) {
  if constexpr (SC1) {
    if constexpr (NEXT >= 0) asm volatile("buffer_load_dwordx4 %0, %1, %2 offen offset:%c3 sc1 lds\n\ts_add_u32 m0, %4, %c5" ::"v"(voff), "s"(rs), "s"(soff), "i"(IMM), "s"(nb), "i"(NEXT) : "scc", "memory");
    else asm volatile("buffer_load_dwordx4 %0, %1, %2 offen offset:%c3 sc1 lds" ::"v"(voff), "s"(rs), "s"(soff), "i"(IMM) : "memory");
  } else {
    if constexpr (NEXT >= 0) asm volatile("buffer_load_dwordx4 %0, %1, %2 offen offset:%c3 lds\n\ts_add_u32 m0, %4, %c5" ::"v"(voff), "s"(rs), "s"(soff), "i"(IMM), "s"(nb), "i"(NEXT) : "scc", "memory");
    else asm volatile("buffer_load_dwordx4 %0, %1, %2 offen offset:%c3 lds" ::"v"(voff), "s"(rs), "s"(soff), "i"(IMM) : "memory");
  }
}
#define NOP1() asm volatile("s_nop 0")
template <int R, int IMM> DI void ld_a(unsigned voff, rsrc_t rs, unsigned soff) {
  asm volatile("buffer_load_dwordx4 a[%c0:%c1], %2, %3, %4 offen offset:%c5 sc1" ::"i"(R), "i"(R + 3), "v"(voff), "s"(rs), "s"(soff), "i"(IMM) : "memory");
}
template <int IMM> DI void ld128c(v4u& d, unsigned voff, rsrc_t rs, unsigned soff) {
  asm volatile("buffer_load_dwordx4 %0, %1, %2, %3 offen offset:%c4 sc1" : "=v"(d) : "v"(voff), "s"(rs), "s"(soff), "i"(IMM) : "memory");
}
template <int IMM> DI void ld64c(v2u& d, unsigned voff, rsrc_t rs, unsigned soff) {
  asm volatile("buffer_load_dwordx2 %0, %1, %2, %3 offen offset:%c4 sc1" : "=v"(d) : "v"(voff), "s"(rs), "s"(soff), "i"(IMM) : "memory");
}
template <int IMM> DI void ld32c(unsigned& d, unsigned voff, rsrc_t rs, unsigned soff) {
  asm volatile("buffer_load_dword %0, %1, %2, %3 offen offset:%c4 sc1" : "=v"(d) : "v"(voff), "s"(rs), "s"(soff), "i"(IMM) : "memory");
}
DI void st128c(v4u d, unsigned voff, rsrc_t rs, unsigned soff) {
  asm volatile("buffer_store_dwordx4 %0, %1, %2, %3 offen sc1\n\ts_nop 1" ::"v"(d), "v"(voff), "s"(rs), "s"(soff) : "memory");
}
DI void st64c(v2u d, unsigned voff, rsrc_t rs, unsigned soff) {
  asm volatile("buffer_store_dwordx2 %0, %1, %2, %3 offen sc1" ::"v"(d), "v"(voff), "s"(rs), "s"(soff) : "memory");
}
DI void st32c(unsigned d, unsigned voff, rsrc_t rs, unsigned soff) {
  asm volatile("buffer_store_dword %0, %1, %2, %3 offen sc1" ::"v"(d), "v"(voff), "s"(rs), "s"(soff) : "memory");
}
DI void publish(unsigned tag, unsigned voff, rsrc_t rs, unsigned soff) {
  asm volatile("buffer_atomic_swap %0, %1, %2, %3 offen sc1" ::"v"(tag), "v"(voff), "s"(rs), "s"(soff) : "memory");
}
DI void st128p(v4u d, unsigned voff, rsrc_t rs, unsigned soff) {
  asm volatile("buffer_store_dwordx4 %0, %1, %2, %3 offen\n\ts_nop 1" ::"v"(d), "v"(voff), "s"(rs), "s"(soff) : "memory");
}
DI void st64p(v2u d, unsigned voff, rsrc_t rs, unsigned soff) {
  asm volatile("buffer_store_dwordx2 %0, %1, %2, %3 offen" ::"v"(d), "v"(voff), "s"(rs), "s"(soff) : "memory");
}
template <int OFF> DI void dsr128(v4u& d, unsigned addr) { asm volatile("ds_read_b128 %0, %1 offset:%c2" : "=v"(d) : "v"(addr), "i"(OFF) : "memory"); }
template <int OFF> DI void dsr128f(f32x4& d, unsigned addr) { asm volatile("ds_read_b128 %0, %1 offset:%c2" : "=v"(d) : "v"(addr), "i"(OFF) : "memory"); }
template <int R, int OFF> DI void dsr128a(unsigned addr) { asm volatile("ds_read_b128 a[%c0:%c1], %2 offset:%c3" ::"i"(R), "i"(R + 3), "v"(addr), "i"(OFF) : "memory"); }
template <bool Z, int RA> DI void mf16_av(f32x4& acc, const v4u& b) {
  if constexpr (Z) asm volatile("v_mfma_f32_16x16x32_bf16 %0, a[%c1:%c2], %3, 0" : "=&v"(acc) : "i"(RA), "i"(RA + 3), "v"(b));
  else asm volatile("v_mfma_f32_16x16x32_bf16 %0, a[%c1:%c2], %3, %0" : "+v"(acc) : "i"(RA), "i"(RA + 3), "v"(b));
}
template <bool Z, int RB> DI void mf16_va(f32x4& acc, const v4u& a) {
  if constexpr (Z) asm volatile("v_mfma_f32_16x16x32_bf16 %0, %3, a[%c1:%c2], 0" : "=&v"(acc) : "i"(RB), "i"(RB + 3), "v"(a));
  else asm volatile("v_mfma_f32_16x16x32_bf16 %0, %3, a[%c1:%c2], %0" : "+v"(acc) : "i"(RB), "i"(RB + 3), "v"(a));
}
template <bool Z, int RA, int RB> DI void mf16_aa(f32x4& acc) {
  if constexpr (Z) asm volatile("v_mfma_f32_16x16x32_bf16 %0, a[%c1:%c2], a[%c3:%c4], 0" : "=&v"(acc) : "i"(RA), "i"(RA + 3), "i"(RB), "i"(RB + 3));
  else asm volatile("v_mfma_f32_16x16x32_bf16 %0, a[%c1:%c2], a[%c3:%c4], %0" : "+v"(acc) : "i"(RA), "i"(RA + 3), "i"(RB), "i"(RB + 3));
}
template <bool Z> DI void mf16_vv(f32x4& acc, const v4u& a, const v4u& b) {
  if constexpr (Z) asm volatile("s_nop 1\n\tv_mfma_f32_16x16x32_bf16 %0, %1, %2, 0" : "=&v"(acc) : "v"(a), "v"(b));
  else asm volatile("s_nop 1\n\tv_mfma_f32_16x16x32_bf16 %0, %1, %2, %0" : "+v"(acc) : "v"(a), "v"(b));
}
template <bool Z, int RB> DI void mf32_va(f32x16& acc, const v4u& a) {
  if constexpr (Z) asm volatile("v_mfma_f32_32x32x16_bf16 %0, %3, a[%c1:%c2], 0" : "=&v"(acc) : "i"(RB), "i"(RB + 3), "v"(a));
  else asm volatile("v_mfma_f32_32x32x16_bf16 %0, %3, a[%c1:%c2], %0" : "+v"(acc) : "i"(RB), "i"(RB + 3), "v"(a));
}
DI void mf32_vv(f32x16& acc, const v4u& a, const v4u& b) { asm volatile("v_mfma_f32_32x32x16_bf16 %0, %1, %2, %0" : "+v"(acc) : "v"(a), "v"(b)); }
template <int RA, int RB> DI void mf16_lva(f32x4& acc) {
  asm volatile("v_mfma_f32_16x16x32_bf16 %0, v[%c1:%c2], a[%c3:%c4], %0" : "+v"(acc) : "i"(RA), "i"(RA + 3), "i"(RB), "i"(RB + 3));
}
template <int RA, int RB> DI void mf16_lva0(f32x4& acc) {
  asm volatile("v_mfma_f32_16x16x32_bf16 %0, v[%c1:%c2], a[%c3:%c4], 0" : "=&v"(acc) : "i"(RA), "i"(RA + 3), "i"(RB), "i"(RB + 3));
}
template <int R, int IMM> DI void ld_v(unsigned voff, rsrc_t rs, unsigned soff) {
  asm volatile("buffer_load_dwordx4 v[%c0:%c1], %2, %3, %4 offen offset:%c5 sc1" ::"i"(R), "i"(R + 3), "v"(voff), "s"(rs), "s"(soff), "i"(IMM) : "memory");
}
template <class T> DI void pin(T& x) { asm volatile("" : "+v"(x)); }

DI unsigned cvtpk(float lo, float hi) { f32x2 v = {lo, hi}; bf16x2 b = __builtin_convertvector(v, bf16x2); return __builtin_bit_cast(unsigned, b); }
DI float asf(unsigned u) { return __builtin_bit_cast(float, u); }
DI float bflo(unsigned u) { return __builtin_bit_cast(float, u << 16); }
DI float bfhi(unsigned u) { return __builtin_bit_cast(float, u & 0xffff0000u); }
DI float phi1(float t1) {
  const float e = __builtin_amdgcn_fmed3f(__builtin_amdgcn_exp2f(fmaf(t1, 1.44269504f, -1.44269504e-6f)), 0.f, 1.f);
  return fmaxf(t1, 1e-6f) + e;
}
template <int R, int OFF> DI void tr_a(unsigned addr) {
  asm volatile("ds_read_b64_tr_b16 a[%c0:%c1], %4 offset:%c5\n\tds_read_b64_tr_b16 a[%c2:%c3], %4 offset:%c6" ::"i"(R), "i"(R + 1), "i"(R + 2), "i"(R + 3), "v"(addr), "i"(OFF), "i"(OFF + 512) : "memory");
}
template <int R> DI void st_a(unsigned voff, rsrc_t rs, unsigned soff) {
  asm volatile("buffer_store_dwordx4 a[%c0:%c1], %2, %3, %4 offen sc1" ::"i"(R), "i"(R + 3), "v"(voff), "s"(rs), "s"(soff) : "memory");
}
template <int OFF> DI void dsw128f(const f32x4& d, unsigned addr) { asm volatile("ds_write_b128 %0, %1 offset:%c2" ::"v"(addr), "v"(d), "i"(OFF) : "memory"); }
DI rsrc_t mk(const void* p, unsigned bytes) { return __builtin_amdgcn_make_buffer_rsrc((void*)p, 0, (int)bytes, 0x00020000); }

#define ACLOB "a0","a1","a2","a3","a4","a5","a6","a7","a8","a9","a10","a11","a12","a13","a14","a15","a16","a17","a18","a19","a20","a21","a22","a23","a24","a25","a26","a27","a28","a29","a30","a31","a32","a33","a34","a35","a36","a37","a38","a39","a40","a41","a42","a43","a44","a45","a46","a47","a48","a49","a50","a51","a52","a53","a54","a55","a56","a57","a58","a59","a60","a61","a62","a63","a64","a65","a66","a67","a68","a69","a70","a71","a72","a73","a74","a75","a76","a77","a78","a79","a80","a81","a82","a83","a84","a85","a86","a87","a88","a89","a90","a91","a92","a93","a94","a95","a96","a97","a98","a99","a100","a101","a102","a103","a104","a105","a106","a107","a108","a109","a110","a111","a112","a113","a114","a115","a116","a117","a118","a119","a120","a121","a122","a123","a124","a125","a126","a127","a128","a129","a130","a131","a132","a133","a134","a135","a136","a137","a138","a139","a140","a141","a142","a143","a144","a145","a146","a147","a148","a149","a150","a151","a152","a153","a154","a155","a156","a157","a158","a159","a160","a161","a162","a163","a164","a165","a166","a167","a168","a169","a170","a171","a172","a173","a174","a175","a176","a177","a178","a179","a180","a181","a182","a183","a184","a185","a186","a187","a188","a189","a190","a191","a192","a193","a194","a195","a196","a197","a198","a199","a200","a201","a202","a203","a204","a205","a206","a207","a208","a209","a210","a211","a212","a213","a214","a215","a216","a217","a218","a219","a220","a221","a222","a223","a224","a225","a226","a227","a228","a229","a230","a231","a232","a233","a234","a235","a236","a237","a238","a239","a240","a241","a242","a243","a244","a245","a246","a247","a248","a249","a250","a251","a252","a253","a254","a255"

DI void poll_w0(rsrc_t ws, unsigned voff, unsigned tag, int& budget, int w) {
  unsigned f;
  asm volatile("s_cmp_lg_u32 %5, 0\n\ts_cbranch_scc1 2f\n"
               "1:\n\tbuffer_load_dword %0, %2, %3, 0 offen sc1\n\ts_waitcnt vmcnt(0)\n\tv_cmp_ne_u32 vcc, %4, %0\n\ts_nop 1\n\ts_cbranch_vccz 2f\n\t"
               "s_sleep 2\n\ts_sub_u32 %1, %1, 1\n\ts_cmp_gt_i32 %1, 0\n\ts_cbranch_scc1 1b\n2:"
               : "=&v"(f), "+s"(budget) : "v"(voff), "s"(ws), "s"(tag), "s"(w) : "vcc", "scc", "memory");
}
template <int N> DI void poll_chk(unsigned f, rsrc_t ws, unsigned voff, unsigned soff, unsigned tag, int& budget) {
  asm volatile("s_waitcnt vmcnt(%c6)\n"
               "1:\n\tv_cmp_ne_u32 vcc, %4, %0\n\ts_nop 1\n\ts_cbranch_vccz 2f\n\t"
               "s_sub_u32 %1, %1, 1\n\ts_cmp_gt_i32 %1, 0\n\ts_cbranch_scc0 2f\n\ts_sleep 4\n\t"
               "buffer_load_dword %0, %2, %3, %5 offen sc1\n\ts_waitcnt vmcnt(0)\n\ts_branch 1b\n2:"
               : "+v"(f), "+s"(budget) : "v"(voff), "s"(ws), "s"(tag), "s"(soff), "i"(N) : "vcc", "scc", "memory");
}

constexpr int slot_of(int u) { return (u % 7) * (int)SLOT; }
template <int U, int FR, int NEXT> DI void unit_dma(rsrc_t ws, unsigned lane16, unsigned lane16b, unsigned soff, unsigned ldsw) {
  if constexpr (FR < 3) dma<FR * 1024, true, -1>(lane16, ws, soff, ldsw);
  else if constexpr (FR == 3) dma<3072, true, slot_of(U) + 1024>(lane16, ws, soff, ldsw);
  else dma<3072, true, NEXT>(lane16b, ws, soff, ldsw);
}

#define WL(n) __builtin_amdgcn_s_waitcnt(0xC07F | ((n) << 8))
template <int R, int OFF> DI void dsr64a(unsigned addr) { asm volatile("ds_read_b64 a[%c0:%c1], %2 offset:%c3" ::"i"(R), "i"(R + 1), "v"(addr), "i"(OFF) : "memory"); }
template <int OFF> DI void dsr32f(float& d, unsigned addr) { asm volatile("ds_read_b32 %0, %1 offset:%c2" : "=v"(d) : "v"(addr), "i"(OFF) : "memory"); }
constexpr unsigned long long kv_mask(int a, int bb) {
  unsigned long long m = 0;
  for (int l = 0; l < 64; ++l) {
    const int li = l & 15, dq = 4 * a + (l >> 4), hd = dq >= 5, eg = bb < 2 ? 16 * bb + li : 24 + li;
    const bool ok = dq < 10 && (bb == 3 ? li == 0 : (bb < 2 || li >= 8) && eg / 20 == hd);
    if (ok) m |= 1ull << l;
  }
  return m;
}
template <int A, int BB, int OFF> DI void dsw_kv(const f32x4& d, unsigned addr) {
  asm volatile("s_mov_b32 exec_lo, %c2\n\ts_mov_b32 exec_hi, %c3\n\tds_write_b128 %0, %1 offset:%c4" ::"v"(addr), "v"(d), "i"((int)(unsigned)kv_mask(A, BB)), "i"((int)(unsigned)(kv_mask(A, BB) >> 32)), "i"(OFF) : "memory");
}
DI float sel4(const f32x4& v, int i) { const float a = (i & 1) ? v[1] : v[0], b = (i & 1) ? v[3] : v[2]; return (i & 2) ? b : a; }
DI float xsum32(float s) {
  const unsigned u = __builtin_bit_cast(unsigned, s);
  const auto r = __builtin_amdgcn_permlane32_swap(u, u, false, false);
  return asf(r[0]) + asf(r[1]);
}
constexpr int VRES = 176;
constexpr int P3_HUP = 6;
constexpr int P3_LASTU = 24;
constexpr int p3_dma_start(int nu) { int d = nu - 15; int a = 85 + 15 * d, b = 36 + 20 * d; return a > b ? a : b; }
constexpr int p3_dma_at(int g) {
  for (int nu = 14; nu <= P3_LASTU; ++nu) { int s = p3_dma_start(nu); if (g >= s && g < s + 15 && (g - s) % 3 == 0) return nu * 8 + (g - s) / 3; }
  return -1;
}
constexpr int p3_w4a_at(int g) { return (g >= 72 && (g - 72) % 6 == 0 && (g - 72) / 6 < 30) ? (g - 72) / 6 : -1; }
constexpr int p3_epi_start(int p) { return p < 5 ? 123 + 24 * p : 246; }
constexpr int P3_EPI_ST = 26;
constexpr int p3_st_step(int p) { return p < 5 ? p3_epi_start(p) + P3_EPI_ST : p3_epi_start(5) + P3_EPI_ST / 2; }
constexpr int P3_FL0 = 196, P3_FL1 = 236, P3_FL2 = 276;
constexpr int P3_POLL_A0 = 232, P3_CHK_A0 = 246, P3_POLL_A1 = 278, P3_AL0 = 247;
constexpr bool p3_hload(int g) { return (g & 1) && (g >> 1) + P3_HUP < 40; }
constexpr bool p3_aload(int g) { return g >= P3_AL0 && g < P3_AL0 + 32 && (g - P3_AL0) % 2 == 0; }
constexpr bool p3_store(int g) { for (int p = 0; p < 6; ++p) if (g == p3_st_step(p)) return true; return false; }
constexpr int p3_nvm(int g) { return p3_hload(g) + (p3_dma_at(g) >= 0) + (p3_w4a_at(g) >= 0) + p3_aload(g) + p3_store(g) + (g == P3_POLL_A0) + (g == P3_POLL_A1) + (g == P3_FL0) + (g == P3_FL1) + (g == P3_FL2); }
constexpr int p3_cnt(int gx, int g) { int n = 0; for (int k = gx + 1; k < g; ++k) n += p3_nvm(k); return n > 63 ? 63 : n; }
DI void split3(float x, unsigned& a, unsigned& b, unsigned& c) {
  a = __builtin_bit_cast(unsigned, x) & 0xffff0000u;
  const float r1 = x - asf(a);
  b = __builtin_bit_cast(unsigned, r1) & 0xffff0000u;
  c = __builtin_bit_cast(unsigned, r1 - asf(b)) & 0xffff0000u;
}
constexpr int areg(int u, int mm) { return (u == 0 ? 240 : u < 11 ? 16 * (u - 1) : 16 * (u - 11)) + 4 * mm; }
typedef unsigned v16u __attribute__((ext_vector_type(16)));
DI const void* ptr2(unsigned lo, unsigned hi) { return (const void*)(((unsigned long long)hi << 32) | lo); }

__global__ void __launch_bounds__(256, 1) __attribute__((amdgpu_num_vgpr(VRES)))
fused_block(const void* __restrict__ xp_, const void* __restrict__ wqkvp_, const void* __restrict__ wop_, const void* __restrict__ wgp_,
            const void* __restrict__ wup_, const void* __restrict__ wdp_, void* __restrict__ outp_, char* __restrict__ wsp_) {
  extern __shared__ __attribute__((aligned(16))) char smem_[];
  LAS char* L = (LAS char*)smem_;
  unsigned long long pc0; unsigned codelen; v16u ka;
  asm volatile("s_load_dwordx16 %2, %3, 0\nfbk_begin:\n\ts_getpc_b64 %0\n\ts_mov_b32 %1, fbk_end-fbk_begin"
               : "=&s"(pc0), "=&s"(codelen), "=&s"(ka) : "s"(__builtin_amdgcn_kernarg_segment_ptr()) : ACLOB, "v255");
  FENCE();
  const int tid = threadIdx.x, lane = tid & 63;
  const int w = __builtin_amdgcn_readfirstlane(tid >> 6);
  const int b = blockIdx.x, xcd = b & 7, bi = b >> 3;
  const int r = (xcd >> 1) * 4 + (bi & 3), c = (xcd & 1) * 8 + (bi >> 2);
  const int j = (c >> 3) * 4 + (c & 3), hf = (c >> 2) & 1;
  const int li = lane & 15, lg = lane >> 4;
  const unsigned lane16 = lane * 16, lane16b = lane16 + 1024;
  const unsigned ldsw = w * 5120;
  int budget = 200000;
  const int drow = lane >> 3, dch = lane & 7, tq = li >> 2, tp = li & 3;
  const int pp = 2 * r + hf;
  const unsigned xst = L_XST + w * 20480;
  unsigned vo1, vo1b, vo2, vct;
  {
    const int z = 4 * w + (dch & 3);
    int col = 640 + 8 * z;
    col = z >= 5 ? 1232 + 8 * z : col; col = z == 5 ? 1312 : col; col = z >= 10 ? 8 * z - 80 : col; col = z == 15 ? 32 : col;
    vo1 = ((40 * r + 8 * (dch >> 2) + drow) * 1920 + 40 * c + col) * 2;
    vo1b = (dch >> 2) ? OOB : vo1;
    pin(vo1); pin(vo1b);
  }
  set_m0<2 * SLOT>(ldsw);
  asm volatile("s_waitcnt lgkmcnt(0)" : "+s"(ka));
  char* const wsp = (char*)ptr2(ka[14], ka[15]);
  const rsrc_t ws = mk(wsp, WS_END);
  const rsrc_t rx = mk(ptr2(ka[0], ka[1]), 2048 * 640 * 2);
  unsigned epoch, cpf;
  {
    const rsrc_t rw = mk(ptr2(ka[2], ka[3]), 640 * 1920 * 2);
    dma<0, false, -1>(vo1, rw, 0, 0);
    dma<1024, false, -1>(vo1, rw, 16 * 3840 - 1024, 0);
    dma<2048, false, -1>(vo1b, rw, 32 * 3840 - 2048, 0);
    const int oc = 32 * (w < 3 ? w : dch >> 1) + pp, n = oc >> 2, g = oc & 3;
    const int ra = 40 * n + 8 * g, rb = 160 * (n - 16) + 40 * g + 32;
    vo2 = ((n < 16 ? ra : rb) + drow) * 1280 + 160 * j + (w < 3 ? dch * 16 : 128 + (dch & 1) * 16);
    vo2 = oc < 80 ? vo2 : OOB;
    vct = tid < 16 ? (unsigned)(bi * 16 + tid) * 128 : OOB;
    const rsrc_t rw2 = mk((const char*)ptr2(ka[4], ka[5]) - 3072, 640 * 640 * 2 + 3072);
    dma<3072, false, 0>(vo2, rw2, 0, xst);
    unsigned z = 0; ld32c<0>(epoch, z, ws, 0);
    const rsrc_t rc = mk((const void*)pc0, codelen);
    asm volatile("buffer_load_dword %0, %1, %2, 0 offen" : "=v"(cpf) : "v"(vct), "s"(rc) : "memory");
  }
  unsigned xv[4];
  SF(4, pc) xv[pc] = drow * 1280 + ((dch ^ (4 * (pc & 1) + lg)) * 16) + pc * (10240 - 1024); SE
  const unsigned xrow = (128 * r + 32 * w) * 1280;
  auto x_piece = [&](auto ch_, auto pc_) __attribute__((always_inline)) {
    constexpr int ch = decltype(ch_)::value, pc = decltype(pc_)::value;
    dma<pc * 1024, false, (pc == 3 ? ((ch + 1) % 5) * 4096 : -1)>(xv[pc], rx, xrow + ch * 128, xst);
  };
  auto x_chunk = [&](auto ch_) __attribute__((always_inline)) { SF(4, pc) x_piece(ch_, IC<pc>{}); SE NOP1(); };
  x_chunk(IC<0>{});
  const unsigned trY = tq * 128 + lg * 32 + tp * 8, trX = tq * 128 + (lg >> 1) * 64 + tp * 16 + (lg & 1) * 8;
  unsigned dst1[3], d2, ta, tb, dc; v4u sel;
  {
    const unsigned s1 = (li == (lg & 1)) ? 0x3f803f80u : 0u;
    sel = v4u{s1, s1, s1, s1};
    const int cg = 2 * w + (lg & 1);
    SF(3, k)
      const int ol = 2 * k + (lg >> 1), oc = 5 * r + ol, ks = oc >> 2;
      const int fr = cg < 5 ? ks * 5 + cg : 95 + ks * 3 + cg;
      dst1[k] = ol < 5 ? (unsigned)(fr * 1024 + (oc & 3) * 256 + li * 16) : OOB;
      pin(dst1[k]);
    SE
    const int oc = 32 * (w < 3 ? w : lg) + pp;
    d2 = oc < 80 ? (unsigned)((oc >> 2) * 5120 + (oc & 3) * 256 + (w < 3 ? lg : 4) * 1024 + li * 16) : OOB;
    ta = 2 * SLOT + ldsw + trY; tb = 2 * SLOT + ldsw + (w < 3 ? trX : trY); dc = lane < 16 ? (unsigned)(16 * w + li) * 8 : OOB;
    pin(sel); pin(d2); pin(ta); pin(tb); pin(dc);
  }
  x_chunk(IC<1>{});
  unsigned fl_r = tid == 0 ? (unsigned)(4 * r) : OOB, fl_c = tid == 0 ? (unsigned)(4 * c) : OOB, fl_p = tid == 0 ? (unsigned)(4 * pp) : OOB;
  pin(fl_r); pin(fl_c); pin(fl_p);
  unsigned tag, vtag;
  {
    asm volatile("s_waitcnt vmcnt(8)" : "+v"(epoch), "+v"(cpf)::"memory");
    SF(3, k) tr_a<160 + 4 * k, k * 1024>(ta); SE
    tr_a<172, 3072>(tb);
    tag = __builtin_amdgcn_readfirstlane(epoch) * 1664525u + 1013904223u;
    vtag = tag; pin(vtag);
    f32x4 cs;
    WAITL(2);
    SF(3, k) mf16_va<k == 0, 160 + 4 * k>(cs, sel); SE
    SF(3, k) st_a<160 + 4 * k>(dst1[k], ws, WS_W1T + c * 163840); SE
    WAITL(0);
    st_a<172>(d2, ws, WS_W2T + j * 102400);
    asm volatile("s_nop 3" : "+v"(cs));
    const float c0 = cs[0], c1 = cs[1];
    st64c(v2u{__builtin_bit_cast(unsigned, c0), __builtin_bit_cast(unsigned, c1)}, dc, ws, WS_CS1 + (c * 16 + r) * 512);
  }
  x_chunk(IC<2>{}); x_chunk(IC<3>{}); x_chunk(IC<4>{});
  unsigned xa0 = xst + li * 128 + ((lg ^ (li >> 1)) * 16), xa1 = xst + li * 128 + (((4 + lg) ^ (li >> 1)) * 16);
  auto x_read = [&](auto ch_) __attribute__((always_inline)) {
    constexpr int ch = decltype(ch_)::value;
    dsr128a<(4 * ch + 0) * 4, (ch % 5) * 4096>(xa0);
    dsr128a<(4 * ch + 1) * 4, (ch % 5) * 4096 + 2048>(xa0);
    dsr128a<(4 * ch + 2) * 4, (ch % 5) * 4096>(xa1);
    dsr128a<(4 * ch + 3) * 4, (ch % 5) * 4096 + 2048>(xa1);
  };
  v4u ones = {0x3f803f80u, 0x3f803f80u, 0x3f803f80u, 0x3f803f80u};
  unsigned pv2 = flag_off(0, j) + (lane & 31) * 4;
  pin(ones); pin(pv2);
  f32x4 gr[2], sm[2];
  auto x_stat1 = [&](auto ch_, auto q_) __attribute__((always_inline)) {
    constexpr int ch = decltype(ch_)::value, q = decltype(q_)::value;
    mf16_aa<(ch == 0 && q < 2), (4 * ch + q) * 4, (4 * ch + q) * 4>(gr[q & 1]);
    mf16_va<(ch == 0 && q < 2), (4 * ch + q) * 4>(sm[q & 1], ones);
  };
  const unsigned w1src = WS_W1T + c * 163840 + ldsw, w3src = WS_W3T + c * 286720 + ldsw;
  unsigned pf;
  auto x_step = [&](auto k_) __attribute__((always_inline)) {
    constexpr int k = decltype(k_)::value;
    WAITL(4);
    SF(4, q) x_stat1(IC<k>{}, IC<q>{}); if constexpr (k < 5) x_piece(IC<5 + k>{}, IC<q>{}); SE
  };
  WAITV(17); x_read(IC<0>{}); x_read(IC<1>{});
  WAITV(12);
  BARRIER();
  publish(vtag, fl_p, ws, flag_off(F_W1, j));
  x_step(IC<0>{});
  WAITV(13); x_read(IC<2>{}); x_step(IC<1>{});
  WAITV(13); x_read(IC<3>{}); x_step(IC<2>{});
  WAITV(13); x_read(IC<4>{}); x_step(IC<3>{});
  ld32c<0>(pf, pv2, ws, F_W1 * 4096);
  WAITV(13); x_read(IC<5>{}); x_step(IC<4>{});
  {
    const v4u z4 = {0u, 0u, 0u, 0u};
    *(LAS v4u*)(L + L_KVT + tid * 16) = z4;
    if (tid < 16) *(LAS v4u*)(L + L_KVT + 4096 + tid * 16) = z4;
  }
  WAITV(13); x_read(IC<6>{}); x_step(IC<5>{});
  WAITV(9); x_read(IC<7>{});
  set_m0<0>(ldsw);
  poll_chk<4>(pf, ws, pv2, F_W1 * 4096, tag, budget);
  x_read(IC<8>{});
  SF(5, f) unit_dma<0, f, slot_of(1)>(ws, lane16, lane16b, w1src, ldsw); if constexpr (f >= 3) NOP1(); SE
  WAITL(4);
  SF(2, k) SF(4, q)
    x_stat1(IC<6 + k>{}, IC<q>{});
    constexpr int i = 4 * k + q;
    if constexpr (i < 5) unit_dma<1, i, slot_of(2)>(ws, lane16, lane16b, w1src + SLOT, ldsw);
    else unit_dma<2, i - 5, -1>(ws, lane16, lane16b, w1src + 2 * SLOT, ldsw);
  SE SE
  WAITV(13); x_read(IC<9>{});
  unit_dma<2, 3, -1>(ws, lane16, lane16b, w1src + 2 * SLOT, ldsw); NOP1();
  unit_dma<2, 4, slot_of(3)>(ws, lane16, lane16b, w1src + 2 * SLOT, ldsw);
  x_step(IC<8>{});
  WAITL(0);
  SF(4, q) x_stat1(IC<9>{}, IC<q>{}); SE
  const unsigned lb0 = lane16, lb1 = lane16 + 3 * SLOT, lb2 = lane16 + 6 * SLOT;
  auto rd = [&](auto fr_, v4u& d) __attribute__((always_inline)) {
    constexpr int fr = decltype(fr_)::value, sl = (fr / 20) % 7, off = (sl % 3) * SLOT + (fr % 20) * 1024;
    dsr128<off>(d, sl < 3 ? lb0 : sl < 6 ? lb1 : lb2);
  };
  unsigned v3, s4[2], d4[2], v4y, d3, d3s, d4y;
  f32x4 akv[5][2], aq[3][2], enb[2], era[2];
  float ecs[5];
  const unsigned v4x = drow * 1280 + dch * 16, d4x = lg * 1024 + li * 16;
  const rsrc_t rw3 = mk((w & 1) ? ptr2(ka[8], ka[9]) : ptr2(ka[6], ka[7]), 640 * 1728 * 2);
  const rsrc_t rw4 = mk((const char*)ptr2(ka[10], ka[11]) - 2048, 1728 * 640 * 2 + 2048);
  {
    v4u bf[8];
    unsigned cs1p[8];
    unsigned vc1 = (lane >> 5) * 4096 + (32 * w + (lane & 31)) * 4;
    unsigned ea = L_MEAN + (32 * w + 4 * lg) * 4, ec = L_CS1 + li * 4;
    pin(vc1); pin(ea); pin(ec);
    WAITV(10);
    BARRIER();
    SF(6, f) rd(IC<f>{}, bf[f]); SE
    SF(100, f)
      constexpr int ks = f / 5, t = f % 5;
      if constexpr (f % 20 == 14 && f < 80) { WAITV(f == 14 ? 14 : f == 34 ? 19 : f == 54 ? 16 : 15); BARRIER(); }
      if constexpr (!(f & 1)) WL(f + 6 <= 100 ? 4 : f == 96 ? 6 : 9);
      mf16_av<ks == 0, (2 * ks) * 4>(akv[t][0], bf[f & 7]);
      if constexpr (f + 6 < 100) rd(IC<f + 6>{}, bf[(f + 6) & 7]);
      mf16_av<ks == 0, (2 * ks + 1) * 4>(akv[t][1], bf[f & 7]);
      if constexpr (f % 3 == 1 && f < 75) { constexpr int i = f / 3; unit_dma<3 + i / 5, i % 5, slot_of(4 + i / 5)>(ws, lane16, lane16b, w1src + (3 + i / 5) * SLOT, ldsw); }
      if constexpr (f >= 76 && f % 3 == 1) {
        constexpr int k = (f - 76) / 3;
        if constexpr (k < 5) dma<(k < 4 ? k : 3) * 1024, false, (k == 3 ? (int)SLOT + 1024 : k == 4 ? 2 * (int)SLOT : -1)>(v3, rw3, k * 8 * 3456 - (k < 4 ? k : 3) * 1024, ldsw);
        else if constexpr (k == 5) dma<0, false, -1>(v4x, rw4, s4[0] + 2048, 0);
        else if constexpr (k == 6) dma<1024, false, -1>(v4x, rw4, s4[1] + 1024, 0);
        else dma<2048, false, slot_of(8)>(v4y, rw4, 0, ldsw);
      }
      if constexpr (f % 3 == 2 && f < 24) ld32c<(f / 3) * 512>(cs1p[f / 3], vc1, ws, WS_CS1 + c * 8192);
      if constexpr (f == 27) {
    float sta[2], stb[2];
    SF(2, m)
      pin(gr[m]); pin(sm[m]);
      const float sq = sel4(gr[m], tp);
      const float mean = sm[m][0] * (1.f / 640.f);
      const float rs = __builtin_amdgcn_rsqf(fmaxf(sq * (1.f / 640.f) - mean * mean, 0.f) + 1e-5f);
      stb[m] = -mean * rs; sta[m] = rs;
    SE
    if (lg == tq) {
      SF(2, m) *(LAS float*)(L + L_MEAN + (32 * w + 16 * m + li) * 4) = stb[m]; *(LAS float*)(L + L_RSTD + (32 * w + 16 * m + li) * 4) = sta[m]; SE
    }
      }
      if constexpr (f == 39)
  {
    const int ps = 4 * (w >> 1) + (dch >> 1);
    const unsigned fa = (6 * c + ps) * 32, fb = c < 12 ? (96 + c) * 32 : OOB;
    v3 = (ps < 6 ? fa : ps == 6 ? fb : OOB) + (40 * r + drow) * 3456 + 16 * (dch & 1);
    const int pd = 4 * (w >> 1) + lg;
    d3s = pd < 3 ? 2048u : 0u;
    d3 = (pd < 7 ? (unsigned)(pd * (pd < 3 ? 1024 : 40960)) : OOB) + (w & 1) * 256 + li * 16;
    pin(v3); pin(d3); pin(d3s);
  }
      if constexpr (f == 63)
  {
    SF(2, k)
      const int oc = 32 * (2 * w + k) + pp, nn = oc >> 2, g = oc & 3;
      const int ft = (w < 3 ? 6 * (nn & 15) + 2 * w : 2 * nn) + (g >> 1);
      const unsigned sv = (16 * ft + 8 * (g & 1)) * 1280 + 160 * j, dv = WS_W4T + j * 286720 + nn * 5120 + g * 256;
      s4[k] = oc < 216 ? sv : OOB; d4[k] = oc < 216 ? dv : OOB;
    SE
    v4y = (dch < 2 ? s4[0] : dch < 4 ? s4[1] : OOB) + drow * 1280 + 128 + (dch & 1) * 16;
    d4y = (lg == 0 ? d4[0] : lg == 1 ? d4[1] : OOB) + 4096 + li * 16;
    pin(v4y); pin(d4y);
  }
      if constexpr (f == 94) { dsr128f<0>(enb[0], ea); dsr128f<512>(era[0], ea); }
      if constexpr (f == 95) { dsr128f<64>(enb[1], ea); dsr128f<576>(era[1], ea); }
      if constexpr (f == 96) { dsr32f<0>(ecs[0], ec); dsr32f<64>(ecs[1], ec); dsr32f<128>(ecs[2], ec); }
      if constexpr (f == 97) { dsr32f<192>(ecs[3], ec); dsr32f<256>(ecs[4], ec); }
      if constexpr (f == 56) {
        WAITV(11);
        SF(8, k) pin(cs1p[k]); SE
        const float s = xsum32(((asf(cs1p[0]) + asf(cs1p[1])) + (asf(cs1p[2]) + asf(cs1p[3]))) + ((asf(cs1p[4]) + asf(cs1p[5])) + (asf(cs1p[6]) + asf(cs1p[7]))));
        *(LAS float*)(L + L_CS1 + (32 * w + (lane & 1) * 16 + ((lane & 31) >> 1)) * 4) = s;
      }
    SE
  }
  const unsigned w2src = WS_W2T + j * 102400 + w * 25600;
  auto ldw2 = [&](auto i_) __attribute__((always_inline)) {
    constexpr int i = decltype(i_)::value;
    ld_a<(i < 24 ? 160 + 4 * i : 0), (i & 3) * 1024>(lane16, ws, w2src + (i >> 2) * 4096);
  };
  {
    v4u fr[5];
    unsigned ib = L_RED + w * 3360 + (5 * li + lg) * 16, iz = L_RED + w * 3360 + (lg ? 204 + lg : 104) * 16;
    pin(ib); pin(iz);
    asm volatile("s_waitcnt lgkmcnt(0)\n\ts_nop 5" : "+v"(enb[0]), "+v"(enb[1]), "+v"(era[0]), "+v"(era[1]), "+v"(ecs[0]), "+v"(ecs[1]), "+v"(ecs[2]), "+v"(ecs[3]), "+v"(ecs[4])::"memory");
    FENCE();
    SF(5, t)
      float v[2][4];
      const float eps = t < 2 ? 1e-6f : (t == 2 && li < 8) ? 1e-6f : 0.f;
      SF(2, m)
        if constexpr (t < 3) ldw2(IC<2 * t + m>{});
        pin(akv[t][m]);
        SF(4, e)
          float x = fmaf(akv[t][m][e], era[m][e], fmaf(enb[m][e], ecs[t], eps));
          if constexpr (t < 2) x = phi1(x);
          if constexpr (t == 2) { const float y = phi1(x); x = li < 8 ? y : x; }
          v[m][e] = x;
        SE
        fr[t][2 * m] = cvtpk(v[m][0], v[m][1]); fr[t][2 * m + 1] = cvtpk(v[m][2], v[m][3]);
      SE
      if constexpr (t < 3) pin(fr[t]);
    SE
    f32x4 dd[10];
    asm volatile("s_nop 1\n\t"
                 "v_mfma_f32_16x16x32_bf16 %7, %10, %15, 0\n\tv_mfma_f32_16x16x32_bf16 %8, %11, %15, 0\n\tv_mfma_f32_16x16x32_bf16 %9, %12, %15, 0\n\t"
                 "v_mfma_f32_16x16x32_bf16 %4, %11, %12, 0\n\tv_mfma_f32_16x16x32_bf16 %6, %12, %12, 0\n\t"
                 "v_mfma_f32_16x16x32_bf16 %0, %10, %13, 0\n\tv_mfma_f32_16x16x32_bf16 %2, %11, %13, 0\n\t"
                 "v_mfma_f32_16x16x32_bf16 %1, %10, %14, 0\n\tv_mfma_f32_16x16x32_bf16 %3, %11, %14, 0\n\tv_mfma_f32_16x16x32_bf16 %5, %12, %14, 0"
                 : "=&v"(dd[0]), "=&v"(dd[1]), "=&v"(dd[2]), "=&v"(dd[3]), "=&v"(dd[4]), "=&v"(dd[5]), "=&v"(dd[6]), "=&v"(dd[7]), "=&v"(dd[8]), "=&v"(dd[9])
                 : "v"(fr[0]), "v"(fr[1]), "v"(fr[2]), "v"(fr[3]), "v"(fr[4]), "v"(ones));
    FENCE();
    dsw_kv<0, 3, 100 * 16>(dd[7], ib); dsw_kv<1, 3, 0>(dd[8], iz); dsw_kv<2, 3, 208 * 16>(dd[9], ib);
    dsw_kv<1, 2, 124 * 16>(dd[4], ib); dsw_kv<2, 2, 128 * 16>(dd[6], ib);
    dsw_kv<0, 0, 0>(dd[0], ib); dsw_kv<1, 0, 4 * 16>(dd[2], ib);
    dsw_kv<0, 1, 80 * 16>(dd[1], ib); dsw_kv<1, 1, 84 * 16>(dd[3], ib); dsw_kv<2, 1, 88 * 16>(dd[5], ib);
    asm volatile("s_mov_b64 exec, -1" ::: "memory");
    FENCE();
    ldw2(IC<6>{}); ldw2(IC<7>{});
    unsigned ra = L_RED + tid * 16, gv0 = tid < 210 ? (unsigned)tid * 16 : OOB;
    pin(ra); pin(gv0);
    WAITV(16);
    BARRIER_L();
    f32x4 p[4];
    SF(4, k) dsr128f<k * 3360>(p[k], ra); SE
    asm volatile("s_waitcnt lgkmcnt(0)" : "+v"(p[0]), "+v"(p[1]), "+v"(p[2]), "+v"(p[3])::"memory");
    st128c(__builtin_bit_cast(v4u, (p[0] + p[1]) + (p[2] + p[3])), gv0, ws, WS_KVP + (c * 16 + r) * 4096);
  }
  v4u pq[2][2];
  v4u xr[2]; v2u xr4;
  unsigned pvk = flag_off(F_KV, c) + li * 4;
  {
    v4u bf[8];
    SF(6, f) rd(IC<100 + f>{}, bf[f]); SE
    unsigned xo = ((128 * r + 64 * hf + 16 * w + li) * 640 + 80 * j) * 2;
    unsigned xo16 = xo + lg * 16, xo8 = xo + lg * 8;
    pin(xo16); pin(xo8); pin(pvk);
    f32x4 cs3;
    unsigned t3 = SLOT + ldsw + tq * 128 + lg * 32 + (2 * (tp & 1) + (tp >> 1)) * 8, t4x = 2 * SLOT + ldsw + trX, t4y = 2 * SLOT + ldsw + trY;
    unsigned dc3 = lane < 16 ? (unsigned)(((w & 1) * 16 + li) * 32 + (w >> 1) * 16) : OOB;
    const unsigned s13 = li == lg ? 0x3f803f80u : 0u;
    v4u sel3 = {s13, s13, s13, s13};
    pin(t3); pin(t4x); pin(t4y); pin(dc3); pin(sel3);
    WAITV(9);
    SF(60, f)
      constexpr int ks = f / 3, t = f % 3;
      if constexpr (f == 16) { WAITV(5); BARRIER(); publish(vtag, fl_r, ws, flag_off(F_KV, c)); }
      if constexpr (!(f & 1)) WL(f + 6 <= 60 ? 4 : 58 - f);
      mf16_va<ks == 0, (2 * ks) * 4>(aq[t][0], bf[f & 7]);
      if constexpr (f < 16 && !(f & 1)) {
        constexpr int k = f / 2;
        if constexpr (k < 5) tr_a<224 + 4 * k, k * 1024>(t3);
        else tr_a<224 + 4 * k, (k - 5) * 1024>(k < 7 ? t4x : t4y);
      }
      if constexpr (f + 6 < 60) rd(IC<100 + f + 6>{}, bf[(f + 6) & 7]);
      mf16_va<ks == 0, (2 * ks + 1) * 4>(aq[t][1], bf[f & 7]);
      if constexpr (f >= 7 && f < 23 && (f & 1)) {
        constexpr int k = (f - 7) / 2;
        if constexpr (k < 5) { st_a<224 + 4 * k>(d3 + d3s * ((5 * r + k) >> 1), ws, WS_W3T + c * 286720 + (5 * r + k) * 512); mf16_va<k == 0, 224 + 4 * k>(cs3, sel3); }
        else if constexpr (k < 7) st_a<224 + 4 * k>(d4x, ws, d4[(k - 5) & 1]);
        else st_a<224 + 4 * k>(d4y, ws, 0);
      }
      if constexpr (f == 24) {
        pin(cs3);
        st128c(__builtin_bit_cast(v4u, cs3), dc3, ws, WS_CS3 + (c * 16 + r) * 1024);
      }
      if constexpr (f >= 26 && f % 4 == 2 && f < 58) ldw2(IC<8 + (f - 26) / 4>{});
      if constexpr (f == 32) ld128c<0>(xr[0], xo16, rx, 0);
      if constexpr (f == 41) ld128c<64>(xr[1], xo16, rx, 0);
      if constexpr (f == 50) ld64c<128>(xr4, xo8, rx, 0);
      if constexpr (f == 58) ld32c<0>(pf, pvk, ws, 0);
    SE
  }
  const unsigned w4src = WS_W4T + j * 286720 + ldsw;
  auto pfx = [&](auto i_) __attribute__((always_inline)) {
    constexpr int i = decltype(i_)::value;
    if constexpr (i < 30) { unit_dma<8 + i / 5, i % 5, slot_of(9 + i / 5)>(ws, lane16, lane16b, w3src + (i / 5) * SLOT, ldsw); if constexpr (i % 5 >= 3) NOP1(); }
    else { constexpr int k = i - 30; ld_v<VRES + 4 * k, (k % 5 < 4 ? k % 5 : 3) * 1024>(k % 5 < 4 ? lane16 : lane16b, ws, w4src + (k / 5) * SLOT); }
  };
  f32x4 acc2[4][5];
  {
    float nb[2], ra[2]; f32x4 cs[3];
    SF(2, m) nb[m] = *(LAS float*)(L + L_MEAN + (32 * w + 16 * m + li) * 4); ra[m] = *(LAS float*)(L + L_RSTD + (32 * w + 16 * m + li) * 4); SE
    SF(3, t) cs[t] = *(LAS f32x4*)(L + L_CS1 + (80 + 16 * t + 4 * lg) * 4); SE
    asm volatile("s_nop 7" ::: "memory");
    FENCE();
    unsigned pk[2][3][2];
    auto qepi = [&](auto m_, auto t_) __attribute__((always_inline)) {
      constexpr int m = decltype(m_)::value, t = decltype(t_)::value;
      pin(aq[t][m]);
      float v[4];
      SF(4, e) v[e] = phi1(fmaf(aq[t][m][e], ra[m], fmaf(nb[m], cs[t][e], 1e-6f))); SE
      pk[m][t][0] = cvtpk(v[0], v[1]); pk[m][t][1] = cvtpk(v[2], v[3]);
      pin(pk[m][t][0]); pin(pk[m][t][1]);
    };
    SF(3, t) qepi(IC<0>{}, IC<t>{}); SE
    unsigned gv = tid < 210 ? (unsigned)tid * 16 : OOB;
    unsigned kw, k0 = L_KVT + (8 * tq + tp) * 96 + lg * 8, k2 = L_KVT + (li >= 8 ? 24 + li : 42) * 96 + lg * 8, k3 = L_KVT + (tp < 2 ? 40 + tp : 42) * 96 + lg * 8;
    {
      const int hd = tid >= 105, u = tid - 105 * hd, e = (u * 205) >> 10, dq = u - 5 * e + 5 * hd;
      kw = L_KVT + (tid >= 210 ? 43 * 96 : (e == 20 ? 40 + hd : e + 20 * hd) * 96 + dq * 8);
    }
    pin(gv); pin(kw); pin(k0); pin(k2); pin(k3);
    poll_chk<0>(pf, ws, pvk, 0, tag, budget);
    pin(xr[0]); pin(xr[1]); pin(xr4);
    v4u g[16];
    SF(16, k)
      ld128c<0>(g[k], gv, ws, WS_KVP + (c * 16 + k) * 4096);
      if constexpr (k == 3 || k == 7 || k == 11) qepi(IC<1>{}, IC<k / 4>{});
    SE
    SF(2, m)
      pq[m][0] = v4u{pk[m][0][0], pk[m][0][1], pk[m][1][0], pk[m][1][1]};
      pq[m][1] = v4u{lg < 2 ? pk[m][2][0] : 0u, lg < 2 ? pk[m][2][1] : 0u, 0u, 0u};
      pin(pq[m][0]); pin(pq[m][1]);
    SE
    asm volatile("v_accvgpr_write_b32 a106, 0\n\tv_accvgpr_write_b32 a107, 0\n\tv_accvgpr_write_b32 a114, 0\n\tv_accvgpr_write_b32 a115, 0\n\t"
                 "v_accvgpr_write_b32 a122, 0\n\tv_accvgpr_write_b32 a123, 0\n\tv_accvgpr_write_b32 a130, 0\n\tv_accvgpr_write_b32 a131, 0");
    f32x4 s;
    SF(4, q)
      ldw2(IC<16 + q>{});
      WAITV(13 - 3 * q);
      SF(4, k) pin(g[4 * q + k]); SE
      const f32x4 t4 = (__builtin_bit_cast(f32x4, g[4 * q]) + __builtin_bit_cast(f32x4, g[4 * q + 1])) + (__builtin_bit_cast(f32x4, g[4 * q + 2]) + __builtin_bit_cast(f32x4, g[4 * q + 3]));
      if constexpr (q == 0) s = t4; else s += t4;
      pin(s);
    SE
    *(LAS v2u*)(L + kw) = v2u{cvtpk(s[0], s[1]), cvtpk(s[2], s[3])};
    BARRIER_L();
    publish(vtag, fl_p, ws, flag_off(F_W4, j));
    SF(4, t1) constexpr int t = (t1 + 3) & 3; SF(3, p) dsr64a<100 + 8 * t + 2 * p, (t == 1 ? 4 * 96 : 0) + p * 32>(t < 2 ? k0 : t == 2 ? k2 : k3); SE SE
    f32x4 od[2][4];
    SF(4, t1)
      constexpr int t = (t1 + 3) & 3;
      WL(9 - 3 * t1);
      SF(2, m) mf16_av<true, 100 + 8 * t>(od[m][t], pq[m][0]); SE
      SF(2, m) mf16_av<false, 104 + 8 * t>(od[m][t], pq[m][1]); SE
      ldw2(IC<20 + t1>{});
    SE
    asm volatile("s_nop 7");
    FENCE();
    SF(2, m)
      SF(4, t) pin(od[m][t]); SE
      const float r0 = __builtin_amdgcn_rcpf(od[m][3][0] + 1e-6f), r1 = __builtin_amdgcn_rcpf(od[m][3][1] + 1e-6f);
      const float ra = lg < 3 ? r0 : r1, rb = lg < 2 ? r0 : r1;
      const v4u o = {cvtpk(od[m][0][0] * ra, od[m][0][1] * ra), cvtpk(od[m][0][2] * ra, od[m][0][3] * ra),
                     cvtpk(od[m][1][0] * rb, od[m][1][1] * rb), cvtpk(od[m][1][2] * rb, od[m][1][3] * rb)};
      const v2u o2 = {cvtpk(od[m][2][0] * r1, od[m][2][1] * r1), cvtpk(od[m][2][2] * r1, od[m][2][3] * r1)};
      const unsigned ob = WS_O + (8 * r + 2 * w + m) * 20480;
      st128c(o, lane16, ws, ob + c * 1024);
      st64c(o2, lg >= 2 ? (unsigned)(li * 16 + (lg - 2) * 8) : OOB, ws, ob + (16 + (c >> 2)) * 1024 + (c & 3) * 256);
    SE
    ld32c<0>(pf, pv2, ws, F_W4 * 4096);
    ldw2(IC<24>{});
    SF(5, t)
      const unsigned x0 = t < 4 ? xr[(t >> 1) & 1][(t & 1) * 2] : xr4[0], x1 = t < 4 ? xr[(t >> 1) & 1][(t & 1) * 2 + 1] : xr4[1];
      acc2[3][t] = f32x4{bflo(x0), bfhi(x0), bflo(x1), bfhi(x1)};
      pin(acc2[3][t]);
    SE
    WAITV(2);
    BARRIER();
    publish(vtag, fl_c, ws, flag_off(F_O, r));
    poll_chk<2>(pf, ws, pv2, F_W4 * 4096, tag, budget);
    SF(14, k) pfx(IC<k>{}); SE
    poll_w0(ws, flag_off(F_O, r) + li * 4, tag, budget, w);
    st32c(vtag, (b == 0 && tid == 0) ? 0u : OOB, ws, 0);
    BARRIER();
  }
  const int mt0 = 8 * r + 4 * hf;
  const bool dead = w >= 2;
  auto ldw4a = [&](auto i_) __attribute__((always_inline)) {
    constexpr int i = decltype(i_)::value;
    ld_a<160 + 4 * i, (i % 5 < 4 ? i % 5 : 3) * 1024>(i % 5 < 4 ? lane16 : lane16b, ws, (i >= 15 && dead) ? OOB : w4src + (10 + i / 5) * SLOT);
  };
  v4u hp[2]; v2u hp4;
  const unsigned pb0 = lb0 + ldsw, pb1 = lb1 + ldsw, pb2 = lb2 + ldsw;
  {
    unsigned cs3p[16];
    auto ldo = [&](auto i_) __attribute__((always_inline)) {
      constexpr int i = decltype(i_)::value, mm = i / 5, kk = i % 5;
      ld_a<4 + i * 4, (kk < 4 ? kk : 3) * 1024>(kk < 4 ? lane16 : lane16b, ws, WS_O + (mt0 + ((w + mm + 1) & 3)) * 20480 + ldsw);
    };
    constexpr int UP = 10;
    SF(UP, i) ldo(IC<i>{}); SE
    f32x4 pr[3][5];
    unsigned rb[3];
    SF(3, mm) rb[mm] = slot_of(12 + mm) + ((w + 3 - mm) & 3) * 5120 + lane16; pin(rb[mm]); SE
    SF(20, i)
      constexpr int mm = i / 5, kk = i % 5;
      WAITV(i + UP <= 20 ? UP - 1 : 19 - i + (i - 10));
      if constexpr (i == 17) BARRIER_L();
      SF(5, t)
        constexpr int fb = kk * 5 + t;
        mf16_aa<(kk == 0 && mm < 3), (fb < 24 ? 160 + 4 * fb : 0), 4 + i * 4>(acc2[mm][t]);
        if constexpr (t == 0 && i + UP < 20) ldo(IC<i + UP>{});
        if constexpr (i >= 10 && t == 0) pfx(IC<(i < 16 ? 14 + i - 10 : 30 + i - 16)>{});
        if constexpr (mm > 0 && kk == 1) {
          constexpr int sl = (12 + mm - 1) % 7;
          dsw128f<(sl % 3) * SLOT + t * 1024>(acc2[mm - 1][t], sl < 3 ? pb0 : sl < 6 ? pb1 : pb2);
        }
        if constexpr (i == 17) { dsr128f<t * 1024>(pr[0][t], rb[0]); dsr128f<t * 1024>(pr[1][t], rb[1]); }
        if constexpr (i == 18) dsr128f<t * 1024>(pr[2][t], rb[2]);
        if constexpr (i == 19) { WL(4 - t); pin(pr[0][t]); pin(pr[1][t]); pin(pr[2][t]); pr[0][t] = (pr[0][t] + pr[1][t]) + pr[2][t]; pin(pr[0][t]); }
      SE
    SE
    asm volatile("s_nop 3");
    f32x4 v[5];
    SF(5, t) pin(acc2[3][t]); v[t] = pr[0][t] + acc2[3][t]; SE
    SF(2, k) hp[k] = v4u{cvtpk(v[2 * k][0], v[2 * k][1]), cvtpk(v[2 * k][2], v[2 * k][3]), cvtpk(v[2 * k + 1][0], v[2 * k + 1][1]), cvtpk(v[2 * k + 1][2], v[2 * k + 1][3])}; SE
    hp4 = v2u{cvtpk(v[4][0], v[4][1]), cvtpk(v[4][2], v[4][3])};
    const v4u h4 = {hp4.x, hp4.y, 0u, 0u};
    f32x4 g2, s2;
    mf16_vv<true>(g2, hp[0], hp[0]); mf16_vv<true>(s2, ones, hp[0]);
    mf16_vv<false>(g2, hp[1], hp[1]); mf16_vv<false>(s2, ones, hp[1]);
    mf16_vv<false>(g2, h4, h4); mf16_vv<false>(s2, ones, h4);
    const int mt = mt0 + w;
    const unsigned hb = WS_H + (mt >> 1) * 40960 + 5 * j * 1024 + (mt & 1) * 256;
    const unsigned hv = (lg >> 1) * 1024 + (lg & 1) * 512 + li * 16;
    st128c(hp[0], hv, ws, hb); st128c(hp[1], hv, ws, hb + 2048);
    st64c(hp4, (lg >> 1) * 512 + li * 16 + (lg & 1) * 8, ws, hb + 4096);
    asm volatile("s_nop 1" : "+v"(g2), "+v"(s2));
    const float sq = sel4(g2, tp), sv = s2[0];
    st64c(v2u{__builtin_bit_cast(unsigned, sv), __builtin_bit_cast(unsigned, sq)}, lg == tq ? (unsigned)li * 8 : OOB, ws, WS_HS + (j * 2048 + 16 * mt) * 8);
    SF(16, k) ld32c<(k & 3) * 1024>(cs3p[k], (unsigned)tid * 4, ws, WS_CS3 + c * 16384 + (k >> 2) * 4096); SE
    SF(4, k) pfx(IC<34 + k>{}); SE
    WAITV(20);
    BARRIER();
    publish(vtag, fl_c, ws, flag_off(F_H, r));
    SF(10, k) pfx(IC<20 + k>{}); SE
    SF(2, k) pfx(IC<38 + k>{}); SE
    {
      WAITV(17);
      SF(16, k) pin(cs3p[k]); SE
      float q4[4];
      SF(4, k) q4[k] = (asf(cs3p[4 * k]) + asf(cs3p[4 * k + 1])) + (asf(cs3p[4 * k + 2]) + asf(cs3p[4 * k + 3])); SE
      unsigned c1, c2, c3;
      split3((q4[0] + q4[1]) + (q4[2] + q4[3]), c1, c2, c3);
      *(LAS v4u*)(L + L_RED + ((tid & 7) * 32 + (tid >> 3)) * 16) = v4u{c1 | (c1 >> 16), c2 | (c2 >> 16), c3 | (c1 >> 16), c3 | (c2 >> 16)};
    }
    poll_w0(ws, flag_off(F_H, r) + li * 4, tag, budget, w);
    BARRIER();
  }
  unsigned va[4];
  auto lda = [&](auto u_, auto mm_) __attribute__((always_inline)) {
    constexpr int u = decltype(u_)::value, mm = decltype(mm_)::value;
    ld_a<areg(u, mm), 0>(va[mm], ws, (u == 13 && dead) ? OOB : WS_ACT + u * 4096);
  };
  unsigned pa = flag_off(F_A0, r) + li * 4;
  {
    v2u hs[4];
    const unsigned hsrc = WS_H + (4 * r + w) * 40960;
    SF(4, s) ld_a<4 * s, (s & 3) * 1024>(lane16, ws, hsrc); SE
    v4u bf[6];
    SF(4, k) rd(IC<160 + k>{}, bf[k]); SE
    SF(4, k) ld64c<0>(hs[k], (unsigned)(((lane >> 5) * 4 * 2048 + (lane & 31)) * 8), ws, WS_HS + (k * 2048 + 128 * r + 32 * w) * 8); SE
    SF(P3_HUP - 4, s) ld_a<4 * (s + 4), (s & 3) * 1024>(lane16, ws, hsrc + ((s + 4) >> 2) * 4096); SE
    f32x16 acc[4];
    v4u cf[3], mf;
    float c1, kk2, eP[2], eT[2], eO[8];
    unsigned ov[4];
    unsigned csa = L_RED + (lane & 31) * 16;
    unsigned av = ((lane >> 4) & 1) * 57344 + (lane >> 5) * 256 + li * 16;
    SF(4, mm) va[mm] = lane16 + (mt0 + ((w + mm + 1) & 3)) * 57344 + w * 1024; pin(va[mm]); SE
    pin(csa); pin(av); pin(pa);
    const unsigned ab = WS_ACT + (8 * r + 2 * w) * 57344;
    auto epi = [&](auto p_, auto q_) __attribute__((always_inline)) {
      constexpr int p = decltype(p_)::value, q = decltype(q_)::value;
      f32x16& a = acc[p % 4];
      if constexpr (q < 24) {
        constexpr int e = q / 3, ph = q % 3, x = e & 1, y = x ^ 1;
        const float ag = a[e], au = a[8 + e];
        if constexpr (ph == 0) {
          if constexpr (e == 0) asm volatile("v_mul_f32 %0, %1, %2" : "=v"(eT[x]) : "v"(ag), "v"(c1));
          else asm volatile("v_rcp_f32 %1, %1\n\tv_mul_f32 %0, %2, %3" : "=v"(eT[x]), "+v"(eT[y]) : "v"(ag), "v"(c1));
        } else if constexpr (ph == 1) {
          if constexpr (e == 0) asm volatile("v_exp_f32 %0, %0" : "+v"(eT[x]));
          else asm volatile("v_exp_f32 %0, %0\n\tv_mul_f32 %1, %2, %3" : "+v"(eT[x]), "=v"(eO[e - 1]) : "v"(eP[y]), "v"(eT[y]));
        } else {
          if constexpr (e >= 2 && !(e & 1)) asm volatile("v_mul_f32 %0, %3, %4\n\tv_fma_f32 %1, %1, %5, %5\n\tv_cvt_pk_bf16_f32 %2, %6, %7" : "=&v"(eP[x]), "+v"(eT[x]), "=v"(ov[(e - 2) >> 1]) : "v"(ag), "v"(au), "v"(kk2), "v"(eO[e - 2]), "v"(eO[e - 1]));
          else asm volatile("v_mul_f32 %0, %2, %3\n\tv_fma_f32 %1, %1, %4, %4" : "=&v"(eP[x]), "+v"(eT[x]) : "v"(ag), "v"(au), "v"(kk2));
        }
      } else if constexpr (q == 24) asm volatile("v_rcp_f32 %0, %0\n\ts_nop 0" : "+v"(eT[1]));
      else if constexpr (q == 25) asm volatile("v_mul_f32 %1, %1, %2\n\tv_cvt_pk_bf16_f32 %0, %3, %1" : "=v"(ov[3]), "+v"(eP[1]) : "v"(eT[1]), "v"(eO[6]));
      else if constexpr (q == P3_EPI_ST) {
        unsigned so;
        if constexpr (p == 6) so = c < 12 ? ab + (48 + (c >> 1)) * 1024 + (c & 1) * 512 : OOB;
        else so = ab + (16 * (p >> 1) + c) * 1024 + (p & 1) * 512;
        st128c(v4u{ov[0], ov[1], ov[2], ov[3]}, av, ws, so);
      }
    };
    SF(280, g)
      constexpr int p = g < 120 ? g % 3 : g / 40, s = g < 120 ? g / 3 : g % 40;
      if constexpr (g == P3_CHK_A0) poll_chk<p3_cnt(P3_POLL_A0, g)>(pf, ws, pa, 0, tag, budget);
      if constexpr (g % 20 == 16) {
        constexpr int nu = 8 + g / 20 + 1;
        if constexpr (g == P3_FL0) WAITV(p3_cnt(p3_st_step(1), g));
        else if constexpr (g == P3_FL1) WAITV(p3_cnt(p3_st_step(3), g));
        else if constexpr (g == P3_FL2) WAITV(p3_cnt(p3_st_step(5), g));
        else if constexpr (nu >= 14 && nu < 22) WAITV(p3_cnt(p3_dma_start(nu) + 12, g));
        BARRIER();
      }
      if constexpr (g < 120 && g % 3 == 0) {
        if constexpr (s < 4) WAITV(P3_HUP + 3 - s + p3_cnt(-1, g));
        else if constexpr (s < P3_HUP) WAITV(P3_HUP - 1 - s + p3_cnt(-1, g));
        else WAITV(p3_cnt(2 * (s - P3_HUP) + 1, g));
      }
      WL(g + 4 < 280 ? 3 : 279 - g);
      mf32_va<s == 0, 4 * s>(acc[p % 4], bf[g % 6]);
      if constexpr (s == 34) dsr128<p * 512>(cf[p % 3], csa);
      if constexpr (g + 4 < 280) rd(IC<160 + g + 4>{}, bf[(g + 4) % 6]);
      if constexpr (g == P3_FL0) publish(vtag, fl_c, ws, flag_off(F_A0, r));
      if constexpr (g == P3_FL1) publish(vtag, fl_c, ws, flag_off(F_A1, r));
      if constexpr (g == P3_FL2) publish(vtag, fl_c, ws, flag_off(F_A2, r));
      if constexpr (p3_hload(g)) { constexpr int hsx = (g >> 1) + P3_HUP; ld_a<4 * hsx, (hsx & 3) * 1024>(lane16, ws, hsrc + (hsx >> 2) * 4096); }
      if constexpr (p3_dma_at(g) >= 0) {
        constexpr int nu = p3_dma_at(g) / 8, f = p3_dma_at(g) % 8;
        unit_dma<nu, f, slot_of(nu + 1)>(ws, lane16, lane16b, nu < 22 ? w3src + (nu - 8) * SLOT : w4src + (nu - 18) * SLOT, ldsw);
      }
      if constexpr (p3_w4a_at(g) >= 10) ldw4a(IC<p3_w4a_at(g) - 10>{});
      else if constexpr (p3_w4a_at(g) >= 0) pfx(IC<40 + p3_w4a_at(g)>{});
      if constexpr (p3_aload(g)) lda(IC<(g - P3_AL0) / 8>{}, IC<((g - P3_AL0) / 2) % 4>{});
      if constexpr (g == P3_POLL_A0) ld32c<0>(pf, pa, ws, 0);
      if constexpr (g == P3_POLL_A1) ld32c<0>(pf, pa, ws, 4096);
      if constexpr (g == 13) {
        SF(4, k) pin(hs[k]); SE
        const float s1 = xsum32((asf(hs[0].x) + asf(hs[1].x)) + (asf(hs[2].x) + asf(hs[3].x)));
        const float s2 = xsum32((asf(hs[0].y) + asf(hs[1].y)) + (asf(hs[2].y) + asf(hs[3].y)));
        const float mean = s1 * (1.f / 640.f);
        kk2 = fmaxf(s2 * (1.f / 640.f) - mean * mean, 0.f) + 1e-5f;
        c1 = -1.44269504f * __builtin_amdgcn_rsqf(kk2);
        unsigned m1, m2, m3;
        split3(lane < 32 ? -mean : 0.f, m1, m2, m3);
        mf = v4u{m2 | (m1 >> 16), m2 | (m1 >> 16), m1 | (m3 >> 16), m2 | (m3 >> 16)};
        pin(kk2); pin(c1); pin(mf);
      }
      SF(5, ep) if constexpr (g >= p3_epi_start(ep) && g <= p3_epi_start(ep) + P3_EPI_ST) epi(IC<ep>{}, IC<g - p3_epi_start(ep)>{}); SE
      if constexpr (g >= 246 && g < 260) { epi(IC<5>{}, IC<2 * (g - 246)>{}); epi(IC<5>{}, IC<2 * (g - 246) + 1>{}); }
      if constexpr (s == 39) mf32_vv(acc[p % 4], cf[p % 3], mf);
    SE
    asm volatile("s_nop 11");
    {
      f32x16& a = acc[2];
      pin(a);
      float o[8];
      SF(8, e) o[e] = a[e] * a[8 + e] * __builtin_amdgcn_rcpf(fmaf(__builtin_amdgcn_exp2f(a[e] * c1), kk2, kk2)); SE
      SF(4, k) ov[k] = cvtpk(o[2 * k], o[2 * k + 1]); SE
      epi(IC<6>{}, IC<P3_EPI_ST>{});
    }
    poll_chk<1>(pf, ws, pa, 4096, tag, budget);
  }
  {
    f32x4 acc[4][5];
    v4u bf[2][5];
    auto rdw = [&](auto u_, auto t_) __attribute__((always_inline)) {
      constexpr int u = decltype(u_)::value, t = decltype(t_)::value, sl = (18 + u) % 7;
      dsr128<(sl % 3) * SLOT + t * 1024>(bf[u & 1][t], sl < 3 ? pb0 : sl < 6 ? pb1 : pb2);
    };
    SF(5, t)
      const unsigned x0 = t < 4 ? hp[(t >> 1) & 1][(t & 1) * 2] : hp4[0], x1 = t < 4 ? hp[(t >> 1) & 1][(t & 1) * 2 + 1] : hp4[1];
      acc[3][t] = f32x4{bflo(x0), bfhi(x0), bflo(x1), bfhi(x1)};
      pin(acc[3][t]);
    SE
    SF(11, u)
      if constexpr (u == 2) { WAITV(12); BARRIER(); publish(vtag, fl_c, ws, flag_off(F_A3, r)); }
      if constexpr (u == 4) poll_chk<6>(pf, ws, pa, 8192, tag, budget);
      if constexpr (u == 7) WAITV(13);
      if constexpr (u == 8) WAITV(4);
      if constexpr (u == 9) poll_chk<0>(pf, ws, pa, 12288, tag, budget);
      if constexpr (u >= 4 && u < 10) WL(0);
      SF(4, mm) SF(5, t)
        if constexpr (u == 0 && mm < 3) mf16_lva0<VRES + 4 * t, areg(u, mm)>(acc[mm][t]);
        else if constexpr (u < 4) mf16_lva<VRES + 20 * u + 4 * t, areg(u, mm)>(acc[mm][t]);
        else if constexpr (u < 10) mf16_va<false, areg(u, mm)>(acc[mm][t], bf[u & 1][t]);
        else mf16_aa<false, 160 + 20 * (u - 10) + 4 * t, areg(u, mm)>(acc[mm][t]);
        constexpr int i = mm * 5 + t;
        if constexpr (u >= 3 && u < 9 && i < 10 && (i & 1)) rdw(IC<u + 1>{}, IC<i / 2>{});
        if constexpr (i == 0 && u == 3) ld32c<0>(pf, pa, ws, 8192);
        if constexpr (i == 0 && u == 8) ld32c<0>(pf, pa, ws, 12288);
        if constexpr (u < 8 && i < 12 && i % 3 == 2) lda(IC<u + 4>{}, IC<i / 3>{});
        if constexpr (u == 9 && i < 16 && !(i & 1)) lda(IC<12 + i / 8>{}, IC<(i / 2) % 4>{});
        if constexpr (u < 7 && (i == 13 || i == 16 || (i == 19 && u == 5))) {
          constexpr int k = 2 * u + (u > 5) + (i - 13) / 3;
          unit_dma<25 + k / 5, k % 5, slot_of(26 + k / 5)>(ws, lane16, lane16b, w4src + (7 + k / 5) * SLOT, ldsw);
        }
      SE SE
    SE
    f32x4 pr[3][5];
    unsigned rb[3];
    SF(3, mm) rb[mm] = mm * SLOT + ((w + 3 - mm) & 3) * 5120 + lane16; pin(rb[mm]); SE
    SF(4, mm)
      SF(5, t)
        mf16_aa<false, 180 + 4 * t, areg(11, mm)>(acc[mm][t]);
        if constexpr (mm > 0 && t >= 3) dsw128f<(mm - 1) * SLOT + (t - 3) * 1024>(acc[mm - 1][t - 3], pb0);
      SE
      if constexpr (mm == 0) WAITV(4);
      SF(5, t)
        mf16_aa<false, 200 + 4 * t, areg(12, mm)>(acc[mm][t]);
        if constexpr (mm > 0 && t < 3) dsw128f<(mm - 1) * SLOT + (t + 2) * 1024>(acc[mm - 1][t + 2], pb0);
      SE
      if constexpr (mm == 0) WAITV(0);
      if constexpr (mm == 3) BARRIER_L();
      SF(5, t)
        mf16_aa<false, 220 + 4 * t, areg(13, mm)>(acc[mm][t]);
        if constexpr (mm == 3) { SF(3, k) dsr128f<t * 1024>(pr[k][t], rb[k]); SE }
      SE
    SE
    f32x4 v[5];
    SF(5, t)
      WL(12 - 3 * t);
      pin(pr[0][t]); pin(pr[1][t]); pin(pr[2][t]); pin(acc[3][t]);
      v[t] = ((pr[0][t] + pr[1][t]) + pr[2][t]) + acc[3][t];
    SE
    const rsrc_t ro = mk(ptr2(ka[12], ka[13]), 2048 * 640 * 2);
    const unsigned oo = ((128 * r + 64 * hf + 16 * w + li) * 640 + 80 * j) * 2;
    SF(2, k) st128p(v4u{cvtpk(v[2 * k][0], v[2 * k][1]), cvtpk(v[2 * k][2], v[2 * k][3]), cvtpk(v[2 * k + 1][0], v[2 * k + 1][1]), cvtpk(v[2 * k + 1][2], v[2 * k + 1][3])}, oo + lg * 16, ro, k * 64); SE
    st64p(v2u{cvtpk(v[4][0], v[4][1]), cvtpk(v[4][2], v[4][3])}, oo + lg * 8, ro, 128);
  }
  asm volatile("fbk_end:");
}

extern "C" void kernel_launch(void* const* d_in, const int* in_sizes, int n_in, void* d_out, int out_size, void* d_ws, size_t ws_size, hipStream_t stream) {
  (void)in_sizes; (void)n_in; (void)out_size; (void)ws_size;
  (void)hipFuncSetAttribute((const void*)fused_block, hipFuncAttributeMaxDynamicSharedMemorySize, (int)LDS_BYTES);
  fused_block<<<256, 256, LDS_BYTES, stream>>>(d_in[0], d_in[1], d_in[2], d_in[4], d_in[5], d_in[6], d_out, (char*)d_ws);
}
```
